```python
import torch

def _softmax(x, axis=-1):
    return torch.softmax(x, dim=axis)
def _gelu(x):
    return torch.nn.functional.gelu(x, approximate="tanh")
def _silu(x):
    return torch.nn.functional.silu(x)
def _sigmoid(x):
    return torch.sigmoid(x)
def _layernorm(x, axis=-1, eps=1e-5):
    m = x.mean(dim=axis, keepdim=True)
    v = x.var(dim=axis, keepdim=True, unbiased=False)
    return (x - m) / torch.sqrt(v + eps)
def _scatter_add(tgt, idx, src, axis=0):
    return tgt.clone().index_add_(axis, idx, src)
def _segment_sum(src, seg, n):
    out = torch.zeros((n,) + src.shape[1:], dtype=src.dtype)
    return out.index_add_(0, seg, src)
def _pad(x, pw):
    flat = tuple(v for pair in reversed(pw) for v in pair)
    return torch.nn.functional.pad(x, flat)

kernel_name = 'standalone__mha_block_cached__near_ridge'

def setup_inputs():

    _w = (lambda a: (lambda t: t.to(torch.bfloat16) if t.is_floating_point() else t)(torch.as_tensor(a)))
    B, Sq, D = 32, 160, 640
    H, Sk, Dh = 32, 512, 128
    return {
        "x": _w((lambda *s: torch.randn(*s, dtype=torch.bfloat16) * 0.8)(B, Sq, D)),
        "k_cache": _w((lambda *s: torch.randn(*s, dtype=torch.bfloat16) * 0.8)(B, H, Sk, Dh)),
        "v_cache": _w((lambda *s: torch.randn(*s, dtype=torch.bfloat16) * 0.05)(B, H, Sk, Dh)),
        "wq": _w((lambda *s: torch.randn(*s, dtype=torch.bfloat16) * 0.8)(D, H * Dh) * (D ** -0.5)),
        "wo": _w((lambda *s: torch.randn(*s, dtype=torch.bfloat16) * 0.05)(H * Dh, D) * ((H * Dh) ** -0.5)),
    }

def reference(x, k_cache, v_cache, wq, wo):
    B, Sq, D = x.shape
    H, Dh = 32, 128
    q = (lambda x, *d: x.permute(*d))((x @ wq).reshape(B, Sq, H, Dh), 0, 2, 1, 3)
    scores = torch.einsum("bhqd,bhkd->bhqk", q, k_cache) * (Dh ** -0.5)
    p = _softmax(scores, axis=-1)
    o = torch.einsum("bhqk,bhkd->bhqd", p, v_cache)
    return (lambda x, *d: x.permute(*d))(o, 0, 2, 1, 3).reshape(B, Sq, H * Dh) @ wo
```

```cpp
#include <hip/hip_runtime.h>
#include <hip/hip_bf16.h>
typedef unsigned short u16;
static __device__ __forceinline__ float b2f(u16 x){ return __uint_as_float(((unsigned)x)<<16); }
static __device__ __forceinline__ u16 f2b(float f){ unsigned u=__float_as_uint(f); u += 0x7fffu + ((u>>16)&1u); return (u16)(u>>16); }
__global__ __launch_bounds__(256) void gemm(const u16* A, const u16* B, u16* C, int M, int N, int K){
  __shared__ float As[16][65], Bs[16][65];
  int tx=threadIdx.x&15, ty=threadIdx.x>>4;
  int m0=blockIdx.y*64, n0=blockIdx.x*64;
  float acc[4][4]={};
  for(int k0=0;k0<K;k0+=16){
    for(int i=threadIdx.x;i<1024;i+=256){ int r=i>>4,c=i&15; As[c][r]=b2f(A[(long)(m0+r)*K+k0+c]); }
    for(int i=threadIdx.x;i<1024;i+=256){ int r=i>>6,c=i&63; Bs[r][c]=b2f(B[(long)(k0+r)*N+n0+c]); }
    __syncthreads();
    for(int k=0;k<16;k++){
      float a[4],b[4];
      for(int i=0;i<4;i++){a[i]=As[k][ty*4+i]; b[i]=Bs[k][tx*4+i];}
      for(int i=0;i<4;i++)for(int j=0;j<4;j++)acc[i][j]+=a[i]*b[j];
    }
    __syncthreads();
  }
  for(int i=0;i<4;i++)for(int j=0;j<4;j++)C[(long)(m0+ty*4+i)*N+n0+tx*4+j]=f2b(acc[i][j]);
}
__global__ __launch_bounds__(256) void attn(const u16* Q, const u16* K, const u16* V, u16* O){
  __shared__ float qs[16][128];
  __shared__ float s[16][512];
  int bh=blockIdx.y, b=bh>>5, h=bh&31, q0=blockIdx.x*16;
  for(int i=threadIdx.x;i<16*128;i+=256){int r=i>>7,c=i&127; qs[r][c]=b2f(Q[((long)(b*160+q0+r))*4096+h*128+c]);}
  __syncthreads();
  const u16* Kp=K+(long)bh*512*128; const u16* Vp=V+(long)bh*512*128;
  for(int k=threadIdx.x;k<512;k+=256){
    float acc[16]={};
    for(int d=0;d<128;d++){ float kv=b2f(Kp[k*128+d]); for(int r=0;r<16;r++)acc[r]+=qs[r][d]*kv; }
    for(int r=0;r<16;r++)s[r][k]=acc[r]*0.08838834764831845f;
  }
  __syncthreads();
  { int r=threadIdx.x>>4, l=threadIdx.x&15; float m=-1e30f;
    for(int k=l;k<512;k+=16)m=fmaxf(m,s[r][k]);
    for(int o=8;o;o>>=1)m=fmaxf(m,__shfl_xor(m,o));
    float sum=0; for(int k=l;k<512;k+=16){float e=__expf(s[r][k]-m); s[r][k]=e; sum+=e;}
    for(int o=8;o;o>>=1)sum+=__shfl_xor(sum,o);
    float inv=1.f/sum; for(int k=l;k<512;k+=16)s[r][k]*=inv; }
  __syncthreads();
  { int d=threadIdx.x&127, rh=threadIdx.x>>7; float acc[8]={};
    for(int k=0;k<512;k++){ float v=b2f(Vp[k*128+d]); for(int r=0;r<8;r++)acc[r]+=s[rh*8+r][k]*v; }
    for(int r=0;r<8;r++)O[((long)(b*160+q0+rh*8+r))*4096+h*128+d]=f2b(acc[r]); }
}
extern "C" void kernel_launch(void* const* d_in, const int* in_sizes, int n_in, void* d_out, int out_size, void* d_ws, size_t ws_size, hipStream_t stream){
  const u16* x=(const u16*)d_in[0]; const u16* k=(const u16*)d_in[1]; const u16* v=(const u16*)d_in[2];
  const u16* wq=(const u16*)d_in[3]; const u16* wo=(const u16*)d_in[4];
  u16* Q=(u16*)d_ws; u16* O=Q+(size_t)5120*4096;
  gemm<<<dim3(4096/64,5120/64),256,0,stream>>>(x,wq,Q,5120,4096,640);
  attn<<<dim3(10,1024),256,0,stream>>>(Q,k,v,O);
  gemm<<<dim3(640/64,5120/64),256,0,stream>>>(O,wo,(u16*)d_out,5120,640,4096);
}
```

```cpp
#include <hip/hip_runtime.h>
#include <hip/hip_bf16.h>
typedef unsigned long long u64;

__global__ __launch_bounds__(256, 1) void mha_main(const char* __restrict__ x, const char* __restrict__ k, const char* __restrict__ v,
                                                   const char* __restrict__ wq, const char* __restrict__ wo, char* __restrict__ ws, char* __restrict__ out) {
  extern __shared__ __attribute__((aligned(16))) char lds[];
  const unsigned i = blockIdx.x;
  const unsigned b = ((i & 7u) >> 1) * 8u + (i >> 5), grp = (i & 1u) * 4u + ((i >> 3) & 3u);
  const unsigned shr = i >> 5;
  const unsigned w = __builtin_amdgcn_readfirstlane(threadIdx.x >> 6);
  const u64 px = (u64)(x + (size_t)b * 204800u);
  const u64 pwq = (u64)(wq + grp * 1024u);
  const u64 pk = (u64)(k + (size_t)(b * 32u + 4u * grp) * 131072u);
  const u64 pv = (u64)(v + (size_t)(b * 32u + 4u * grp) * 131072u);
  const u64 pwo = (u64)(wo + (size_t)grp * 655360u);
  const u64 pout = (u64)(out + (size_t)b * 204800u);
  const unsigned flip = grp & 1u;
  const unsigned rot = (0x8530u >> (4u * (((b >> 1) + grp) & 3u))) & 15u;
  const unsigned info = w | (flip << 2) | (grp << 3) | (b << 6) | (shr << 11) | (rot << 14) | ((i >> 3) << 18);
  const u64 pws = (u64)ws;
  asm volatile(
"s_mov_b64 s[36:37], %[x]\n"
"s_and_b32 s37, s37, 0xffff\n"
"s_mov_b32 s38, 204800\n"
"s_mov_b32 s39, 0x20000\n"
"s_mov_b64 s[40:41], %[wq]\n"
"s_and_b32 s41, s41, 0xffff\n"
"s_mov_b32 s42, 5242880\n"
"s_mov_b32 s43, 0x20000\n"
"s_bfe_u32 s78, %[info], 0x30003\n"
"s_lshl_b32 s78, s78, 10\n"
"s_sub_u32 s42, s42, s78\n"
"s_bfe_u32 s73, %[info], 0x20000\n"
"s_and_b32 s92, s73, 0\n"
"s_mov_b32 s74, 0\n"
"s_mov_b32 s69, 40960\n"
"s_mov_b32 s70, 81920\n"
"s_mov_b32 s71, 122880\n"
"s_mov_b32 s72, 163840\n"
"s_lshl_b32 s80, s73, 11\n"
"s_lshl_b32 s81, s73, 10\n"
"s_bfe_u32 s87, %[info], 0x10002\n"
"v_mbcnt_lo_u32_b32 v16, -1, 0\n"
"v_mbcnt_hi_u32_b32 v16, -1, v16\n"
"v_bfe_u32 v0, v16, 2, 3\n"
"s_lshl_b32 s78, s73, 3\n"
"v_add_u32 v3, s78, v0\n"
"v_lshrrev_b32 v1, 5, v16\n"
"v_lshlrev_b32 v1, 6, v1\n"
"v_and_b32 v2, 3, v16\n"
"s_and_b32 s83, s73, 1\n"
"s_mul_i32 s78, s83, 3\n"
"v_xor_b32 v4, s78, v2\n"
"v_lshl_add_u32 v4, v4, 4, v1\n"
"v_lshl_add_u32 v5, v2, 4, v1\n"
"v_mul_u32_u24 v17, 1280, v3\n"
"v_add_u32 v248, v17, v4\n"
"v_lshl_add_u32 v247, v3, 13, v5\n"
"v_accvgpr_write_b32 a172, v247\n"
"v_accvgpr_write_b32 a173, v248\n"
"s_lshl_b32 s78, s87, 9\n"
"v_add_u32 v247, s78, v247\n"
"s_mov_b32 s24, 128\n"
"s_bfe_u32 s17, %[info], 0x4000e\n"
"s_lshl_b32 s16, s17, 19\n"
"s_lshl_b32 s17, s17, 7\n"
"v_add_u32 v248, s17, v248\n"
"s_mov_b64 s[56:57], %[ws]\n"
"s_and_b32 s57, s57, 0xffff\n"
"s_mov_b32 s58, 1024\n"
"s_mov_b32 s59, 0x20000\n"
"s_bfe_u32 s78, %[info], 0x50006\n"
"s_mul_i32 s78, s78, 1024\n"
"s_add_u32 s78, s78, 62914560\n"
"s_add_u32 s56, s56, s78\n"
"s_addc_u32 s57, s57, 0\n"
"v_mov_b32 v29, 0\n"
"buffer_load_dwordx4 v[32:35], v29, s[56:59], 0 offen sc1\n"
"s_add_u32 s83, s80, 32768\n"
"s_add_u32 m0, s83, 0\n"
"s_nop 0\n"
"buffer_load_dwordx4 v247, s[40:43], s16 offen lds\n"
"s_add_u32 m0, s83, 896\n"
"s_nop 0\n"
"buffer_load_dwordx4 v247, s[40:43], s16 offen offset:128 lds\n"
"s_add_u32 m0, s83, 7936\n"
"s_nop 0\n"
"buffer_load_dwordx4 v247, s[40:43], s16 offen offset:256 lds\n"
"s_add_u32 m0, s83, 8832\n"
"s_nop 0\n"
"buffer_load_dwordx4 v247, s[40:43], s16 offen offset:384 lds\n"
"s_add_u32 s16, s16, 262144\n"
"s_cmp_eq_u32 s16, 5242880\n"
"s_cselect_b32 s16, 0, s16\n"
"s_add_u32 s83, s81, 65536\n"
"s_add_u32 m0, s83, 0\n"
"s_nop 0\n"
"buffer_load_dwordx4 v248, s[36:39], 0 offen lds\n"
"s_add_u32 m0, s83, 4096\n"
"s_nop 0\n"
"buffer_load_dwordx4 v248, s[36:39], s69 offen lds\n"
"s_add_u32 m0, s83, 8192\n"
"s_nop 0\n"
"buffer_load_dwordx4 v248, s[36:39], s70 offen lds\n"
"s_add_u32 m0, s83, 12288\n"
"s_nop 0\n"
"buffer_load_dwordx4 v248, s[36:39], s71 offen lds\n"
"s_add_u32 m0, s83, 16384\n"
"s_nop 0\n"
"buffer_load_dwordx4 v248, s[36:39], s72 offen lds\n"
"s_add_u32 s17, s17, 128\n"
"s_cmp_eq_u32 s17, 1280\n"
"s_cselect_b32 s18, -1152, s24\n"
"s_cselect_b32 s17, 0, s17\n"
"v_add_u32 v248, s18, v248\n"
"s_getpc_b64 s[64:65]\n"
"s_and_b32 s65, s65, 0xffff\n"
"s_mov_b32 s66, L_codeend%=-L_codeprobe%=\n"
"L_codeprobe%=:\n"
"s_mov_b32 s67, 0x20000\n"
"s_bfe_u32 s78, %[info], 0x50012\n"
"s_lshl_b32 s78, s78, 2\n"
"s_add_u32 s78, s78, s73\n"
"s_lshl_b32 s78, s78, 9\n"
"v_lshlrev_b32 v18, 7, v16\n"
"v_cmp_gt_u32 vcc, 4, v16\n"
"v_mov_b32 v19, 1073741824\n"
"s_nop 0\n"
"v_cndmask_b32 v18, v19, v18, vcc\n"
"buffer_load_dword a175, v18, s[64:67], s78 offen\n"
"s_add_u32 s83, s80, 49152\n"
"s_add_u32 m0, s83, 0\n"
"s_nop 0\n"
"buffer_load_dwordx4 v247, s[40:43], s16 offen lds\n"
"s_add_u32 m0, s83, 896\n"
"s_nop 0\n"
"buffer_load_dwordx4 v247, s[40:43], s16 offen offset:128 lds\n"
"s_add_u32 m0, s83, 7936\n"
"s_nop 0\n"
"buffer_load_dwordx4 v247, s[40:43], s16 offen offset:256 lds\n"
"s_add_u32 m0, s83, 8832\n"
"s_nop 0\n"
"buffer_load_dwordx4 v247, s[40:43], s16 offen offset:384 lds\n"
"s_add_u32 s16, s16, 262144\n"
"s_cmp_eq_u32 s16, 5242880\n"
"s_cselect_b32 s16, 0, s16\n"
"s_bfe_u32 s78, %[info], 0x50006\n"
"s_mul_i32 s78, s78, 20\n"
"s_mul_i32 s83, s73, 5\n"
"s_add_u32 s78, s78, s83\n"
"s_lshl_b32 s78, s78, 13\n"
"s_xor_b32 s83, s87, 1\n"
"s_lshl_b32 s83, s83, 9\n"
"s_add_u32 s78, s78, s83\n"
"v_lshrrev_b32 v18, 2, v16\n"
"v_lshlrev_b32 v18, 13, v18\n"
"v_lshl_add_u32 v18, v2, 7, v18\n"
"v_add_u32 v18, s78, v18\n"
"v_cmp_gt_u32 vcc, 20, v16\n"
"v_mov_b32 v19, 1073741824\n"
"s_nop 0\n"
"v_cndmask_b32 v252, v19, v18, vcc\n"
"s_mov_b64 s[44:45], %[k]\n"
"s_and_b32 s45, s45, 0xffff\n"
"s_mov_b32 s46, 524288\n"
"s_mov_b32 s47, 0x20000\n"
"s_mov_b64 s[48:49], %[v]\n"
"s_and_b32 s49, s49, 0xffff\n"
"s_mov_b32 s50, 524288\n"
"s_mov_b32 s51, 0x20000\n"
"s_mov_b64 s[52:53], %[wo]\n"
"s_and_b32 s53, s53, 0xffff\n"
"s_mov_b32 s54, 655360\n"
"s_mov_b32 s55, 0x20000\n"
"s_mov_b64 s[60:61], %[ws]\n"
"s_and_b32 s61, s61, 0xffff\n"
"s_mov_b32 s62, 1638400\n"
"s_mov_b32 s63, 0x20000\n"
"s_bfe_u32 s78, %[info], 0x50006\n"
"s_mul_i32 s78, s78, 1638400\n"
"s_add_u32 s78, s78, 67108864\n"
"s_add_u32 s60, s60, s78\n"
"s_addc_u32 s61, s61, 0\n"
"s_mov_b32 s68, 131072\n"
"s_mov_b32 s85, 8192\n"
"s_mov_b32 s82, 0x3e0293ee\n"
"s_lshl_b32 s84, s87, 18\n"
"s_mov_b32 s86, 3\n"
"s_mov_b32 s91, 0x53800000\n"
"s_mov_b32 s88, 16384\n"
"s_mov_b32 s89, 147456\n"
"s_mov_b32 s90, 24576\n"
"s_mov_b32 s93, 155648\n"
"s_mov_b32 s22, 8192\n"
"s_mov_b32 s23, 139264\n"
"s_mov_b32 s94, 32768\n"
"s_mov_b32 s95, 163840\n"
"s_bfe_u32 s79, %[info], 0x50006\n"
"s_mul_i32 s79, s79, 20480\n"
"s_add_u32 s64, s52, s79\n"
"s_addc_u32 s65, s53, 0\n"
"s_mov_b32 s66, 20480\n"
"s_mov_b32 s67, 0x20000\n"
"s_lshl_b32 s78, s73, 13\n"
"v_lshlrev_b32 v253, 7, v16\n"
"v_add_u32 v253, s78, v253\n"
"v_lshl_add_u32 v243, v3, 8, v4\n"
"v_accvgpr_write_b32 a170, v243\n"
"v_add_u32 v243, s84, v243\n"
"v_lshrrev_b32 v6, 2, v0\n"
"v_lshlrev_b32 v6, 1, v6\n"
"v_xor_b32 v6, v6, v2\n"
"v_lshl_add_u32 v6, v6, 4, v1\n"
"v_lshl_add_u32 v244, v3, 8, v6\n"
"v_accvgpr_write_b32 a171, v244\n"
"v_add_u32 v244, s84, v244\n"
"v_add_u32 v17, v17, v6\n"
"v_accvgpr_write_b32 a174, v17\n"
"v_lshrrev_b32 v7, 4, v16\n"
"v_bfe_u32 v8, v16, 2, 2\n"
"v_bfe_u32 v9, v16, 3, 1\n"
"v_and_b32 v10, 7, v16\n"
"v_mul_u32_u24 v11, 3, v9\n"
"v_xor_b32 v11, v11, v7\n"
"v_lshlrev_b32 v11, 4, v11\n"
"v_lshl_add_u32 v11, v10, 6, v11\n"
"s_lshr_b32 s78, s73, 1\n"
"s_lshl_b32 s78, s78, 13\n"
"v_lshl_add_u32 v12, v9, 11, v11\n"
"v_add_u32 v12, s78, v12\n"
"v_accvgpr_write_b32 a168, v12\n"
"v_and_b32 v13, 1, v7\n"
"v_lshl_add_u32 v14, v13, 2, v8\n"
"v_lshlrev_b32 v14, 6, v14\n"
"v_lshl_add_u32 v14, v13, 5, v14\n"
"v_lshl_add_u32 v14, v2, 3, v14\n"
"v_lshrrev_b32 v15, 1, v7\n"
"v_lshl_add_u32 v14, v15, 11, v14\n"
"v_add_u32 v14, s78, v14\n"
"v_accvgpr_write_b32 a169, v14\n"
"v_lshlrev_b32 v249, 11, v7\n"
"v_lshl_add_u32 v249, v8, 6, v249\n"
"v_lshl_add_u32 v249, v2, 4, v249\n"
"v_add_u32 v249, s78, v249\n"
"s_and_b32 s78, s73, 1\n"
"s_mul_i32 s78, s78, 10240\n"
"v_lshl_add_u32 v250, v9, 10, v11\n"
"v_add_u32 v250, s78, v250\n"
"v_lshlrev_b32 v251, 4, v16\n"
"v_mov_b32 v255, 0x3f803f80\n"
"v_accvgpr_write_b32 a164, v255\n"
"v_accvgpr_write_b32 a165, v255\n"
"v_accvgpr_write_b32 a166, v255\n"
"v_accvgpr_write_b32 a167, v255\n"
"s_add_u32 s83, s80, 86016\n"
"s_add_u32 m0, s83, 0\n"
"s_nop 0\n"
"buffer_load_dwordx4 v247, s[40:43], s16 offen lds\n"
"s_add_u32 m0, s83, 896\n"
"s_nop 0\n"
"buffer_load_dwordx4 v247, s[40:43], s16 offen offset:128 lds\n"
"s_add_u32 m0, s83, 7936\n"
"s_nop 0\n"
"buffer_load_dwordx4 v247, s[40:43], s16 offen offset:256 lds\n"
"s_add_u32 m0, s83, 8832\n"
"s_nop 0\n"
"buffer_load_dwordx4 v247, s[40:43], s16 offen offset:384 lds\n"
"s_add_u32 s16, s16, 262144\n"
"s_cmp_eq_u32 s16, 5242880\n"
"s_cselect_b32 s16, 0, s16\n"
"s_add_u32 s83, s81, 102400\n"
"s_add_u32 m0, s83, 0\n"
"s_nop 0\n"
"buffer_load_dwordx4 v248, s[36:39], 0 offen lds\n"
"s_add_u32 m0, s83, 4096\n"
"s_nop 0\n"
"buffer_load_dwordx4 v248, s[36:39], s69 offen lds\n"
"s_add_u32 m0, s83, 8192\n"
"s_nop 0\n"
"buffer_load_dwordx4 v248, s[36:39], s70 offen lds\n"
"s_add_u32 m0, s83, 12288\n"
"s_nop 0\n"
"buffer_load_dwordx4 v248, s[36:39], s71 offen lds\n"
"s_add_u32 m0, s83, 16384\n"
"s_nop 0\n"
"buffer_load_dwordx4 v248, s[36:39], s72 offen lds\n"
"s_add_u32 s17, s17, 128\n"
"s_cmp_eq_u32 s17, 1280\n"
"s_cselect_b32 s18, -1152, s24\n"
"s_cselect_b32 s17, 0, s17\n"
"v_add_u32 v248, s18, v248\n"
"s_add_u32 s83, s80, 147456\n"
"s_add_u32 m0, s83, 0\n"
"s_nop 0\n"
"buffer_load_dwordx4 v247, s[40:43], s16 offen lds\n"
"s_add_u32 m0, s83, 896\n"
"s_nop 0\n"
"buffer_load_dwordx4 v247, s[40:43], s16 offen offset:128 lds\n"
"s_add_u32 m0, s83, 7936\n"
"s_nop 0\n"
"buffer_load_dwordx4 v247, s[40:43], s16 offen offset:256 lds\n"
"s_add_u32 m0, s83, 8832\n"
"s_nop 0\n"
"buffer_load_dwordx4 v247, s[40:43], s16 offen offset:384 lds\n"
"s_add_u32 s16, s16, 262144\n"
"s_cmp_eq_u32 s16, 5242880\n"
"s_cselect_b32 s16, 0, s16\n"
"v_add_u32 v245, 32768, v249\n"
"v_add_u32 v246, 65536, v250\n"
"s_waitcnt vmcnt(18)\n"
"s_barrier\n"
"ds_read_b128 v[160:163], v246 offset:0\n"
"ds_read_b128 v[164:167], v246 offset:2048\n"
"ds_read_b128 v[168:171], v246 offset:4096\n"
"ds_read_b128 v[172:175], v246 offset:6144\n"
"ds_read_b128 v[176:179], v246 offset:8192\n"
"ds_read_b64_tr_b16 a[80:81], v245 offset:0\n"
"ds_read_b64_tr_b16 a[82:83], v245 offset:256\n"
"ds_read_b64_tr_b16 a[84:85], v245 offset:8\n"
"ds_read_b64_tr_b16 a[86:87], v245 offset:264\n"
"ds_read_b64_tr_b16 a[88:89], v245 offset:512\n"
"ds_read_b64_tr_b16 a[90:91], v245 offset:768\n"
"ds_read_b64_tr_b16 a[92:93], v245 offset:520\n"
"ds_read_b64_tr_b16 a[94:95], v245 offset:776\n"
"ds_read_b64_tr_b16 a[96:97], v245 offset:1024\n"
"ds_read_b64_tr_b16 a[98:99], v245 offset:1280\n"
"ds_read_b64_tr_b16 a[100:101], v245 offset:1032\n"
"ds_read_b64_tr_b16 a[102:103], v245 offset:1288\n"
"ds_read_b64_tr_b16 a[104:105], v245 offset:1536\n"
"ds_read_b64_tr_b16 a[106:107], v245 offset:1792\n"
"ds_read_b64_tr_b16 a[108:109], v245 offset:1544\n"
"ds_read_b64_tr_b16 a[110:111], v245 offset:1800\n"
"v_readfirstlane_b32 s30, v32\n"
"v_readfirstlane_b32 s31, v33\n"
"v_readfirstlane_b32 s29, v34\n"
"s_nop 3\n"
"s_add_u32 s30, s30, 1\n"
"s_addc_u32 s31, s31, 0\n"
"L_q%=:\n"
"s_waitcnt vmcnt(13) lgkmcnt(0)\n"
"s_barrier\n"
"v_mfma_f32_16x16x32_bf16 v[0:3], a[80:83], v[160:163], 0\n"
"v_add_u32 v245, 49152, v249\n"
"s_add_u32 m0, s80, 32768\n"
"v_mfma_f32_16x16x32_bf16 v[20:23], a[84:87], v[160:163], 0\n"
"ds_read_b128 v[180:183], v246 offset:512\n"
"v_mfma_f32_16x16x32_bf16 v[40:43], a[88:91], v[160:163], 0\n"
"buffer_load_dwordx4 v247, s[40:43], s16 offen lds\n"
"v_mfma_f32_16x16x32_bf16 v[60:63], a[92:95], v[160:163], 0\n"
"ds_read_b128 v[184:187], v246 offset:2560\n"
"v_mfma_f32_16x16x32_bf16 v[80:83], a[96:99], v[160:163], 0\n"
"ds_read_b128 v[188:191], v246 offset:4608\n"
"s_add_u32 m0, s80, 33664\n"
"v_mfma_f32_16x16x32_bf16 v[100:103], a[100:103], v[160:163], 0\n"
"ds_read_b128 v[192:195], v246 offset:6656\n"
"v_mfma_f32_16x16x32_bf16 v[120:123], a[104:107], v[160:163], 0\n"
"buffer_load_dwordx4 v247, s[40:43], s16 offen offset:128 lds\n"
"v_mfma_f32_16x16x32_bf16 v[140:143], a[108:111], v[160:163], 0\n"
"ds_read_b128 v[196:199], v246 offset:8704\n"
"v_mfma_f32_16x16x32_bf16 v[4:7], a[80:83], v[164:167], 0\n"
"ds_read_b64_tr_b16 a[112:113], v245 offset:0\n"
"s_add_u32 m0, s80, 40704\n"
"v_mfma_f32_16x16x32_bf16 v[24:27], a[84:87], v[164:167], 0\n"
"ds_read_b64_tr_b16 a[114:115], v245 offset:256\n"
"v_mfma_f32_16x16x32_bf16 v[44:47], a[88:91], v[164:167], 0\n"
"buffer_load_dwordx4 v247, s[40:43], s16 offen offset:256 lds\n"
"v_mfma_f32_16x16x32_bf16 v[64:67], a[92:95], v[164:167], 0\n"
"ds_read_b64_tr_b16 a[116:117], v245 offset:8\n"
"v_mfma_f32_16x16x32_bf16 v[84:87], a[96:99], v[164:167], 0\n"
"ds_read_b64_tr_b16 a[118:119], v245 offset:264\n"
"s_add_u32 m0, s80, 41600\n"
"v_mfma_f32_16x16x32_bf16 v[104:107], a[100:103], v[164:167], 0\n"
"ds_read_b64_tr_b16 a[120:121], v245 offset:512\n"
"v_mfma_f32_16x16x32_bf16 v[124:127], a[104:107], v[164:167], 0\n"
"buffer_load_dwordx4 v247, s[40:43], s16 offen offset:384 lds\n"
"v_mfma_f32_16x16x32_bf16 v[144:147], a[108:111], v[164:167], 0\n"
"ds_read_b64_tr_b16 a[122:123], v245 offset:768\n"
"s_add_u32 s16, s16, 262144\n"
"v_mfma_f32_16x16x32_bf16 v[8:11], a[80:83], v[168:171], 0\n"
"ds_read_b64_tr_b16 a[124:125], v245 offset:520\n"
"v_mfma_f32_16x16x32_bf16 v[28:31], a[84:87], v[168:171], 0\n"
"ds_read_b64_tr_b16 a[126:127], v245 offset:776\n"
"v_mfma_f32_16x16x32_bf16 v[48:51], a[88:91], v[168:171], 0\n"
"ds_read_b64_tr_b16 a[128:129], v245 offset:1024\n"
"v_mfma_f32_16x16x32_bf16 v[68:71], a[92:95], v[168:171], 0\n"
"ds_read_b64_tr_b16 a[130:131], v245 offset:1280\n"
"v_mfma_f32_16x16x32_bf16 v[88:91], a[96:99], v[168:171], 0\n"
"ds_read_b64_tr_b16 a[132:133], v245 offset:1032\n"
"v_mfma_f32_16x16x32_bf16 v[108:111], a[100:103], v[168:171], 0\n"
"ds_read_b64_tr_b16 a[134:135], v245 offset:1288\n"
"v_mfma_f32_16x16x32_bf16 v[128:131], a[104:107], v[168:171], 0\n"
"ds_read_b64_tr_b16 a[136:137], v245 offset:1536\n"
"v_mfma_f32_16x16x32_bf16 v[148:151], a[108:111], v[168:171], 0\n"
"ds_read_b64_tr_b16 a[138:139], v245 offset:1792\n"
"v_mfma_f32_16x16x32_bf16 v[12:15], a[80:83], v[172:175], 0\n"
"ds_read_b64_tr_b16 a[140:141], v245 offset:1544\n"
"v_mfma_f32_16x16x32_bf16 v[32:35], a[84:87], v[172:175], 0\n"
"ds_read_b64_tr_b16 a[142:143], v245 offset:1800\n"
"v_mfma_f32_16x16x32_bf16 v[52:55], a[88:91], v[172:175], 0\n"
"s_cmp_eq_u32 s16, 5242880\n"
"s_cselect_b32 s16, 0, s16\n"
"v_mfma_f32_16x16x32_bf16 v[72:75], a[92:95], v[172:175], 0\n"
"v_mfma_f32_16x16x32_bf16 v[92:95], a[96:99], v[172:175], 0\n"
"v_mfma_f32_16x16x32_bf16 v[112:115], a[100:103], v[172:175], 0\n"
"v_mfma_f32_16x16x32_bf16 v[132:135], a[104:107], v[172:175], 0\n"
"v_mfma_f32_16x16x32_bf16 v[152:155], a[108:111], v[172:175], 0\n"
"v_mfma_f32_16x16x32_bf16 v[16:19], a[80:83], v[176:179], 0\n"
"v_mfma_f32_16x16x32_bf16 v[36:39], a[84:87], v[176:179], 0\n"
"v_mfma_f32_16x16x32_bf16 v[56:59], a[88:91], v[176:179], 0\n"
"v_mfma_f32_16x16x32_bf16 v[76:79], a[92:95], v[176:179], 0\n"
"v_mfma_f32_16x16x32_bf16 v[96:99], a[96:99], v[176:179], 0\n"
"v_mfma_f32_16x16x32_bf16 v[116:119], a[100:103], v[176:179], 0\n"
"v_mfma_f32_16x16x32_bf16 v[136:139], a[104:107], v[176:179], 0\n"
"v_mfma_f32_16x16x32_bf16 v[156:159], a[108:111], v[176:179], 0\n"
"s_waitcnt vmcnt(8) lgkmcnt(0)\n"
"s_barrier\n"
"v_mfma_f32_16x16x32_bf16 v[0:3], a[112:115], v[180:183], v[0:3]\n"
"v_add_u32 v245, 86016, v249\n"
"v_add_u32 v246, 102400, v250\n"
"v_mfma_f32_16x16x32_bf16 v[20:23], a[116:119], v[180:183], v[20:23]\n"
"ds_read_b128 v[160:163], v246 offset:0\n"
"s_add_u32 m0, s81, 65536\n"
"v_mfma_f32_16x16x32_bf16 v[40:43], a[120:123], v[180:183], v[40:43]\n"
"buffer_load_dwordx4 v248, s[36:39], 0 offen lds\n"
"v_mfma_f32_16x16x32_bf16 v[60:63], a[124:127], v[180:183], v[60:63]\n"
"ds_read_b128 v[164:167], v246 offset:2048\n"
"s_add_u32 m0, s81, 69632\n"
"v_mfma_f32_16x16x32_bf16 v[80:83], a[128:131], v[180:183], v[80:83]\n"
"ds_read_b128 v[168:171], v246 offset:4096\n"
"v_mfma_f32_16x16x32_bf16 v[100:103], a[132:135], v[180:183], v[100:103]\n"
"buffer_load_dwordx4 v248, s[36:39], s69 offen lds\n"
"v_mfma_f32_16x16x32_bf16 v[120:123], a[136:139], v[180:183], v[120:123]\n"
"ds_read_b128 v[172:175], v246 offset:6144\n"
"s_add_u32 m0, s81, 73728\n"
"v_mfma_f32_16x16x32_bf16 v[140:143], a[140:143], v[180:183], v[140:143]\n"
"ds_read_b128 v[176:179], v246 offset:8192\n"
"v_mfma_f32_16x16x32_bf16 v[4:7], a[112:115], v[184:187], v[4:7]\n"
"buffer_load_dwordx4 v248, s[36:39], s70 offen lds\n"
"v_mfma_f32_16x16x32_bf16 v[24:27], a[116:119], v[184:187], v[24:27]\n"
"ds_read_b64_tr_b16 a[80:81], v245 offset:0\n"
"s_add_u32 m0, s81, 77824\n"
"v_mfma_f32_16x16x32_bf16 v[44:47], a[120:123], v[184:187], v[44:47]\n"
"ds_read_b64_tr_b16 a[82:83], v245 offset:256\n"
"v_mfma_f32_16x16x32_bf16 v[64:67], a[124:127], v[184:187], v[64:67]\n"
"buffer_load_dwordx4 v248, s[36:39], s71 offen lds\n"
"v_mfma_f32_16x16x32_bf16 v[84:87], a[128:131], v[184:187], v[84:87]\n"
"ds_read_b64_tr_b16 a[84:85], v245 offset:8\n"
"s_add_u32 m0, s81, 81920\n"
"v_mfma_f32_16x16x32_bf16 v[104:107], a[132:135], v[184:187], v[104:107]\n"
"ds_read_b64_tr_b16 a[86:87], v245 offset:264\n"
"v_mfma_f32_16x16x32_bf16 v[124:127], a[136:139], v[184:187], v[124:127]\n"
"buffer_load_dwordx4 v248, s[36:39], s72 offen lds\n"
"v_mfma_f32_16x16x32_bf16 v[144:147], a[140:143], v[184:187], v[144:147]\n"
"ds_read_b64_tr_b16 a[88:89], v245 offset:512\n"
"s_add_u32 m0, s80, 49152\n"
"v_mfma_f32_16x16x32_bf16 v[8:11], a[112:115], v[188:191], v[8:11]\n"
"ds_read_b64_tr_b16 a[90:91], v245 offset:768\n"
"s_add_u32 s17, s17, 128\n"
"v_mfma_f32_16x16x32_bf16 v[28:31], a[116:119], v[188:191], v[28:31]\n"
"buffer_load_dwordx4 v247, s[40:43], s16 offen lds\n"
"v_mfma_f32_16x16x32_bf16 v[48:51], a[120:123], v[188:191], v[48:51]\n"
"ds_read_b64_tr_b16 a[92:93], v245 offset:520\n"
"s_add_u32 m0, s80, 50048\n"
"v_mfma_f32_16x16x32_bf16 v[68:71], a[124:127], v[188:191], v[68:71]\n"
"ds_read_b64_tr_b16 a[94:95], v245 offset:776\n"
"v_mfma_f32_16x16x32_bf16 v[88:91], a[128:131], v[188:191], v[88:91]\n"
"buffer_load_dwordx4 v247, s[40:43], s16 offen offset:128 lds\n"
"v_mfma_f32_16x16x32_bf16 v[108:111], a[132:135], v[188:191], v[108:111]\n"
"ds_read_b64_tr_b16 a[96:97], v245 offset:1024\n"
"s_add_u32 m0, s80, 57088\n"
"v_mfma_f32_16x16x32_bf16 v[128:131], a[136:139], v[188:191], v[128:131]\n"
"ds_read_b64_tr_b16 a[98:99], v245 offset:1280\n"
"v_mfma_f32_16x16x32_bf16 v[148:151], a[140:143], v[188:191], v[148:151]\n"
"buffer_load_dwordx4 v247, s[40:43], s16 offen offset:256 lds\n"
"v_mfma_f32_16x16x32_bf16 v[12:15], a[112:115], v[192:195], v[12:15]\n"
"ds_read_b64_tr_b16 a[100:101], v245 offset:1032\n"
"s_add_u32 m0, s80, 57984\n"
"v_mfma_f32_16x16x32_bf16 v[32:35], a[116:119], v[192:195], v[32:35]\n"
"ds_read_b64_tr_b16 a[102:103], v245 offset:1288\n"
"v_mfma_f32_16x16x32_bf16 v[52:55], a[120:123], v[192:195], v[52:55]\n"
"buffer_load_dwordx4 v247, s[40:43], s16 offen offset:384 lds\n"
"v_mfma_f32_16x16x32_bf16 v[72:75], a[124:127], v[192:195], v[72:75]\n"
"ds_read_b64_tr_b16 a[104:105], v245 offset:1536\n"
"s_add_u32 s16, s16, 262144\n"
"v_mfma_f32_16x16x32_bf16 v[92:95], a[128:131], v[192:195], v[92:95]\n"
"ds_read_b64_tr_b16 a[106:107], v245 offset:1792\n"
"v_mfma_f32_16x16x32_bf16 v[112:115], a[132:135], v[192:195], v[112:115]\n"
"buffer_load_dwordx4 a[16:19], v244, s[48:51], s22 offen nt\n"
"v_mfma_f32_16x16x32_bf16 v[132:135], a[136:139], v[192:195], v[132:135]\n"
"ds_read_b64_tr_b16 a[108:109], v245 offset:1544\n"
"v_mfma_f32_16x16x32_bf16 v[152:155], a[140:143], v[192:195], v[152:155]\n"
"ds_read_b64_tr_b16 a[110:111], v245 offset:1800\n"
"v_mfma_f32_16x16x32_bf16 v[16:19], a[112:115], v[196:199], v[16:19]\n"
"buffer_load_dwordx4 a[36:39], v244, s[48:51], s22 offen offset:128 nt\n"
"v_mfma_f32_16x16x32_bf16 v[36:39], a[116:119], v[196:199], v[36:39]\n"
"s_cmp_eq_u32 s17, 1280\n"
"s_cselect_b32 s18, -1152, s24\n"
"s_cselect_b32 s17, 0, s17\n"
"v_mfma_f32_16x16x32_bf16 v[56:59], a[120:123], v[196:199], v[56:59]\n"
"v_add_u32 v248, s18, v248\n"
"v_mfma_f32_16x16x32_bf16 v[76:79], a[124:127], v[196:199], v[76:79]\n"
"buffer_load_dwordx4 a[56:59], v244, s[48:51], s23 offen nt\n"
"v_mfma_f32_16x16x32_bf16 v[96:99], a[128:131], v[196:199], v[96:99]\n"
"s_cmp_eq_u32 s16, 5242880\n"
"s_cselect_b32 s16, 0, s16\n"
"v_mfma_f32_16x16x32_bf16 v[116:119], a[132:135], v[196:199], v[116:119]\n"
"v_mfma_f32_16x16x32_bf16 v[136:139], a[136:139], v[196:199], v[136:139]\n"
"buffer_load_dwordx4 a[76:79], v244, s[48:51], s23 offen offset:128 nt\n"
"v_mfma_f32_16x16x32_bf16 v[156:159], a[140:143], v[196:199], v[156:159]\n"
"s_waitcnt vmcnt(17) lgkmcnt(0)\n"
"s_barrier\n"
"v_mfma_f32_16x16x32_bf16 v[0:3], a[80:83], v[160:163], v[0:3]\n"
"v_add_u32 v245, 147456, v249\n"
"s_add_u32 m0, s80, 86016\n"
"v_mfma_f32_16x16x32_bf16 v[20:23], a[84:87], v[160:163], v[20:23]\n"
"ds_read_b128 v[180:183], v246 offset:512\n"
"v_mfma_f32_16x16x32_bf16 v[40:43], a[88:91], v[160:163], v[40:43]\n"
"buffer_load_dwordx4 v247, s[40:43], s16 offen lds\n"
"v_mfma_f32_16x16x32_bf16 v[60:63], a[92:95], v[160:163], v[60:63]\n"
"ds_read_b128 v[184:187], v246 offset:2560\n"
"v_mfma_f32_16x16x32_bf16 v[80:83], a[96:99], v[160:163], v[80:83]\n"
"ds_read_b128 v[188:191], v246 offset:4608\n"
"s_add_u32 m0, s80, 86912\n"
"v_mfma_f32_16x16x32_bf16 v[100:103], a[100:103], v[160:163], v[100:103]\n"
"ds_read_b128 v[192:195], v246 offset:6656\n"
"v_mfma_f32_16x16x32_bf16 v[120:123], a[104:107], v[160:163], v[120:123]\n"
"buffer_load_dwordx4 v247, s[40:43], s16 offen offset:128 lds\n"
"v_mfma_f32_16x16x32_bf16 v[140:143], a[108:111], v[160:163], v[140:143]\n"
"ds_read_b128 v[196:199], v246 offset:8704\n"
"v_mfma_f32_16x16x32_bf16 v[4:7], a[80:83], v[164:167], v[4:7]\n"
"ds_read_b64_tr_b16 a[112:113], v245 offset:0\n"
"s_add_u32 m0, s80, 93952\n"
"v_mfma_f32_16x16x32_bf16 v[24:27], a[84:87], v[164:167], v[24:27]\n"
"ds_read_b64_tr_b16 a[114:115], v245 offset:256\n"
"v_mfma_f32_16x16x32_bf16 v[44:47], a[88:91], v[164:167], v[44:47]\n"
"buffer_load_dwordx4 v247, s[40:43], s16 offen offset:256 lds\n"
"v_mfma_f32_16x16x32_bf16 v[64:67], a[92:95], v[164:167], v[64:67]\n"
"ds_read_b64_tr_b16 a[116:117], v245 offset:8\n"
"v_mfma_f32_16x16x32_bf16 v[84:87], a[96:99], v[164:167], v[84:87]\n"
"ds_read_b64_tr_b16 a[118:119], v245 offset:264\n"
"s_add_u32 m0, s80, 94848\n"
"v_mfma_f32_16x16x32_bf16 v[104:107], a[100:103], v[164:167], v[104:107]\n"
"ds_read_b64_tr_b16 a[120:121], v245 offset:512\n"
"v_mfma_f32_16x16x32_bf16 v[124:127], a[104:107], v[164:167], v[124:127]\n"
"buffer_load_dwordx4 v247, s[40:43], s16 offen offset:384 lds\n"
"v_mfma_f32_16x16x32_bf16 v[144:147], a[108:111], v[164:167], v[144:147]\n"
"ds_read_b64_tr_b16 a[122:123], v245 offset:768\n"
"s_add_u32 s16, s16, 262144\n"
"v_mfma_f32_16x16x32_bf16 v[8:11], a[80:83], v[168:171], v[8:11]\n"
"ds_read_b64_tr_b16 a[124:125], v245 offset:520\n"
"v_mfma_f32_16x16x32_bf16 v[28:31], a[84:87], v[168:171], v[28:31]\n"
"ds_read_b64_tr_b16 a[126:127], v245 offset:776\n"
"v_mfma_f32_16x16x32_bf16 v[48:51], a[88:91], v[168:171], v[48:51]\n"
"ds_read_b64_tr_b16 a[128:129], v245 offset:1024\n"
"v_mfma_f32_16x16x32_bf16 v[68:71], a[92:95], v[168:171], v[68:71]\n"
"ds_read_b64_tr_b16 a[130:131], v245 offset:1280\n"
"v_mfma_f32_16x16x32_bf16 v[88:91], a[96:99], v[168:171], v[88:91]\n"
"ds_read_b64_tr_b16 a[132:133], v245 offset:1032\n"
"v_mfma_f32_16x16x32_bf16 v[108:111], a[100:103], v[168:171], v[108:111]\n"
"ds_read_b64_tr_b16 a[134:135], v245 offset:1288\n"
"v_mfma_f32_16x16x32_bf16 v[128:131], a[104:107], v[168:171], v[128:131]\n"
"ds_read_b64_tr_b16 a[136:137], v245 offset:1536\n"
"v_mfma_f32_16x16x32_bf16 v[148:151], a[108:111], v[168:171], v[148:151]\n"
"ds_read_b64_tr_b16 a[138:139], v245 offset:1792\n"
"v_mfma_f32_16x16x32_bf16 v[12:15], a[80:83], v[172:175], v[12:15]\n"
"ds_read_b64_tr_b16 a[140:141], v245 offset:1544\n"
"v_mfma_f32_16x16x32_bf16 v[32:35], a[84:87], v[172:175], v[32:35]\n"
"ds_read_b64_tr_b16 a[142:143], v245 offset:1800\n"
"v_mfma_f32_16x16x32_bf16 v[52:55], a[88:91], v[172:175], v[52:55]\n"
"s_cmp_eq_u32 s16, 5242880\n"
"s_cselect_b32 s16, 0, s16\n"
"v_mfma_f32_16x16x32_bf16 v[72:75], a[92:95], v[172:175], v[72:75]\n"
"v_mfma_f32_16x16x32_bf16 v[92:95], a[96:99], v[172:175], v[92:95]\n"
"v_mfma_f32_16x16x32_bf16 v[112:115], a[100:103], v[172:175], v[112:115]\n"
"v_mfma_f32_16x16x32_bf16 v[132:135], a[104:107], v[172:175], v[132:135]\n"
"v_mfma_f32_16x16x32_bf16 v[152:155], a[108:111], v[172:175], v[152:155]\n"
"v_mfma_f32_16x16x32_bf16 v[16:19], a[80:83], v[176:179], v[16:19]\n"
"v_mfma_f32_16x16x32_bf16 v[36:39], a[84:87], v[176:179], v[36:39]\n"
"v_mfma_f32_16x16x32_bf16 v[56:59], a[88:91], v[176:179], v[56:59]\n"
"v_mfma_f32_16x16x32_bf16 v[76:79], a[92:95], v[176:179], v[76:79]\n"
"v_mfma_f32_16x16x32_bf16 v[96:99], a[96:99], v[176:179], v[96:99]\n"
"v_mfma_f32_16x16x32_bf16 v[116:119], a[100:103], v[176:179], v[116:119]\n"
"v_mfma_f32_16x16x32_bf16 v[136:139], a[104:107], v[176:179], v[136:139]\n"
"v_mfma_f32_16x16x32_bf16 v[156:159], a[108:111], v[176:179], v[156:159]\n"
"s_waitcnt vmcnt(12) lgkmcnt(0)\n"
"s_barrier\n"
"v_mfma_f32_16x16x32_bf16 v[0:3], a[112:115], v[180:183], v[0:3]\n"
"v_add_u32 v245, 32768, v249\n"
"v_add_u32 v246, 65536, v250\n"
"v_mfma_f32_16x16x32_bf16 v[20:23], a[116:119], v[180:183], v[20:23]\n"
"ds_read_b128 v[160:163], v246 offset:0\n"
"s_add_u32 m0, s81, 102400\n"
"v_mfma_f32_16x16x32_bf16 v[40:43], a[120:123], v[180:183], v[40:43]\n"
"buffer_load_dwordx4 v248, s[36:39], 0 offen lds\n"
"v_mfma_f32_16x16x32_bf16 v[60:63], a[124:127], v[180:183], v[60:63]\n"
"ds_read_b128 v[164:167], v246 offset:2048\n"
"s_add_u32 m0, s81, 106496\n"
"v_mfma_f32_16x16x32_bf16 v[80:83], a[128:131], v[180:183], v[80:83]\n"
"ds_read_b128 v[168:171], v246 offset:4096\n"
"v_mfma_f32_16x16x32_bf16 v[100:103], a[132:135], v[180:183], v[100:103]\n"
"buffer_load_dwordx4 v248, s[36:39], s69 offen lds\n"
"v_mfma_f32_16x16x32_bf16 v[120:123], a[136:139], v[180:183], v[120:123]\n"
"ds_read_b128 v[172:175], v246 offset:6144\n"
"s_add_u32 m0, s81, 110592\n"
"v_mfma_f32_16x16x32_bf16 v[140:143], a[140:143], v[180:183], v[140:143]\n"
"ds_read_b128 v[176:179], v246 offset:8192\n"
"v_mfma_f32_16x16x32_bf16 v[4:7], a[112:115], v[184:187], v[4:7]\n"
"buffer_load_dwordx4 v248, s[36:39], s70 offen lds\n"
"v_mfma_f32_16x16x32_bf16 v[24:27], a[116:119], v[184:187], v[24:27]\n"
"ds_read_b64_tr_b16 a[80:81], v245 offset:0\n"
"s_add_u32 m0, s81, 114688\n"
"v_mfma_f32_16x16x32_bf16 v[44:47], a[120:123], v[184:187], v[44:47]\n"
"ds_read_b64_tr_b16 a[82:83], v245 offset:256\n"
"v_mfma_f32_16x16x32_bf16 v[64:67], a[124:127], v[184:187], v[64:67]\n"
"buffer_load_dwordx4 v248, s[36:39], s71 offen lds\n"
"v_mfma_f32_16x16x32_bf16 v[84:87], a[128:131], v[184:187], v[84:87]\n"
"ds_read_b64_tr_b16 a[84:85], v245 offset:8\n"
"s_add_u32 m0, s81, 118784\n"
"v_mfma_f32_16x16x32_bf16 v[104:107], a[132:135], v[184:187], v[104:107]\n"
"ds_read_b64_tr_b16 a[86:87], v245 offset:264\n"
"v_mfma_f32_16x16x32_bf16 v[124:127], a[136:139], v[184:187], v[124:127]\n"
"buffer_load_dwordx4 v248, s[36:39], s72 offen lds\n"
"v_mfma_f32_16x16x32_bf16 v[144:147], a[140:143], v[184:187], v[144:147]\n"
"ds_read_b64_tr_b16 a[88:89], v245 offset:512\n"
"s_add_u32 m0, s80, 147456\n"
"v_mfma_f32_16x16x32_bf16 v[8:11], a[112:115], v[188:191], v[8:11]\n"
"ds_read_b64_tr_b16 a[90:91], v245 offset:768\n"
"s_add_u32 s17, s17, 128\n"
"v_mfma_f32_16x16x32_bf16 v[28:31], a[116:119], v[188:191], v[28:31]\n"
"buffer_load_dwordx4 v247, s[40:43], s16 offen lds\n"
"v_mfma_f32_16x16x32_bf16 v[48:51], a[120:123], v[188:191], v[48:51]\n"
"ds_read_b64_tr_b16 a[92:93], v245 offset:520\n"
"s_add_u32 m0, s80, 148352\n"
"v_mfma_f32_16x16x32_bf16 v[68:71], a[124:127], v[188:191], v[68:71]\n"
"ds_read_b64_tr_b16 a[94:95], v245 offset:776\n"
"v_mfma_f32_16x16x32_bf16 v[88:91], a[128:131], v[188:191], v[88:91]\n"
"buffer_load_dwordx4 v247, s[40:43], s16 offen offset:128 lds\n"
"v_mfma_f32_16x16x32_bf16 v[108:111], a[132:135], v[188:191], v[108:111]\n"
"ds_read_b64_tr_b16 a[96:97], v245 offset:1024\n"
"s_add_u32 m0, s80, 155392\n"
"v_mfma_f32_16x16x32_bf16 v[128:131], a[136:139], v[188:191], v[128:131]\n"
"ds_read_b64_tr_b16 a[98:99], v245 offset:1280\n"
"v_mfma_f32_16x16x32_bf16 v[148:151], a[140:143], v[188:191], v[148:151]\n"
"buffer_load_dwordx4 v247, s[40:43], s16 offen offset:256 lds\n"
"v_mfma_f32_16x16x32_bf16 v[12:15], a[112:115], v[192:195], v[12:15]\n"
"ds_read_b64_tr_b16 a[100:101], v245 offset:1032\n"
"s_add_u32 m0, s80, 156288\n"
"v_mfma_f32_16x16x32_bf16 v[32:35], a[116:119], v[192:195], v[32:35]\n"
"ds_read_b64_tr_b16 a[102:103], v245 offset:1288\n"
"v_mfma_f32_16x16x32_bf16 v[52:55], a[120:123], v[192:195], v[52:55]\n"
"buffer_load_dwordx4 v247, s[40:43], s16 offen offset:384 lds\n"
"v_mfma_f32_16x16x32_bf16 v[72:75], a[124:127], v[192:195], v[72:75]\n"
"ds_read_b64_tr_b16 a[104:105], v245 offset:1536\n"
"s_add_u32 s16, s16, 262144\n"
"v_mfma_f32_16x16x32_bf16 v[92:95], a[128:131], v[192:195], v[92:95]\n"
"ds_read_b64_tr_b16 a[106:107], v245 offset:1792\n"
"v_mfma_f32_16x16x32_bf16 v[112:115], a[132:135], v[192:195], v[112:115]\n"
"buffer_load_dwordx4 a[0:3], v243, s[44:47], s88 offen nt\n"
"v_mfma_f32_16x16x32_bf16 v[132:135], a[136:139], v[192:195], v[132:135]\n"
"ds_read_b64_tr_b16 a[108:109], v245 offset:1544\n"
"v_mfma_f32_16x16x32_bf16 v[152:155], a[140:143], v[192:195], v[152:155]\n"
"ds_read_b64_tr_b16 a[110:111], v245 offset:1800\n"
"v_mfma_f32_16x16x32_bf16 v[16:19], a[112:115], v[196:199], v[16:19]\n"
"buffer_load_dwordx4 a[20:23], v243, s[44:47], s88 offen offset:128 nt\n"
"v_mfma_f32_16x16x32_bf16 v[36:39], a[116:119], v[196:199], v[36:39]\n"
"s_cmp_eq_u32 s17, 1280\n"
"s_cselect_b32 s18, -1152, s24\n"
"s_cselect_b32 s17, 0, s17\n"
"v_mfma_f32_16x16x32_bf16 v[56:59], a[120:123], v[196:199], v[56:59]\n"
"v_add_u32 v248, s18, v248\n"
"v_mfma_f32_16x16x32_bf16 v[76:79], a[124:127], v[196:199], v[76:79]\n"
"buffer_load_dwordx4 a[40:43], v243, s[44:47], s89 offen nt\n"
"v_mfma_f32_16x16x32_bf16 v[96:99], a[128:131], v[196:199], v[96:99]\n"
"s_cmp_eq_u32 s16, 5242880\n"
"s_cselect_b32 s16, 0, s16\n"
"v_mfma_f32_16x16x32_bf16 v[116:119], a[132:135], v[196:199], v[116:119]\n"
"v_mfma_f32_16x16x32_bf16 v[136:139], a[136:139], v[196:199], v[136:139]\n"
"buffer_load_dwordx4 a[60:63], v243, s[44:47], s89 offen offset:128 nt\n"
"v_mfma_f32_16x16x32_bf16 v[156:159], a[140:143], v[196:199], v[156:159]\n"
"s_waitcnt vmcnt(21) lgkmcnt(0)\n"
"s_barrier\n"
"v_mfma_f32_16x16x32_bf16 v[0:3], a[80:83], v[160:163], v[0:3]\n"
"v_add_u32 v245, 49152, v249\n"
"s_add_u32 m0, s80, 32768\n"
"v_mfma_f32_16x16x32_bf16 v[20:23], a[84:87], v[160:163], v[20:23]\n"
"ds_read_b128 v[180:183], v246 offset:512\n"
"v_mfma_f32_16x16x32_bf16 v[40:43], a[88:91], v[160:163], v[40:43]\n"
"buffer_load_dwordx4 v247, s[40:43], s16 offen lds\n"
"v_mfma_f32_16x16x32_bf16 v[60:63], a[92:95], v[160:163], v[60:63]\n"
"ds_read_b128 v[184:187], v246 offset:2560\n"
"v_mfma_f32_16x16x32_bf16 v[80:83], a[96:99], v[160:163], v[80:83]\n"
"ds_read_b128 v[188:191], v246 offset:4608\n"
"s_add_u32 m0, s80, 33664\n"
"v_mfma_f32_16x16x32_bf16 v[100:103], a[100:103], v[160:163], v[100:103]\n"
"ds_read_b128 v[192:195], v246 offset:6656\n"
"v_mfma_f32_16x16x32_bf16 v[120:123], a[104:107], v[160:163], v[120:123]\n"
"buffer_load_dwordx4 v247, s[40:43], s16 offen offset:128 lds\n"
"v_mfma_f32_16x16x32_bf16 v[140:143], a[108:111], v[160:163], v[140:143]\n"
"ds_read_b128 v[196:199], v246 offset:8704\n"
"v_mfma_f32_16x16x32_bf16 v[4:7], a[80:83], v[164:167], v[4:7]\n"
"ds_read_b64_tr_b16 a[112:113], v245 offset:0\n"
"s_add_u32 m0, s80, 40704\n"
"v_mfma_f32_16x16x32_bf16 v[24:27], a[84:87], v[164:167], v[24:27]\n"
"ds_read_b64_tr_b16 a[114:115], v245 offset:256\n"
"v_mfma_f32_16x16x32_bf16 v[44:47], a[88:91], v[164:167], v[44:47]\n"
"buffer_load_dwordx4 v247, s[40:43], s16 offen offset:256 lds\n"
"v_mfma_f32_16x16x32_bf16 v[64:67], a[92:95], v[164:167], v[64:67]\n"
"ds_read_b64_tr_b16 a[116:117], v245 offset:8\n"
"v_mfma_f32_16x16x32_bf16 v[84:87], a[96:99], v[164:167], v[84:87]\n"
"ds_read_b64_tr_b16 a[118:119], v245 offset:264\n"
"s_add_u32 m0, s80, 41600\n"
"v_mfma_f32_16x16x32_bf16 v[104:107], a[100:103], v[164:167], v[104:107]\n"
"ds_read_b64_tr_b16 a[120:121], v245 offset:512\n"
"v_mfma_f32_16x16x32_bf16 v[124:127], a[104:107], v[164:167], v[124:127]\n"
"buffer_load_dwordx4 v247, s[40:43], s16 offen offset:384 lds\n"
"v_mfma_f32_16x16x32_bf16 v[144:147], a[108:111], v[164:167], v[144:147]\n"
"ds_read_b64_tr_b16 a[122:123], v245 offset:768\n"
"s_add_u32 s16, s16, 262144\n"
"v_mfma_f32_16x16x32_bf16 v[8:11], a[80:83], v[168:171], v[8:11]\n"
"ds_read_b64_tr_b16 a[124:125], v245 offset:520\n"
"v_mfma_f32_16x16x32_bf16 v[28:31], a[84:87], v[168:171], v[28:31]\n"
"ds_read_b64_tr_b16 a[126:127], v245 offset:776\n"
"v_mfma_f32_16x16x32_bf16 v[48:51], a[88:91], v[168:171], v[48:51]\n"
"ds_read_b64_tr_b16 a[128:129], v245 offset:1024\n"
"v_mfma_f32_16x16x32_bf16 v[68:71], a[92:95], v[168:171], v[68:71]\n"
"ds_read_b64_tr_b16 a[130:131], v245 offset:1280\n"
"v_mfma_f32_16x16x32_bf16 v[88:91], a[96:99], v[168:171], v[88:91]\n"
"ds_read_b64_tr_b16 a[132:133], v245 offset:1032\n"
"v_mfma_f32_16x16x32_bf16 v[108:111], a[100:103], v[168:171], v[108:111]\n"
"ds_read_b64_tr_b16 a[134:135], v245 offset:1288\n"
"v_mfma_f32_16x16x32_bf16 v[128:131], a[104:107], v[168:171], v[128:131]\n"
"ds_read_b64_tr_b16 a[136:137], v245 offset:1536\n"
"v_mfma_f32_16x16x32_bf16 v[148:151], a[108:111], v[168:171], v[148:151]\n"
"ds_read_b64_tr_b16 a[138:139], v245 offset:1792\n"
"v_mfma_f32_16x16x32_bf16 v[12:15], a[80:83], v[172:175], v[12:15]\n"
"ds_read_b64_tr_b16 a[140:141], v245 offset:1544\n"
"v_mfma_f32_16x16x32_bf16 v[32:35], a[84:87], v[172:175], v[32:35]\n"
"ds_read_b64_tr_b16 a[142:143], v245 offset:1800\n"
"v_mfma_f32_16x16x32_bf16 v[52:55], a[88:91], v[172:175], v[52:55]\n"
"s_cmp_eq_u32 s16, 5242880\n"
"s_cselect_b32 s16, 0, s16\n"
"v_mfma_f32_16x16x32_bf16 v[72:75], a[92:95], v[172:175], v[72:75]\n"
"v_mfma_f32_16x16x32_bf16 v[92:95], a[96:99], v[172:175], v[92:95]\n"
"v_mfma_f32_16x16x32_bf16 v[112:115], a[100:103], v[172:175], v[112:115]\n"
"v_mfma_f32_16x16x32_bf16 v[132:135], a[104:107], v[172:175], v[132:135]\n"
"v_mfma_f32_16x16x32_bf16 v[152:155], a[108:111], v[172:175], v[152:155]\n"
"v_mfma_f32_16x16x32_bf16 v[16:19], a[80:83], v[176:179], v[16:19]\n"
"v_mfma_f32_16x16x32_bf16 v[36:39], a[84:87], v[176:179], v[36:39]\n"
"v_mfma_f32_16x16x32_bf16 v[56:59], a[88:91], v[176:179], v[56:59]\n"
"v_mfma_f32_16x16x32_bf16 v[76:79], a[92:95], v[176:179], v[76:79]\n"
"v_mfma_f32_16x16x32_bf16 v[96:99], a[96:99], v[176:179], v[96:99]\n"
"v_mfma_f32_16x16x32_bf16 v[116:119], a[100:103], v[176:179], v[116:119]\n"
"v_mfma_f32_16x16x32_bf16 v[136:139], a[104:107], v[176:179], v[136:139]\n"
"v_mfma_f32_16x16x32_bf16 v[156:159], a[108:111], v[176:179], v[156:159]\n"
"s_waitcnt vmcnt(12) lgkmcnt(0)\n"
"s_barrier\n"
"v_mfma_f32_16x16x32_bf16 v[0:3], a[112:115], v[180:183], v[0:3]\n"
"v_add_u32 v245, 86016, v249\n"
"v_add_u32 v246, 102400, v250\n"
"v_mfma_f32_16x16x32_bf16 v[20:23], a[116:119], v[180:183], v[20:23]\n"
"ds_read_b128 v[160:163], v246 offset:0\n"
"s_add_u32 m0, s81, 65536\n"
"v_mfma_f32_16x16x32_bf16 v[40:43], a[120:123], v[180:183], v[40:43]\n"
"buffer_load_dwordx4 v248, s[36:39], 0 offen lds\n"
"v_mfma_f32_16x16x32_bf16 v[60:63], a[124:127], v[180:183], v[60:63]\n"
"ds_read_b128 v[164:167], v246 offset:2048\n"
"s_add_u32 m0, s81, 69632\n"
"v_mfma_f32_16x16x32_bf16 v[80:83], a[128:131], v[180:183], v[80:83]\n"
"ds_read_b128 v[168:171], v246 offset:4096\n"
"v_mfma_f32_16x16x32_bf16 v[100:103], a[132:135], v[180:183], v[100:103]\n"
"buffer_load_dwordx4 v248, s[36:39], s69 offen lds\n"
"v_mfma_f32_16x16x32_bf16 v[120:123], a[136:139], v[180:183], v[120:123]\n"
"ds_read_b128 v[172:175], v246 offset:6144\n"
"s_add_u32 m0, s81, 73728\n"
"v_mfma_f32_16x16x32_bf16 v[140:143], a[140:143], v[180:183], v[140:143]\n"
"ds_read_b128 v[176:179], v246 offset:8192\n"
"v_mfma_f32_16x16x32_bf16 v[4:7], a[112:115], v[184:187], v[4:7]\n"
"buffer_load_dwordx4 v248, s[36:39], s70 offen lds\n"
"v_mfma_f32_16x16x32_bf16 v[24:27], a[116:119], v[184:187], v[24:27]\n"
"ds_read_b64_tr_b16 a[80:81], v245 offset:0\n"
"s_add_u32 m0, s81, 77824\n"
"v_mfma_f32_16x16x32_bf16 v[44:47], a[120:123], v[184:187], v[44:47]\n"
"ds_read_b64_tr_b16 a[82:83], v245 offset:256\n"
"v_mfma_f32_16x16x32_bf16 v[64:67], a[124:127], v[184:187], v[64:67]\n"
"buffer_load_dwordx4 v248, s[36:39], s71 offen lds\n"
"v_mfma_f32_16x16x32_bf16 v[84:87], a[128:131], v[184:187], v[84:87]\n"
"ds_read_b64_tr_b16 a[84:85], v245 offset:8\n"
"s_add_u32 m0, s81, 81920\n"
"v_mfma_f32_16x16x32_bf16 v[104:107], a[132:135], v[184:187], v[104:107]\n"
"ds_read_b64_tr_b16 a[86:87], v245 offset:264\n"
"v_mfma_f32_16x16x32_bf16 v[124:127], a[136:139], v[184:187], v[124:127]\n"
"buffer_load_dwordx4 v248, s[36:39], s72 offen lds\n"
"v_mfma_f32_16x16x32_bf16 v[144:147], a[140:143], v[184:187], v[144:147]\n"
"ds_read_b64_tr_b16 a[88:89], v245 offset:512\n"
"s_add_u32 m0, s80, 49152\n"
"v_mfma_f32_16x16x32_bf16 v[8:11], a[112:115], v[188:191], v[8:11]\n"
"ds_read_b64_tr_b16 a[90:91], v245 offset:768\n"
"s_add_u32 s17, s17, 128\n"
"v_mfma_f32_16x16x32_bf16 v[28:31], a[116:119], v[188:191], v[28:31]\n"
"buffer_load_dwordx4 v247, s[40:43], s16 offen lds\n"
"v_mfma_f32_16x16x32_bf16 v[48:51], a[120:123], v[188:191], v[48:51]\n"
"ds_read_b64_tr_b16 a[92:93], v245 offset:520\n"
"s_add_u32 m0, s80, 50048\n"
"v_mfma_f32_16x16x32_bf16 v[68:71], a[124:127], v[188:191], v[68:71]\n"
"ds_read_b64_tr_b16 a[94:95], v245 offset:776\n"
"v_mfma_f32_16x16x32_bf16 v[88:91], a[128:131], v[188:191], v[88:91]\n"
"buffer_load_dwordx4 v247, s[40:43], s16 offen offset:128 lds\n"
"v_mfma_f32_16x16x32_bf16 v[108:111], a[132:135], v[188:191], v[108:111]\n"
"ds_read_b64_tr_b16 a[96:97], v245 offset:1024\n"
"s_add_u32 m0, s80, 57088\n"
"v_mfma_f32_16x16x32_bf16 v[128:131], a[136:139], v[188:191], v[128:131]\n"
"ds_read_b64_tr_b16 a[98:99], v245 offset:1280\n"
"v_mfma_f32_16x16x32_bf16 v[148:151], a[140:143], v[188:191], v[148:151]\n"
"buffer_load_dwordx4 v247, s[40:43], s16 offen offset:256 lds\n"
"v_mfma_f32_16x16x32_bf16 v[12:15], a[112:115], v[192:195], v[12:15]\n"
"ds_read_b64_tr_b16 a[100:101], v245 offset:1032\n"
"s_add_u32 m0, s80, 57984\n"
"v_mfma_f32_16x16x32_bf16 v[32:35], a[116:119], v[192:195], v[32:35]\n"
"ds_read_b64_tr_b16 a[102:103], v245 offset:1288\n"
"v_mfma_f32_16x16x32_bf16 v[52:55], a[120:123], v[192:195], v[52:55]\n"
"buffer_load_dwordx4 v247, s[40:43], s16 offen offset:384 lds\n"
"v_mfma_f32_16x16x32_bf16 v[72:75], a[124:127], v[192:195], v[72:75]\n"
"ds_read_b64_tr_b16 a[104:105], v245 offset:1536\n"
"s_add_u32 s16, s16, 262144\n"
"v_mfma_f32_16x16x32_bf16 v[92:95], a[128:131], v[192:195], v[92:95]\n"
"ds_read_b64_tr_b16 a[106:107], v245 offset:1792\n"
"v_mfma_f32_16x16x32_bf16 v[112:115], a[132:135], v[192:195], v[112:115]\n"
"buffer_load_dwordx4 a[8:11], v243, s[44:47], s90 offen nt\n"
"v_mfma_f32_16x16x32_bf16 v[132:135], a[136:139], v[192:195], v[132:135]\n"
"ds_read_b64_tr_b16 a[108:109], v245 offset:1544\n"
"v_mfma_f32_16x16x32_bf16 v[152:155], a[140:143], v[192:195], v[152:155]\n"
"ds_read_b64_tr_b16 a[110:111], v245 offset:1800\n"
"v_mfma_f32_16x16x32_bf16 v[16:19], a[112:115], v[196:199], v[16:19]\n"
"buffer_load_dwordx4 a[28:31], v243, s[44:47], s90 offen offset:128 nt\n"
"v_mfma_f32_16x16x32_bf16 v[36:39], a[116:119], v[196:199], v[36:39]\n"
"s_cmp_eq_u32 s17, 1280\n"
"s_cselect_b32 s18, -1152, s24\n"
"s_cselect_b32 s17, 0, s17\n"
"v_mfma_f32_16x16x32_bf16 v[56:59], a[120:123], v[196:199], v[56:59]\n"
"v_add_u32 v248, s18, v248\n"
"v_mfma_f32_16x16x32_bf16 v[76:79], a[124:127], v[196:199], v[76:79]\n"
"buffer_load_dwordx4 a[48:51], v243, s[44:47], s93 offen nt\n"
"v_mfma_f32_16x16x32_bf16 v[96:99], a[128:131], v[196:199], v[96:99]\n"
"s_cmp_eq_u32 s16, 5242880\n"
"s_cselect_b32 s16, 0, s16\n"
"v_mfma_f32_16x16x32_bf16 v[116:119], a[132:135], v[196:199], v[116:119]\n"
"v_mfma_f32_16x16x32_bf16 v[136:139], a[136:139], v[196:199], v[136:139]\n"
"buffer_load_dwordx4 a[68:71], v243, s[44:47], s93 offen offset:128 nt\n"
"v_mfma_f32_16x16x32_bf16 v[156:159], a[140:143], v[196:199], v[156:159]\n"
"s_waitcnt vmcnt(21) lgkmcnt(0)\n"
"s_barrier\n"
"v_mfma_f32_16x16x32_bf16 v[0:3], a[80:83], v[160:163], v[0:3]\n"
"v_add_u32 v245, 147456, v249\n"
"s_add_u32 m0, s80, 86016\n"
"v_mfma_f32_16x16x32_bf16 v[20:23], a[84:87], v[160:163], v[20:23]\n"
"ds_read_b128 v[180:183], v246 offset:512\n"
"v_mfma_f32_16x16x32_bf16 v[40:43], a[88:91], v[160:163], v[40:43]\n"
"buffer_load_dwordx4 v247, s[40:43], s16 offen lds\n"
"v_mfma_f32_16x16x32_bf16 v[60:63], a[92:95], v[160:163], v[60:63]\n"
"ds_read_b128 v[184:187], v246 offset:2560\n"
"v_mfma_f32_16x16x32_bf16 v[80:83], a[96:99], v[160:163], v[80:83]\n"
"ds_read_b128 v[188:191], v246 offset:4608\n"
"s_add_u32 m0, s80, 86912\n"
"v_mfma_f32_16x16x32_bf16 v[100:103], a[100:103], v[160:163], v[100:103]\n"
"ds_read_b128 v[192:195], v246 offset:6656\n"
"v_mfma_f32_16x16x32_bf16 v[120:123], a[104:107], v[160:163], v[120:123]\n"
"buffer_load_dwordx4 v247, s[40:43], s16 offen offset:128 lds\n"
"v_mfma_f32_16x16x32_bf16 v[140:143], a[108:111], v[160:163], v[140:143]\n"
"ds_read_b128 v[196:199], v246 offset:8704\n"
"v_mfma_f32_16x16x32_bf16 v[4:7], a[80:83], v[164:167], v[4:7]\n"
"ds_read_b64_tr_b16 a[112:113], v245 offset:0\n"
"s_add_u32 m0, s80, 93952\n"
"v_mfma_f32_16x16x32_bf16 v[24:27], a[84:87], v[164:167], v[24:27]\n"
"ds_read_b64_tr_b16 a[114:115], v245 offset:256\n"
"v_mfma_f32_16x16x32_bf16 v[44:47], a[88:91], v[164:167], v[44:47]\n"
"buffer_load_dwordx4 v247, s[40:43], s16 offen offset:256 lds\n"
"v_mfma_f32_16x16x32_bf16 v[64:67], a[92:95], v[164:167], v[64:67]\n"
"ds_read_b64_tr_b16 a[116:117], v245 offset:8\n"
"v_mfma_f32_16x16x32_bf16 v[84:87], a[96:99], v[164:167], v[84:87]\n"
"ds_read_b64_tr_b16 a[118:119], v245 offset:264\n"
"s_add_u32 m0, s80, 94848\n"
"v_mfma_f32_16x16x32_bf16 v[104:107], a[100:103], v[164:167], v[104:107]\n"
"ds_read_b64_tr_b16 a[120:121], v245 offset:512\n"
"v_mfma_f32_16x16x32_bf16 v[124:127], a[104:107], v[164:167], v[124:127]\n"
"buffer_load_dwordx4 v247, s[40:43], s16 offen offset:384 lds\n"
"v_mfma_f32_16x16x32_bf16 v[144:147], a[108:111], v[164:167], v[144:147]\n"
"ds_read_b64_tr_b16 a[122:123], v245 offset:768\n"
"s_add_u32 s16, s16, 262144\n"
"v_mfma_f32_16x16x32_bf16 v[8:11], a[80:83], v[168:171], v[8:11]\n"
"ds_read_b64_tr_b16 a[124:125], v245 offset:520\n"
"v_mfma_f32_16x16x32_bf16 v[28:31], a[84:87], v[168:171], v[28:31]\n"
"ds_read_b64_tr_b16 a[126:127], v245 offset:776\n"
"v_mfma_f32_16x16x32_bf16 v[48:51], a[88:91], v[168:171], v[48:51]\n"
"ds_read_b64_tr_b16 a[128:129], v245 offset:1024\n"
"v_mfma_f32_16x16x32_bf16 v[68:71], a[92:95], v[168:171], v[68:71]\n"
"ds_read_b64_tr_b16 a[130:131], v245 offset:1280\n"
"v_mfma_f32_16x16x32_bf16 v[88:91], a[96:99], v[168:171], v[88:91]\n"
"ds_read_b64_tr_b16 a[132:133], v245 offset:1032\n"
"v_mfma_f32_16x16x32_bf16 v[108:111], a[100:103], v[168:171], v[108:111]\n"
"ds_read_b64_tr_b16 a[134:135], v245 offset:1288\n"
"v_mfma_f32_16x16x32_bf16 v[128:131], a[104:107], v[168:171], v[128:131]\n"
"ds_read_b64_tr_b16 a[136:137], v245 offset:1536\n"
"v_mfma_f32_16x16x32_bf16 v[148:151], a[108:111], v[168:171], v[148:151]\n"
"ds_read_b64_tr_b16 a[138:139], v245 offset:1792\n"
"v_mfma_f32_16x16x32_bf16 v[12:15], a[80:83], v[172:175], v[12:15]\n"
"ds_read_b64_tr_b16 a[140:141], v245 offset:1544\n"
"v_mfma_f32_16x16x32_bf16 v[32:35], a[84:87], v[172:175], v[32:35]\n"
"ds_read_b64_tr_b16 a[142:143], v245 offset:1800\n"
"v_mfma_f32_16x16x32_bf16 v[52:55], a[88:91], v[172:175], v[52:55]\n"
"s_cmp_eq_u32 s16, 5242880\n"
"s_cselect_b32 s16, 0, s16\n"
"v_mfma_f32_16x16x32_bf16 v[72:75], a[92:95], v[172:175], v[72:75]\n"
"v_mfma_f32_16x16x32_bf16 v[92:95], a[96:99], v[172:175], v[92:95]\n"
"v_mfma_f32_16x16x32_bf16 v[112:115], a[100:103], v[172:175], v[112:115]\n"
"v_mfma_f32_16x16x32_bf16 v[132:135], a[104:107], v[172:175], v[132:135]\n"
"v_mfma_f32_16x16x32_bf16 v[152:155], a[108:111], v[172:175], v[152:155]\n"
"v_mfma_f32_16x16x32_bf16 v[16:19], a[80:83], v[176:179], v[16:19]\n"
"v_mfma_f32_16x16x32_bf16 v[36:39], a[84:87], v[176:179], v[36:39]\n"
"v_mfma_f32_16x16x32_bf16 v[56:59], a[88:91], v[176:179], v[56:59]\n"
"v_mfma_f32_16x16x32_bf16 v[76:79], a[92:95], v[176:179], v[76:79]\n"
"v_mfma_f32_16x16x32_bf16 v[96:99], a[96:99], v[176:179], v[96:99]\n"
"v_mfma_f32_16x16x32_bf16 v[116:119], a[100:103], v[176:179], v[116:119]\n"
"v_mfma_f32_16x16x32_bf16 v[136:139], a[104:107], v[176:179], v[136:139]\n"
"v_mfma_f32_16x16x32_bf16 v[156:159], a[108:111], v[176:179], v[156:159]\n"
"s_waitcnt vmcnt(12) lgkmcnt(0)\n"
"s_barrier\n"
"v_mfma_f32_16x16x32_bf16 v[0:3], a[112:115], v[180:183], v[0:3]\n"
"v_add_u32 v245, 32768, v249\n"
"v_add_u32 v246, 65536, v250\n"
"v_mfma_f32_16x16x32_bf16 v[20:23], a[116:119], v[180:183], v[20:23]\n"
"ds_read_b128 v[160:163], v246 offset:0\n"
"s_add_u32 m0, s81, 102400\n"
"v_mfma_f32_16x16x32_bf16 v[40:43], a[120:123], v[180:183], v[40:43]\n"
"buffer_load_dwordx4 v248, s[36:39], 0 offen lds\n"
"v_mfma_f32_16x16x32_bf16 v[60:63], a[124:127], v[180:183], v[60:63]\n"
"ds_read_b128 v[164:167], v246 offset:2048\n"
"s_add_u32 m0, s81, 106496\n"
"v_mfma_f32_16x16x32_bf16 v[80:83], a[128:131], v[180:183], v[80:83]\n"
"ds_read_b128 v[168:171], v246 offset:4096\n"
"v_mfma_f32_16x16x32_bf16 v[100:103], a[132:135], v[180:183], v[100:103]\n"
"buffer_load_dwordx4 v248, s[36:39], s69 offen lds\n"
"v_mfma_f32_16x16x32_bf16 v[120:123], a[136:139], v[180:183], v[120:123]\n"
"ds_read_b128 v[172:175], v246 offset:6144\n"
"s_add_u32 m0, s81, 110592\n"
"v_mfma_f32_16x16x32_bf16 v[140:143], a[140:143], v[180:183], v[140:143]\n"
"ds_read_b128 v[176:179], v246 offset:8192\n"
"v_mfma_f32_16x16x32_bf16 v[4:7], a[112:115], v[184:187], v[4:7]\n"
"buffer_load_dwordx4 v248, s[36:39], s70 offen lds\n"
"v_mfma_f32_16x16x32_bf16 v[24:27], a[116:119], v[184:187], v[24:27]\n"
"ds_read_b64_tr_b16 a[80:81], v245 offset:0\n"
"s_add_u32 m0, s81, 114688\n"
"v_mfma_f32_16x16x32_bf16 v[44:47], a[120:123], v[184:187], v[44:47]\n"
"ds_read_b64_tr_b16 a[82:83], v245 offset:256\n"
"v_mfma_f32_16x16x32_bf16 v[64:67], a[124:127], v[184:187], v[64:67]\n"
"buffer_load_dwordx4 v248, s[36:39], s71 offen lds\n"
"v_mfma_f32_16x16x32_bf16 v[84:87], a[128:131], v[184:187], v[84:87]\n"
"ds_read_b64_tr_b16 a[84:85], v245 offset:8\n"
"s_add_u32 m0, s81, 118784\n"
"v_mfma_f32_16x16x32_bf16 v[104:107], a[132:135], v[184:187], v[104:107]\n"
"ds_read_b64_tr_b16 a[86:87], v245 offset:264\n"
"v_mfma_f32_16x16x32_bf16 v[124:127], a[136:139], v[184:187], v[124:127]\n"
"buffer_load_dwordx4 v248, s[36:39], s72 offen lds\n"
"v_mfma_f32_16x16x32_bf16 v[144:147], a[140:143], v[184:187], v[144:147]\n"
"ds_read_b64_tr_b16 a[88:89], v245 offset:512\n"
"s_add_u32 m0, s80, 147456\n"
"v_mfma_f32_16x16x32_bf16 v[8:11], a[112:115], v[188:191], v[8:11]\n"
"ds_read_b64_tr_b16 a[90:91], v245 offset:768\n"
"s_add_u32 s17, s17, 128\n"
"v_mfma_f32_16x16x32_bf16 v[28:31], a[116:119], v[188:191], v[28:31]\n"
"buffer_load_dwordx4 v247, s[40:43], s16 offen lds\n"
"v_mfma_f32_16x16x32_bf16 v[48:51], a[120:123], v[188:191], v[48:51]\n"
"ds_read_b64_tr_b16 a[92:93], v245 offset:520\n"
"s_add_u32 m0, s80, 148352\n"
"v_mfma_f32_16x16x32_bf16 v[68:71], a[124:127], v[188:191], v[68:71]\n"
"ds_read_b64_tr_b16 a[94:95], v245 offset:776\n"
"v_mfma_f32_16x16x32_bf16 v[88:91], a[128:131], v[188:191], v[88:91]\n"
"buffer_load_dwordx4 v247, s[40:43], s16 offen offset:128 lds\n"
"v_mfma_f32_16x16x32_bf16 v[108:111], a[132:135], v[188:191], v[108:111]\n"
"ds_read_b64_tr_b16 a[96:97], v245 offset:1024\n"
"s_add_u32 m0, s80, 155392\n"
"v_mfma_f32_16x16x32_bf16 v[128:131], a[136:139], v[188:191], v[128:131]\n"
"ds_read_b64_tr_b16 a[98:99], v245 offset:1280\n"
"v_mfma_f32_16x16x32_bf16 v[148:151], a[140:143], v[188:191], v[148:151]\n"
"buffer_load_dwordx4 v247, s[40:43], s16 offen offset:256 lds\n"
"v_mfma_f32_16x16x32_bf16 v[12:15], a[112:115], v[192:195], v[12:15]\n"
"ds_read_b64_tr_b16 a[100:101], v245 offset:1032\n"
"s_add_u32 m0, s80, 156288\n"
"v_mfma_f32_16x16x32_bf16 v[32:35], a[116:119], v[192:195], v[32:35]\n"
"ds_read_b64_tr_b16 a[102:103], v245 offset:1288\n"
"v_mfma_f32_16x16x32_bf16 v[52:55], a[120:123], v[192:195], v[52:55]\n"
"buffer_load_dwordx4 v247, s[40:43], s16 offen offset:384 lds\n"
"v_mfma_f32_16x16x32_bf16 v[72:75], a[124:127], v[192:195], v[72:75]\n"
"ds_read_b64_tr_b16 a[104:105], v245 offset:1536\n"
"s_add_u32 s16, s16, 262144\n"
"v_mfma_f32_16x16x32_bf16 v[92:95], a[128:131], v[192:195], v[92:95]\n"
"ds_read_b64_tr_b16 a[106:107], v245 offset:1792\n"
"v_mfma_f32_16x16x32_bf16 v[112:115], a[132:135], v[192:195], v[112:115]\n"
"buffer_load_dwordx4 a[4:7], v244, s[48:51], s90 offen nt\n"
"v_mfma_f32_16x16x32_bf16 v[132:135], a[136:139], v[192:195], v[132:135]\n"
"ds_read_b64_tr_b16 a[108:109], v245 offset:1544\n"
"v_mfma_f32_16x16x32_bf16 v[152:155], a[140:143], v[192:195], v[152:155]\n"
"ds_read_b64_tr_b16 a[110:111], v245 offset:1800\n"
"v_mfma_f32_16x16x32_bf16 v[16:19], a[112:115], v[196:199], v[16:19]\n"
"buffer_load_dwordx4 a[24:27], v244, s[48:51], s90 offen offset:128 nt\n"
"v_mfma_f32_16x16x32_bf16 v[36:39], a[116:119], v[196:199], v[36:39]\n"
"s_cmp_eq_u32 s17, 1280\n"
"s_cselect_b32 s18, -1152, s24\n"
"s_cselect_b32 s17, 0, s17\n"
"v_mfma_f32_16x16x32_bf16 v[56:59], a[120:123], v[196:199], v[56:59]\n"
"v_add_u32 v248, s18, v248\n"
"v_mfma_f32_16x16x32_bf16 v[76:79], a[124:127], v[196:199], v[76:79]\n"
"buffer_load_dwordx4 a[44:47], v244, s[48:51], s93 offen nt\n"
"v_mfma_f32_16x16x32_bf16 v[96:99], a[128:131], v[196:199], v[96:99]\n"
"s_cmp_eq_u32 s16, 5242880\n"
"s_cselect_b32 s16, 0, s16\n"
"v_mfma_f32_16x16x32_bf16 v[116:119], a[132:135], v[196:199], v[116:119]\n"
"v_mfma_f32_16x16x32_bf16 v[136:139], a[136:139], v[196:199], v[136:139]\n"
"buffer_load_dwordx4 a[64:67], v244, s[48:51], s93 offen offset:128 nt\n"
"v_mfma_f32_16x16x32_bf16 v[156:159], a[140:143], v[196:199], v[156:159]\n"
"s_waitcnt vmcnt(21) lgkmcnt(0)\n"
"s_barrier\n"
"v_mfma_f32_16x16x32_bf16 v[0:3], a[80:83], v[160:163], v[0:3]\n"
"v_add_u32 v245, 49152, v249\n"
"s_add_u32 m0, s80, 32768\n"
"v_mfma_f32_16x16x32_bf16 v[20:23], a[84:87], v[160:163], v[20:23]\n"
"ds_read_b128 v[180:183], v246 offset:512\n"
"v_mfma_f32_16x16x32_bf16 v[40:43], a[88:91], v[160:163], v[40:43]\n"
"buffer_load_dwordx4 v247, s[40:43], s16 offen lds\n"
"v_mfma_f32_16x16x32_bf16 v[60:63], a[92:95], v[160:163], v[60:63]\n"
"ds_read_b128 v[184:187], v246 offset:2560\n"
"v_mfma_f32_16x16x32_bf16 v[80:83], a[96:99], v[160:163], v[80:83]\n"
"ds_read_b128 v[188:191], v246 offset:4608\n"
"s_add_u32 m0, s80, 33664\n"
"v_mfma_f32_16x16x32_bf16 v[100:103], a[100:103], v[160:163], v[100:103]\n"
"ds_read_b128 v[192:195], v246 offset:6656\n"
"v_mfma_f32_16x16x32_bf16 v[120:123], a[104:107], v[160:163], v[120:123]\n"
"buffer_load_dwordx4 v247, s[40:43], s16 offen offset:128 lds\n"
"v_mfma_f32_16x16x32_bf16 v[140:143], a[108:111], v[160:163], v[140:143]\n"
"ds_read_b128 v[196:199], v246 offset:8704\n"
"v_mfma_f32_16x16x32_bf16 v[4:7], a[80:83], v[164:167], v[4:7]\n"
"ds_read_b64_tr_b16 a[112:113], v245 offset:0\n"
"s_add_u32 m0, s80, 40704\n"
"v_mfma_f32_16x16x32_bf16 v[24:27], a[84:87], v[164:167], v[24:27]\n"
"ds_read_b64_tr_b16 a[114:115], v245 offset:256\n"
"v_mfma_f32_16x16x32_bf16 v[44:47], a[88:91], v[164:167], v[44:47]\n"
"buffer_load_dwordx4 v247, s[40:43], s16 offen offset:256 lds\n"
"v_mfma_f32_16x16x32_bf16 v[64:67], a[92:95], v[164:167], v[64:67]\n"
"ds_read_b64_tr_b16 a[116:117], v245 offset:8\n"
"v_mfma_f32_16x16x32_bf16 v[84:87], a[96:99], v[164:167], v[84:87]\n"
"ds_read_b64_tr_b16 a[118:119], v245 offset:264\n"
"s_add_u32 m0, s80, 41600\n"
"v_mfma_f32_16x16x32_bf16 v[104:107], a[100:103], v[164:167], v[104:107]\n"
"ds_read_b64_tr_b16 a[120:121], v245 offset:512\n"
"v_mfma_f32_16x16x32_bf16 v[124:127], a[104:107], v[164:167], v[124:127]\n"
"buffer_load_dwordx4 v247, s[40:43], s16 offen offset:384 lds\n"
"v_mfma_f32_16x16x32_bf16 v[144:147], a[108:111], v[164:167], v[144:147]\n"
"ds_read_b64_tr_b16 a[122:123], v245 offset:768\n"
"s_add_u32 s16, s16, 262144\n"
"v_mfma_f32_16x16x32_bf16 v[8:11], a[80:83], v[168:171], v[8:11]\n"
"ds_read_b64_tr_b16 a[124:125], v245 offset:520\n"
"v_mfma_f32_16x16x32_bf16 v[28:31], a[84:87], v[168:171], v[28:31]\n"
"ds_read_b64_tr_b16 a[126:127], v245 offset:776\n"
"v_mfma_f32_16x16x32_bf16 v[48:51], a[88:91], v[168:171], v[48:51]\n"
"ds_read_b64_tr_b16 a[128:129], v245 offset:1024\n"
"v_mfma_f32_16x16x32_bf16 v[68:71], a[92:95], v[168:171], v[68:71]\n"
"ds_read_b64_tr_b16 a[130:131], v245 offset:1280\n"
"v_mfma_f32_16x16x32_bf16 v[88:91], a[96:99], v[168:171], v[88:91]\n"
"ds_read_b64_tr_b16 a[132:133], v245 offset:1032\n"
"v_mfma_f32_16x16x32_bf16 v[108:111], a[100:103], v[168:171], v[108:111]\n"
"ds_read_b64_tr_b16 a[134:135], v245 offset:1288\n"
"v_mfma_f32_16x16x32_bf16 v[128:131], a[104:107], v[168:171], v[128:131]\n"
"ds_read_b64_tr_b16 a[136:137], v245 offset:1536\n"
"v_mfma_f32_16x16x32_bf16 v[148:151], a[108:111], v[168:171], v[148:151]\n"
"ds_read_b64_tr_b16 a[138:139], v245 offset:1792\n"
"v_mfma_f32_16x16x32_bf16 v[12:15], a[80:83], v[172:175], v[12:15]\n"
"ds_read_b64_tr_b16 a[140:141], v245 offset:1544\n"
"v_mfma_f32_16x16x32_bf16 v[32:35], a[84:87], v[172:175], v[32:35]\n"
"ds_read_b64_tr_b16 a[142:143], v245 offset:1800\n"
"v_mfma_f32_16x16x32_bf16 v[52:55], a[88:91], v[172:175], v[52:55]\n"
"s_cmp_eq_u32 s16, 5242880\n"
"s_cselect_b32 s16, 0, s16\n"
"v_mfma_f32_16x16x32_bf16 v[72:75], a[92:95], v[172:175], v[72:75]\n"
"v_mfma_f32_16x16x32_bf16 v[92:95], a[96:99], v[172:175], v[92:95]\n"
"v_mfma_f32_16x16x32_bf16 v[112:115], a[100:103], v[172:175], v[112:115]\n"
"v_mfma_f32_16x16x32_bf16 v[132:135], a[104:107], v[172:175], v[132:135]\n"
"v_mfma_f32_16x16x32_bf16 v[152:155], a[108:111], v[172:175], v[152:155]\n"
"v_mfma_f32_16x16x32_bf16 v[16:19], a[80:83], v[176:179], v[16:19]\n"
"v_mfma_f32_16x16x32_bf16 v[36:39], a[84:87], v[176:179], v[36:39]\n"
"v_mfma_f32_16x16x32_bf16 v[56:59], a[88:91], v[176:179], v[56:59]\n"
"v_mfma_f32_16x16x32_bf16 v[76:79], a[92:95], v[176:179], v[76:79]\n"
"v_mfma_f32_16x16x32_bf16 v[96:99], a[96:99], v[176:179], v[96:99]\n"
"v_mfma_f32_16x16x32_bf16 v[116:119], a[100:103], v[176:179], v[116:119]\n"
"v_mfma_f32_16x16x32_bf16 v[136:139], a[104:107], v[176:179], v[136:139]\n"
"v_mfma_f32_16x16x32_bf16 v[156:159], a[108:111], v[176:179], v[156:159]\n"
"s_waitcnt vmcnt(12) lgkmcnt(0)\n"
"s_barrier\n"
"v_mfma_f32_16x16x32_bf16 v[0:3], a[112:115], v[180:183], v[0:3]\n"
"v_add_u32 v245, 86016, v249\n"
"v_add_u32 v246, 102400, v250\n"
"v_mfma_f32_16x16x32_bf16 v[20:23], a[116:119], v[180:183], v[20:23]\n"
"ds_read_b128 v[160:163], v246 offset:0\n"
"s_add_u32 m0, s81, 65536\n"
"v_mfma_f32_16x16x32_bf16 v[40:43], a[120:123], v[180:183], v[40:43]\n"
"buffer_load_dwordx4 v248, s[36:39], 0 offen lds\n"
"v_mfma_f32_16x16x32_bf16 v[60:63], a[124:127], v[180:183], v[60:63]\n"
"ds_read_b128 v[164:167], v246 offset:2048\n"
"s_add_u32 m0, s81, 69632\n"
"v_mfma_f32_16x16x32_bf16 v[80:83], a[128:131], v[180:183], v[80:83]\n"
"ds_read_b128 v[168:171], v246 offset:4096\n"
"v_mfma_f32_16x16x32_bf16 v[100:103], a[132:135], v[180:183], v[100:103]\n"
"buffer_load_dwordx4 v248, s[36:39], s69 offen lds\n"
"v_mfma_f32_16x16x32_bf16 v[120:123], a[136:139], v[180:183], v[120:123]\n"
"ds_read_b128 v[172:175], v246 offset:6144\n"
"s_add_u32 m0, s81, 73728\n"
"v_mfma_f32_16x16x32_bf16 v[140:143], a[140:143], v[180:183], v[140:143]\n"
"ds_read_b128 v[176:179], v246 offset:8192\n"
"v_mfma_f32_16x16x32_bf16 v[4:7], a[112:115], v[184:187], v[4:7]\n"
"buffer_load_dwordx4 v248, s[36:39], s70 offen lds\n"
"v_mfma_f32_16x16x32_bf16 v[24:27], a[116:119], v[184:187], v[24:27]\n"
"ds_read_b64_tr_b16 a[80:81], v245 offset:0\n"
"s_add_u32 m0, s81, 77824\n"
"v_mfma_f32_16x16x32_bf16 v[44:47], a[120:123], v[184:187], v[44:47]\n"
"ds_read_b64_tr_b16 a[82:83], v245 offset:256\n"
"v_mfma_f32_16x16x32_bf16 v[64:67], a[124:127], v[184:187], v[64:67]\n"
"buffer_load_dwordx4 v248, s[36:39], s71 offen lds\n"
"v_mfma_f32_16x16x32_bf16 v[84:87], a[128:131], v[184:187], v[84:87]\n"
"ds_read_b64_tr_b16 a[84:85], v245 offset:8\n"
"s_add_u32 m0, s81, 81920\n"
"v_mfma_f32_16x16x32_bf16 v[104:107], a[132:135], v[184:187], v[104:107]\n"
"ds_read_b64_tr_b16 a[86:87], v245 offset:264\n"
"v_mfma_f32_16x16x32_bf16 v[124:127], a[136:139], v[184:187], v[124:127]\n"
"buffer_load_dwordx4 v248, s[36:39], s72 offen lds\n"
"v_mfma_f32_16x16x32_bf16 v[144:147], a[140:143], v[184:187], v[144:147]\n"
"ds_read_b64_tr_b16 a[88:89], v245 offset:512\n"
"s_add_u32 m0, s80, 49152\n"
"v_mfma_f32_16x16x32_bf16 v[8:11], a[112:115], v[188:191], v[8:11]\n"
"ds_read_b64_tr_b16 a[90:91], v245 offset:768\n"
"s_add_u32 s17, s17, 128\n"
"v_mfma_f32_16x16x32_bf16 v[28:31], a[116:119], v[188:191], v[28:31]\n"
"buffer_load_dwordx4 v247, s[40:43], s16 offen lds\n"
"v_mfma_f32_16x16x32_bf16 v[48:51], a[120:123], v[188:191], v[48:51]\n"
"ds_read_b64_tr_b16 a[92:93], v245 offset:520\n"
"s_add_u32 m0, s80, 50048\n"
"v_mfma_f32_16x16x32_bf16 v[68:71], a[124:127], v[188:191], v[68:71]\n"
"ds_read_b64_tr_b16 a[94:95], v245 offset:776\n"
"v_mfma_f32_16x16x32_bf16 v[88:91], a[128:131], v[188:191], v[88:91]\n"
"buffer_load_dwordx4 v247, s[40:43], s16 offen offset:128 lds\n"
"v_mfma_f32_16x16x32_bf16 v[108:111], a[132:135], v[188:191], v[108:111]\n"
"ds_read_b64_tr_b16 a[96:97], v245 offset:1024\n"
"s_add_u32 m0, s80, 57088\n"
"v_mfma_f32_16x16x32_bf16 v[128:131], a[136:139], v[188:191], v[128:131]\n"
"ds_read_b64_tr_b16 a[98:99], v245 offset:1280\n"
"v_mfma_f32_16x16x32_bf16 v[148:151], a[140:143], v[188:191], v[148:151]\n"
"buffer_load_dwordx4 v247, s[40:43], s16 offen offset:256 lds\n"
"v_mfma_f32_16x16x32_bf16 v[12:15], a[112:115], v[192:195], v[12:15]\n"
"ds_read_b64_tr_b16 a[100:101], v245 offset:1032\n"
"s_add_u32 m0, s80, 57984\n"
"v_mfma_f32_16x16x32_bf16 v[32:35], a[116:119], v[192:195], v[32:35]\n"
"ds_read_b64_tr_b16 a[102:103], v245 offset:1288\n"
"v_mfma_f32_16x16x32_bf16 v[52:55], a[120:123], v[192:195], v[52:55]\n"
"buffer_load_dwordx4 v247, s[40:43], s16 offen offset:384 lds\n"
"v_mfma_f32_16x16x32_bf16 v[72:75], a[124:127], v[192:195], v[72:75]\n"
"ds_read_b64_tr_b16 a[104:105], v245 offset:1536\n"
"s_add_u32 s16, s16, 262144\n"
"v_mfma_f32_16x16x32_bf16 v[92:95], a[128:131], v[192:195], v[92:95]\n"
"ds_read_b64_tr_b16 a[106:107], v245 offset:1792\n"
"v_mfma_f32_16x16x32_bf16 v[112:115], a[132:135], v[192:195], v[112:115]\n"
"buffer_load_dwordx4 a[12:15], v244, s[48:51], s94 offen nt\n"
"v_mfma_f32_16x16x32_bf16 v[132:135], a[136:139], v[192:195], v[132:135]\n"
"ds_read_b64_tr_b16 a[108:109], v245 offset:1544\n"
"v_mfma_f32_16x16x32_bf16 v[152:155], a[140:143], v[192:195], v[152:155]\n"
"ds_read_b64_tr_b16 a[110:111], v245 offset:1800\n"
"v_mfma_f32_16x16x32_bf16 v[16:19], a[112:115], v[196:199], v[16:19]\n"
"buffer_load_dwordx4 a[32:35], v244, s[48:51], s94 offen offset:128 nt\n"
"v_mfma_f32_16x16x32_bf16 v[36:39], a[116:119], v[196:199], v[36:39]\n"
"s_cmp_eq_u32 s17, 1280\n"
"s_cselect_b32 s18, -1152, s24\n"
"s_cselect_b32 s17, 0, s17\n"
"v_mfma_f32_16x16x32_bf16 v[56:59], a[120:123], v[196:199], v[56:59]\n"
"v_add_u32 v248, s18, v248\n"
"v_mfma_f32_16x16x32_bf16 v[76:79], a[124:127], v[196:199], v[76:79]\n"
"buffer_load_dwordx4 a[52:55], v244, s[48:51], s95 offen nt\n"
"v_mfma_f32_16x16x32_bf16 v[96:99], a[128:131], v[196:199], v[96:99]\n"
"s_cmp_eq_u32 s16, 5242880\n"
"s_cselect_b32 s16, 0, s16\n"
"v_mfma_f32_16x16x32_bf16 v[116:119], a[132:135], v[196:199], v[116:119]\n"
"v_mfma_f32_16x16x32_bf16 v[136:139], a[136:139], v[196:199], v[136:139]\n"
"buffer_load_dwordx4 a[72:75], v244, s[48:51], s95 offen offset:128 nt\n"
"v_mfma_f32_16x16x32_bf16 v[156:159], a[140:143], v[196:199], v[156:159]\n"
"s_waitcnt vmcnt(21) lgkmcnt(0)\n"
"s_barrier\n"
"v_mfma_f32_16x16x32_bf16 v[0:3], a[80:83], v[160:163], v[0:3]\n"
"v_add_u32 v245, 147456, v249\n"
"s_add_u32 m0, s80, 86016\n"
"v_mfma_f32_16x16x32_bf16 v[20:23], a[84:87], v[160:163], v[20:23]\n"
"ds_read_b128 v[180:183], v246 offset:512\n"
"v_mfma_f32_16x16x32_bf16 v[40:43], a[88:91], v[160:163], v[40:43]\n"
"buffer_load_dwordx4 v247, s[40:43], s16 offen lds\n"
"v_mfma_f32_16x16x32_bf16 v[60:63], a[92:95], v[160:163], v[60:63]\n"
"ds_read_b128 v[184:187], v246 offset:2560\n"
"v_mfma_f32_16x16x32_bf16 v[80:83], a[96:99], v[160:163], v[80:83]\n"
"ds_read_b128 v[188:191], v246 offset:4608\n"
"s_add_u32 m0, s80, 86912\n"
"v_mfma_f32_16x16x32_bf16 v[100:103], a[100:103], v[160:163], v[100:103]\n"
"ds_read_b128 v[192:195], v246 offset:6656\n"
"v_mfma_f32_16x16x32_bf16 v[120:123], a[104:107], v[160:163], v[120:123]\n"
"buffer_load_dwordx4 v247, s[40:43], s16 offen offset:128 lds\n"
"v_mfma_f32_16x16x32_bf16 v[140:143], a[108:111], v[160:163], v[140:143]\n"
"ds_read_b128 v[196:199], v246 offset:8704\n"
"v_mfma_f32_16x16x32_bf16 v[4:7], a[80:83], v[164:167], v[4:7]\n"
"ds_read_b64_tr_b16 a[112:113], v245 offset:0\n"
"s_add_u32 m0, s80, 93952\n"
"v_mfma_f32_16x16x32_bf16 v[24:27], a[84:87], v[164:167], v[24:27]\n"
"ds_read_b64_tr_b16 a[114:115], v245 offset:256\n"
"v_mfma_f32_16x16x32_bf16 v[44:47], a[88:91], v[164:167], v[44:47]\n"
"buffer_load_dwordx4 v247, s[40:43], s16 offen offset:256 lds\n"
"v_mfma_f32_16x16x32_bf16 v[64:67], a[92:95], v[164:167], v[64:67]\n"
"ds_read_b64_tr_b16 a[116:117], v245 offset:8\n"
"v_mfma_f32_16x16x32_bf16 v[84:87], a[96:99], v[164:167], v[84:87]\n"
"ds_read_b64_tr_b16 a[118:119], v245 offset:264\n"
"s_add_u32 m0, s80, 94848\n"
"v_mfma_f32_16x16x32_bf16 v[104:107], a[100:103], v[164:167], v[104:107]\n"
"ds_read_b64_tr_b16 a[120:121], v245 offset:512\n"
"v_mfma_f32_16x16x32_bf16 v[124:127], a[104:107], v[164:167], v[124:127]\n"
"buffer_load_dwordx4 v247, s[40:43], s16 offen offset:384 lds\n"
"v_mfma_f32_16x16x32_bf16 v[144:147], a[108:111], v[164:167], v[144:147]\n"
"ds_read_b64_tr_b16 a[122:123], v245 offset:768\n"
"s_add_u32 s16, s16, 262144\n"
"v_mfma_f32_16x16x32_bf16 v[8:11], a[80:83], v[168:171], v[8:11]\n"
"ds_read_b64_tr_b16 a[124:125], v245 offset:520\n"
"v_mfma_f32_16x16x32_bf16 v[28:31], a[84:87], v[168:171], v[28:31]\n"
"ds_read_b64_tr_b16 a[126:127], v245 offset:776\n"
"v_mfma_f32_16x16x32_bf16 v[48:51], a[88:91], v[168:171], v[48:51]\n"
"ds_read_b64_tr_b16 a[128:129], v245 offset:1024\n"
"v_mfma_f32_16x16x32_bf16 v[68:71], a[92:95], v[168:171], v[68:71]\n"
"ds_read_b64_tr_b16 a[130:131], v245 offset:1280\n"
"v_mfma_f32_16x16x32_bf16 v[88:91], a[96:99], v[168:171], v[88:91]\n"
"ds_read_b64_tr_b16 a[132:133], v245 offset:1032\n"
"v_mfma_f32_16x16x32_bf16 v[108:111], a[100:103], v[168:171], v[108:111]\n"
"ds_read_b64_tr_b16 a[134:135], v245 offset:1288\n"
"v_mfma_f32_16x16x32_bf16 v[128:131], a[104:107], v[168:171], v[128:131]\n"
"ds_read_b64_tr_b16 a[136:137], v245 offset:1536\n"
"v_mfma_f32_16x16x32_bf16 v[148:151], a[108:111], v[168:171], v[148:151]\n"
"ds_read_b64_tr_b16 a[138:139], v245 offset:1792\n"
"v_mfma_f32_16x16x32_bf16 v[12:15], a[80:83], v[172:175], v[12:15]\n"
"ds_read_b64_tr_b16 a[140:141], v245 offset:1544\n"
"v_mfma_f32_16x16x32_bf16 v[32:35], a[84:87], v[172:175], v[32:35]\n"
"ds_read_b64_tr_b16 a[142:143], v245 offset:1800\n"
"v_mfma_f32_16x16x32_bf16 v[52:55], a[88:91], v[172:175], v[52:55]\n"
"s_cmp_eq_u32 s16, 5242880\n"
"s_cselect_b32 s16, 0, s16\n"
"v_mfma_f32_16x16x32_bf16 v[72:75], a[92:95], v[172:175], v[72:75]\n"
"v_mfma_f32_16x16x32_bf16 v[92:95], a[96:99], v[172:175], v[92:95]\n"
"v_mfma_f32_16x16x32_bf16 v[112:115], a[100:103], v[172:175], v[112:115]\n"
"v_mfma_f32_16x16x32_bf16 v[132:135], a[104:107], v[172:175], v[132:135]\n"
"v_mfma_f32_16x16x32_bf16 v[152:155], a[108:111], v[172:175], v[152:155]\n"
"v_mfma_f32_16x16x32_bf16 v[16:19], a[80:83], v[176:179], v[16:19]\n"
"v_mfma_f32_16x16x32_bf16 v[36:39], a[84:87], v[176:179], v[36:39]\n"
"v_mfma_f32_16x16x32_bf16 v[56:59], a[88:91], v[176:179], v[56:59]\n"
"v_mfma_f32_16x16x32_bf16 v[76:79], a[92:95], v[176:179], v[76:79]\n"
"v_mfma_f32_16x16x32_bf16 v[96:99], a[96:99], v[176:179], v[96:99]\n"
"v_mfma_f32_16x16x32_bf16 v[116:119], a[100:103], v[176:179], v[116:119]\n"
"v_mfma_f32_16x16x32_bf16 v[136:139], a[104:107], v[176:179], v[136:139]\n"
"v_mfma_f32_16x16x32_bf16 v[156:159], a[108:111], v[176:179], v[156:159]\n"
"s_waitcnt vmcnt(12) lgkmcnt(0)\n"
"s_barrier\n"
"v_mfma_f32_16x16x32_bf16 v[0:3], a[112:115], v[180:183], v[0:3]\n"
"v_add_u32 v245, 32768, v249\n"
"v_add_u32 v246, 65536, v250\n"
"v_mfma_f32_16x16x32_bf16 v[20:23], a[116:119], v[180:183], v[20:23]\n"
"ds_read_b128 v[160:163], v246 offset:0\n"
"s_add_u32 m0, s81, 102400\n"
"v_mfma_f32_16x16x32_bf16 v[40:43], a[120:123], v[180:183], v[40:43]\n"
"buffer_load_dwordx4 v248, s[36:39], 0 offen lds\n"
"v_mfma_f32_16x16x32_bf16 v[60:63], a[124:127], v[180:183], v[60:63]\n"
"ds_read_b128 v[164:167], v246 offset:2048\n"
"s_add_u32 m0, s81, 106496\n"
"v_mfma_f32_16x16x32_bf16 v[80:83], a[128:131], v[180:183], v[80:83]\n"
"ds_read_b128 v[168:171], v246 offset:4096\n"
"v_mfma_f32_16x16x32_bf16 v[100:103], a[132:135], v[180:183], v[100:103]\n"
"buffer_load_dwordx4 v248, s[36:39], s69 offen lds\n"
"v_mfma_f32_16x16x32_bf16 v[120:123], a[136:139], v[180:183], v[120:123]\n"
"ds_read_b128 v[172:175], v246 offset:6144\n"
"s_add_u32 m0, s81, 110592\n"
"v_mfma_f32_16x16x32_bf16 v[140:143], a[140:143], v[180:183], v[140:143]\n"
"ds_read_b128 v[176:179], v246 offset:8192\n"
"v_mfma_f32_16x16x32_bf16 v[4:7], a[112:115], v[184:187], v[4:7]\n"
"buffer_load_dwordx4 v248, s[36:39], s70 offen lds\n"
"v_mfma_f32_16x16x32_bf16 v[24:27], a[116:119], v[184:187], v[24:27]\n"
"ds_read_b64_tr_b16 a[80:81], v245 offset:0\n"
"s_add_u32 m0, s81, 114688\n"
"v_mfma_f32_16x16x32_bf16 v[44:47], a[120:123], v[184:187], v[44:47]\n"
"ds_read_b64_tr_b16 a[82:83], v245 offset:256\n"
"v_mfma_f32_16x16x32_bf16 v[64:67], a[124:127], v[184:187], v[64:67]\n"
"buffer_load_dwordx4 v248, s[36:39], s71 offen lds\n"
"v_mfma_f32_16x16x32_bf16 v[84:87], a[128:131], v[184:187], v[84:87]\n"
"ds_read_b64_tr_b16 a[84:85], v245 offset:8\n"
"s_add_u32 m0, s81, 118784\n"
"v_mfma_f32_16x16x32_bf16 v[104:107], a[132:135], v[184:187], v[104:107]\n"
"ds_read_b64_tr_b16 a[86:87], v245 offset:264\n"
"v_mfma_f32_16x16x32_bf16 v[124:127], a[136:139], v[184:187], v[124:127]\n"
"buffer_load_dwordx4 v248, s[36:39], s72 offen lds\n"
"v_mfma_f32_16x16x32_bf16 v[144:147], a[140:143], v[184:187], v[144:147]\n"
"ds_read_b64_tr_b16 a[88:89], v245 offset:512\n"
"s_add_u32 m0, s80, 147456\n"
"v_mfma_f32_16x16x32_bf16 v[8:11], a[112:115], v[188:191], v[8:11]\n"
"ds_read_b64_tr_b16 a[90:91], v245 offset:768\n"
"s_add_u32 s17, s17, 128\n"
"v_mfma_f32_16x16x32_bf16 v[28:31], a[116:119], v[188:191], v[28:31]\n"
"buffer_load_dwordx4 v247, s[40:43], s16 offen lds\n"
"v_mfma_f32_16x16x32_bf16 v[48:51], a[120:123], v[188:191], v[48:51]\n"
"ds_read_b64_tr_b16 a[92:93], v245 offset:520\n"
"s_add_u32 m0, s80, 148352\n"
"v_mfma_f32_16x16x32_bf16 v[68:71], a[124:127], v[188:191], v[68:71]\n"
"ds_read_b64_tr_b16 a[94:95], v245 offset:776\n"
"v_mfma_f32_16x16x32_bf16 v[88:91], a[128:131], v[188:191], v[88:91]\n"
"buffer_load_dwordx4 v247, s[40:43], s16 offen offset:128 lds\n"
"v_mfma_f32_16x16x32_bf16 v[108:111], a[132:135], v[188:191], v[108:111]\n"
"ds_read_b64_tr_b16 a[96:97], v245 offset:1024\n"
"s_add_u32 m0, s80, 155392\n"
"v_mfma_f32_16x16x32_bf16 v[128:131], a[136:139], v[188:191], v[128:131]\n"
"ds_read_b64_tr_b16 a[98:99], v245 offset:1280\n"
"v_mfma_f32_16x16x32_bf16 v[148:151], a[140:143], v[188:191], v[148:151]\n"
"buffer_load_dwordx4 v247, s[40:43], s16 offen offset:256 lds\n"
"v_mfma_f32_16x16x32_bf16 v[12:15], a[112:115], v[192:195], v[12:15]\n"
"ds_read_b64_tr_b16 a[100:101], v245 offset:1032\n"
"s_add_u32 m0, s80, 156288\n"
"v_mfma_f32_16x16x32_bf16 v[32:35], a[116:119], v[192:195], v[32:35]\n"
"ds_read_b64_tr_b16 a[102:103], v245 offset:1288\n"
"v_mfma_f32_16x16x32_bf16 v[52:55], a[120:123], v[192:195], v[52:55]\n"
"buffer_load_dwordx4 v247, s[40:43], s16 offen offset:384 lds\n"
"v_mfma_f32_16x16x32_bf16 v[72:75], a[124:127], v[192:195], v[72:75]\n"
"ds_read_b64_tr_b16 a[104:105], v245 offset:1536\n"
"s_add_u32 m0, s80, 0\n"
"v_mfma_f32_16x16x32_bf16 v[92:95], a[128:131], v[192:195], v[92:95]\n"
"ds_read_b64_tr_b16 a[106:107], v245 offset:1792\n"
"s_add_u32 s16, s16, 262144\n"
"v_mfma_f32_16x16x32_bf16 v[112:115], a[132:135], v[192:195], v[112:115]\n"
"buffer_load_dwordx4 v243, s[44:47], 0 offen nt lds\n"
"v_mfma_f32_16x16x32_bf16 v[132:135], a[136:139], v[192:195], v[132:135]\n"
"ds_read_b64_tr_b16 a[108:109], v245 offset:1544\n"
"s_add_u32 m0, s80, 896\n"
"v_mfma_f32_16x16x32_bf16 v[152:155], a[140:143], v[192:195], v[152:155]\n"
"ds_read_b64_tr_b16 a[110:111], v245 offset:1800\n"
"v_mfma_f32_16x16x32_bf16 v[16:19], a[112:115], v[196:199], v[16:19]\n"
"buffer_load_dwordx4 v243, s[44:47], 0 offen offset:128 nt lds\n"
"v_mfma_f32_16x16x32_bf16 v[36:39], a[116:119], v[196:199], v[36:39]\n"
"s_add_u32 m0, s80, 8192\n"
"v_mfma_f32_16x16x32_bf16 v[56:59], a[120:123], v[196:199], v[56:59]\n"
"s_cmp_eq_u32 s17, 1280\n"
"s_cselect_b32 s18, -1152, s24\n"
"s_cselect_b32 s17, 0, s17\n"
"v_mfma_f32_16x16x32_bf16 v[76:79], a[124:127], v[196:199], v[76:79]\n"
"buffer_load_dwordx4 v243, s[44:47], s68 offen nt lds\n"
"v_mfma_f32_16x16x32_bf16 v[96:99], a[128:131], v[196:199], v[96:99]\n"
"s_add_u32 m0, s80, 9088\n"
"v_add_u32 v248, s18, v248\n"
"v_mfma_f32_16x16x32_bf16 v[116:119], a[132:135], v[196:199], v[116:119]\n"
"s_cmp_eq_u32 s16, 5242880\n"
"s_cselect_b32 s16, 0, s16\n"
"v_mfma_f32_16x16x32_bf16 v[136:139], a[136:139], v[196:199], v[136:139]\n"
"buffer_load_dwordx4 v243, s[44:47], s68 offen offset:128 nt lds\n"
"v_mfma_f32_16x16x32_bf16 v[156:159], a[140:143], v[196:199], v[156:159]\n"
"s_waitcnt vmcnt(21) lgkmcnt(0)\n"
"s_barrier\n"
"v_mfma_f32_16x16x32_bf16 v[0:3], a[80:83], v[160:163], v[0:3]\n"
"v_add_u32 v245, 49152, v249\n"
"s_add_u32 m0, s80, 32768\n"
"v_mfma_f32_16x16x32_bf16 v[20:23], a[84:87], v[160:163], v[20:23]\n"
"ds_read_b128 v[180:183], v246 offset:512\n"
"v_mfma_f32_16x16x32_bf16 v[40:43], a[88:91], v[160:163], v[40:43]\n"
"buffer_load_dwordx4 v247, s[40:43], s16 offen lds\n"
"v_mfma_f32_16x16x32_bf16 v[60:63], a[92:95], v[160:163], v[60:63]\n"
"ds_read_b128 v[184:187], v246 offset:2560\n"
"v_mfma_f32_16x16x32_bf16 v[80:83], a[96:99], v[160:163], v[80:83]\n"
"ds_read_b128 v[188:191], v246 offset:4608\n"
"s_add_u32 m0, s80, 33664\n"
"v_mfma_f32_16x16x32_bf16 v[100:103], a[100:103], v[160:163], v[100:103]\n"
"ds_read_b128 v[192:195], v246 offset:6656\n"
"v_mfma_f32_16x16x32_bf16 v[120:123], a[104:107], v[160:163], v[120:123]\n"
"buffer_load_dwordx4 v247, s[40:43], s16 offen offset:128 lds\n"
"v_mfma_f32_16x16x32_bf16 v[140:143], a[108:111], v[160:163], v[140:143]\n"
"ds_read_b128 v[196:199], v246 offset:8704\n"
"v_mfma_f32_16x16x32_bf16 v[4:7], a[80:83], v[164:167], v[4:7]\n"
"ds_read_b64_tr_b16 a[112:113], v245 offset:0\n"
"s_add_u32 m0, s80, 40704\n"
"v_mfma_f32_16x16x32_bf16 v[24:27], a[84:87], v[164:167], v[24:27]\n"
"ds_read_b64_tr_b16 a[114:115], v245 offset:256\n"
"v_mfma_f32_16x16x32_bf16 v[44:47], a[88:91], v[164:167], v[44:47]\n"
"buffer_load_dwordx4 v247, s[40:43], s16 offen offset:256 lds\n"
"v_mfma_f32_16x16x32_bf16 v[64:67], a[92:95], v[164:167], v[64:67]\n"
"ds_read_b64_tr_b16 a[116:117], v245 offset:8\n"
"v_mfma_f32_16x16x32_bf16 v[84:87], a[96:99], v[164:167], v[84:87]\n"
"ds_read_b64_tr_b16 a[118:119], v245 offset:264\n"
"s_add_u32 m0, s80, 41600\n"
"v_mfma_f32_16x16x32_bf16 v[104:107], a[100:103], v[164:167], v[104:107]\n"
"ds_read_b64_tr_b16 a[120:121], v245 offset:512\n"
"v_mfma_f32_16x16x32_bf16 v[124:127], a[104:107], v[164:167], v[124:127]\n"
"buffer_load_dwordx4 v247, s[40:43], s16 offen offset:384 lds\n"
"v_mfma_f32_16x16x32_bf16 v[144:147], a[108:111], v[164:167], v[144:147]\n"
"ds_read_b64_tr_b16 a[122:123], v245 offset:768\n"
"s_add_u32 s16, s16, 262144\n"
"v_mfma_f32_16x16x32_bf16 v[8:11], a[80:83], v[168:171], v[8:11]\n"
"ds_read_b64_tr_b16 a[124:125], v245 offset:520\n"
"v_mfma_f32_16x16x32_bf16 v[28:31], a[84:87], v[168:171], v[28:31]\n"
"ds_read_b64_tr_b16 a[126:127], v245 offset:776\n"
"v_mfma_f32_16x16x32_bf16 v[48:51], a[88:91], v[168:171], v[48:51]\n"
"ds_read_b64_tr_b16 a[128:129], v245 offset:1024\n"
"v_mfma_f32_16x16x32_bf16 v[68:71], a[92:95], v[168:171], v[68:71]\n"
"ds_read_b64_tr_b16 a[130:131], v245 offset:1280\n"
"v_mfma_f32_16x16x32_bf16 v[88:91], a[96:99], v[168:171], v[88:91]\n"
"ds_read_b64_tr_b16 a[132:133], v245 offset:1032\n"
"v_mfma_f32_16x16x32_bf16 v[108:111], a[100:103], v[168:171], v[108:111]\n"
"ds_read_b64_tr_b16 a[134:135], v245 offset:1288\n"
"v_mfma_f32_16x16x32_bf16 v[128:131], a[104:107], v[168:171], v[128:131]\n"
"ds_read_b64_tr_b16 a[136:137], v245 offset:1536\n"
"v_mfma_f32_16x16x32_bf16 v[148:151], a[108:111], v[168:171], v[148:151]\n"
"ds_read_b64_tr_b16 a[138:139], v245 offset:1792\n"
"v_mfma_f32_16x16x32_bf16 v[12:15], a[80:83], v[172:175], v[12:15]\n"
"ds_read_b64_tr_b16 a[140:141], v245 offset:1544\n"
"v_mfma_f32_16x16x32_bf16 v[32:35], a[84:87], v[172:175], v[32:35]\n"
"ds_read_b64_tr_b16 a[142:143], v245 offset:1800\n"
"v_mfma_f32_16x16x32_bf16 v[52:55], a[88:91], v[172:175], v[52:55]\n"
"s_cmp_eq_u32 s16, 5242880\n"
"s_cselect_b32 s16, 0, s16\n"
"v_mfma_f32_16x16x32_bf16 v[72:75], a[92:95], v[172:175], v[72:75]\n"
"v_mfma_f32_16x16x32_bf16 v[92:95], a[96:99], v[172:175], v[92:95]\n"
"v_mfma_f32_16x16x32_bf16 v[112:115], a[100:103], v[172:175], v[112:115]\n"
"v_mfma_f32_16x16x32_bf16 v[132:135], a[104:107], v[172:175], v[132:135]\n"
"v_mfma_f32_16x16x32_bf16 v[152:155], a[108:111], v[172:175], v[152:155]\n"
"v_mfma_f32_16x16x32_bf16 v[16:19], a[80:83], v[176:179], v[16:19]\n"
"v_mfma_f32_16x16x32_bf16 v[36:39], a[84:87], v[176:179], v[36:39]\n"
"v_mfma_f32_16x16x32_bf16 v[56:59], a[88:91], v[176:179], v[56:59]\n"
"v_mfma_f32_16x16x32_bf16 v[76:79], a[92:95], v[176:179], v[76:79]\n"
"v_mfma_f32_16x16x32_bf16 v[96:99], a[96:99], v[176:179], v[96:99]\n"
"v_mfma_f32_16x16x32_bf16 v[116:119], a[100:103], v[176:179], v[116:119]\n"
"v_mfma_f32_16x16x32_bf16 v[136:139], a[104:107], v[176:179], v[136:139]\n"
"v_mfma_f32_16x16x32_bf16 v[156:159], a[108:111], v[176:179], v[156:159]\n"
"s_waitcnt vmcnt(12) lgkmcnt(0)\n"
"s_barrier\n"
"v_mfma_f32_16x16x32_bf16 v[0:3], a[112:115], v[180:183], v[0:3]\n"
"v_add_u32 v245, 86016, v249\n"
"v_add_u32 v246, 102400, v250\n"
"v_mfma_f32_16x16x32_bf16 v[20:23], a[116:119], v[180:183], v[20:23]\n"
"ds_read_b128 v[160:163], v246 offset:0\n"
"s_add_u32 m0, s81, 65536\n"
"v_mfma_f32_16x16x32_bf16 v[40:43], a[120:123], v[180:183], v[40:43]\n"
"buffer_load_dwordx4 v248, s[36:39], 0 offen lds\n"
"v_mfma_f32_16x16x32_bf16 v[60:63], a[124:127], v[180:183], v[60:63]\n"
"ds_read_b128 v[164:167], v246 offset:2048\n"
"s_add_u32 m0, s81, 69632\n"
"v_mfma_f32_16x16x32_bf16 v[80:83], a[128:131], v[180:183], v[80:83]\n"
"ds_read_b128 v[168:171], v246 offset:4096\n"
"v_mfma_f32_16x16x32_bf16 v[100:103], a[132:135], v[180:183], v[100:103]\n"
"buffer_load_dwordx4 v248, s[36:39], s69 offen lds\n"
"v_mfma_f32_16x16x32_bf16 v[120:123], a[136:139], v[180:183], v[120:123]\n"
"ds_read_b128 v[172:175], v246 offset:6144\n"
"s_add_u32 m0, s81, 73728\n"
"v_mfma_f32_16x16x32_bf16 v[140:143], a[140:143], v[180:183], v[140:143]\n"
"ds_read_b128 v[176:179], v246 offset:8192\n"
"v_mfma_f32_16x16x32_bf16 v[4:7], a[112:115], v[184:187], v[4:7]\n"
"buffer_load_dwordx4 v248, s[36:39], s70 offen lds\n"
"v_mfma_f32_16x16x32_bf16 v[24:27], a[116:119], v[184:187], v[24:27]\n"
"ds_read_b64_tr_b16 a[80:81], v245 offset:0\n"
"s_add_u32 m0, s81, 77824\n"
"v_mfma_f32_16x16x32_bf16 v[44:47], a[120:123], v[184:187], v[44:47]\n"
"ds_read_b64_tr_b16 a[82:83], v245 offset:256\n"
"v_mfma_f32_16x16x32_bf16 v[64:67], a[124:127], v[184:187], v[64:67]\n"
"buffer_load_dwordx4 v248, s[36:39], s71 offen lds\n"
"v_mfma_f32_16x16x32_bf16 v[84:87], a[128:131], v[184:187], v[84:87]\n"
"ds_read_b64_tr_b16 a[84:85], v245 offset:8\n"
"s_add_u32 m0, s81, 81920\n"
"v_mfma_f32_16x16x32_bf16 v[104:107], a[132:135], v[184:187], v[104:107]\n"
"ds_read_b64_tr_b16 a[86:87], v245 offset:264\n"
"v_mfma_f32_16x16x32_bf16 v[124:127], a[136:139], v[184:187], v[124:127]\n"
"buffer_load_dwordx4 v248, s[36:39], s72 offen lds\n"
"v_mfma_f32_16x16x32_bf16 v[144:147], a[140:143], v[184:187], v[144:147]\n"
"ds_read_b64_tr_b16 a[88:89], v245 offset:512\n"
"s_add_u32 m0, s80, 49152\n"
"v_mfma_f32_16x16x32_bf16 v[8:11], a[112:115], v[188:191], v[8:11]\n"
"ds_read_b64_tr_b16 a[90:91], v245 offset:768\n"
"s_add_u32 s17, s17, 128\n"
"v_mfma_f32_16x16x32_bf16 v[28:31], a[116:119], v[188:191], v[28:31]\n"
"buffer_load_dwordx4 v247, s[40:43], s16 offen lds\n"
"v_mfma_f32_16x16x32_bf16 v[48:51], a[120:123], v[188:191], v[48:51]\n"
"ds_read_b64_tr_b16 a[92:93], v245 offset:520\n"
"s_add_u32 m0, s80, 50048\n"
"v_mfma_f32_16x16x32_bf16 v[68:71], a[124:127], v[188:191], v[68:71]\n"
"ds_read_b64_tr_b16 a[94:95], v245 offset:776\n"
"v_mfma_f32_16x16x32_bf16 v[88:91], a[128:131], v[188:191], v[88:91]\n"
"buffer_load_dwordx4 v247, s[40:43], s16 offen offset:128 lds\n"
"v_mfma_f32_16x16x32_bf16 v[108:111], a[132:135], v[188:191], v[108:111]\n"
"ds_read_b64_tr_b16 a[96:97], v245 offset:1024\n"
"s_add_u32 m0, s80, 57088\n"
"v_mfma_f32_16x16x32_bf16 v[128:131], a[136:139], v[188:191], v[128:131]\n"
"ds_read_b64_tr_b16 a[98:99], v245 offset:1280\n"
"v_mfma_f32_16x16x32_bf16 v[148:151], a[140:143], v[188:191], v[148:151]\n"
"buffer_load_dwordx4 v247, s[40:43], s16 offen offset:256 lds\n"
"v_mfma_f32_16x16x32_bf16 v[12:15], a[112:115], v[192:195], v[12:15]\n"
"ds_read_b64_tr_b16 a[100:101], v245 offset:1032\n"
"s_add_u32 m0, s80, 57984\n"
"v_mfma_f32_16x16x32_bf16 v[32:35], a[116:119], v[192:195], v[32:35]\n"
"ds_read_b64_tr_b16 a[102:103], v245 offset:1288\n"
"v_mfma_f32_16x16x32_bf16 v[52:55], a[120:123], v[192:195], v[52:55]\n"
"buffer_load_dwordx4 v247, s[40:43], s16 offen offset:384 lds\n"
"v_mfma_f32_16x16x32_bf16 v[72:75], a[124:127], v[192:195], v[72:75]\n"
"ds_read_b64_tr_b16 a[104:105], v245 offset:1536\n"
"s_add_u32 m0, s80, 131072\n"
"v_mfma_f32_16x16x32_bf16 v[92:95], a[128:131], v[192:195], v[92:95]\n"
"ds_read_b64_tr_b16 a[106:107], v245 offset:1792\n"
"s_add_u32 s16, s16, 262144\n"
"v_mfma_f32_16x16x32_bf16 v[112:115], a[132:135], v[192:195], v[112:115]\n"
"buffer_load_dwordx4 v244, s[48:51], 0 offen nt lds\n"
"v_mfma_f32_16x16x32_bf16 v[132:135], a[136:139], v[192:195], v[132:135]\n"
"ds_read_b64_tr_b16 a[108:109], v245 offset:1544\n"
"s_add_u32 m0, s80, 131968\n"
"v_mfma_f32_16x16x32_bf16 v[152:155], a[140:143], v[192:195], v[152:155]\n"
"ds_read_b64_tr_b16 a[110:111], v245 offset:1800\n"
"v_mfma_f32_16x16x32_bf16 v[16:19], a[112:115], v[196:199], v[16:19]\n"
"buffer_load_dwordx4 v244, s[48:51], 0 offen offset:128 nt lds\n"
"v_mfma_f32_16x16x32_bf16 v[36:39], a[116:119], v[196:199], v[36:39]\n"
"s_add_u32 m0, s80, 139264\n"
"v_mfma_f32_16x16x32_bf16 v[56:59], a[120:123], v[196:199], v[56:59]\n"
"s_cmp_eq_u32 s17, 1280\n"
"s_cselect_b32 s18, -1152, s24\n"
"s_cselect_b32 s17, 0, s17\n"
"v_mfma_f32_16x16x32_bf16 v[76:79], a[124:127], v[196:199], v[76:79]\n"
"buffer_load_dwordx4 v244, s[48:51], s68 offen nt lds\n"
"v_mfma_f32_16x16x32_bf16 v[96:99], a[128:131], v[196:199], v[96:99]\n"
"s_add_u32 m0, s80, 140160\n"
"v_add_u32 v248, s18, v248\n"
"v_mfma_f32_16x16x32_bf16 v[116:119], a[132:135], v[196:199], v[116:119]\n"
"s_cmp_eq_u32 s16, 5242880\n"
"s_cselect_b32 s16, 0, s16\n"
"v_mfma_f32_16x16x32_bf16 v[136:139], a[136:139], v[196:199], v[136:139]\n"
"buffer_load_dwordx4 v244, s[48:51], s68 offen offset:128 nt lds\n"
"v_mfma_f32_16x16x32_bf16 v[156:159], a[140:143], v[196:199], v[156:159]\n"
"s_waitcnt vmcnt(21) lgkmcnt(0)\n"
"s_barrier\n"
"v_mfma_f32_16x16x32_bf16 v[0:3], a[80:83], v[160:163], v[0:3]\n"
"v_add_u32 v245, 147456, v249\n"
"s_add_u32 m0, s80, 86016\n"
"v_mfma_f32_16x16x32_bf16 v[20:23], a[84:87], v[160:163], v[20:23]\n"
"ds_read_b128 v[180:183], v246 offset:512\n"
"v_mfma_f32_16x16x32_bf16 v[40:43], a[88:91], v[160:163], v[40:43]\n"
"buffer_load_dwordx4 v247, s[40:43], s16 offen lds\n"
"v_mfma_f32_16x16x32_bf16 v[60:63], a[92:95], v[160:163], v[60:63]\n"
"ds_read_b128 v[184:187], v246 offset:2560\n"
"v_mfma_f32_16x16x32_bf16 v[80:83], a[96:99], v[160:163], v[80:83]\n"
"ds_read_b128 v[188:191], v246 offset:4608\n"
"s_add_u32 m0, s80, 86912\n"
"v_mfma_f32_16x16x32_bf16 v[100:103], a[100:103], v[160:163], v[100:103]\n"
"ds_read_b128 v[192:195], v246 offset:6656\n"
"v_mfma_f32_16x16x32_bf16 v[120:123], a[104:107], v[160:163], v[120:123]\n"
"buffer_load_dwordx4 v247, s[40:43], s16 offen offset:128 lds\n"
"v_mfma_f32_16x16x32_bf16 v[140:143], a[108:111], v[160:163], v[140:143]\n"
"ds_read_b128 v[196:199], v246 offset:8704\n"
"v_mfma_f32_16x16x32_bf16 v[4:7], a[80:83], v[164:167], v[4:7]\n"
"ds_read_b64_tr_b16 a[112:113], v245 offset:0\n"
"s_add_u32 m0, s80, 93952\n"
"v_mfma_f32_16x16x32_bf16 v[24:27], a[84:87], v[164:167], v[24:27]\n"
"ds_read_b64_tr_b16 a[114:115], v245 offset:256\n"
"v_mfma_f32_16x16x32_bf16 v[44:47], a[88:91], v[164:167], v[44:47]\n"
"buffer_load_dwordx4 v247, s[40:43], s16 offen offset:256 lds\n"
"v_mfma_f32_16x16x32_bf16 v[64:67], a[92:95], v[164:167], v[64:67]\n"
"ds_read_b64_tr_b16 a[116:117], v245 offset:8\n"
"v_mfma_f32_16x16x32_bf16 v[84:87], a[96:99], v[164:167], v[84:87]\n"
"ds_read_b64_tr_b16 a[118:119], v245 offset:264\n"
"s_add_u32 m0, s80, 94848\n"
"v_mfma_f32_16x16x32_bf16 v[104:107], a[100:103], v[164:167], v[104:107]\n"
"ds_read_b64_tr_b16 a[120:121], v245 offset:512\n"
"v_mfma_f32_16x16x32_bf16 v[124:127], a[104:107], v[164:167], v[124:127]\n"
"buffer_load_dwordx4 v247, s[40:43], s16 offen offset:384 lds\n"
"v_mfma_f32_16x16x32_bf16 v[144:147], a[108:111], v[164:167], v[144:147]\n"
"ds_read_b64_tr_b16 a[122:123], v245 offset:768\n"
"s_add_u32 s16, s16, 262144\n"
"v_mfma_f32_16x16x32_bf16 v[8:11], a[80:83], v[168:171], v[8:11]\n"
"ds_read_b64_tr_b16 a[124:125], v245 offset:520\n"
"v_mfma_f32_16x16x32_bf16 v[28:31], a[84:87], v[168:171], v[28:31]\n"
"ds_read_b64_tr_b16 a[126:127], v245 offset:776\n"
"v_mfma_f32_16x16x32_bf16 v[48:51], a[88:91], v[168:171], v[48:51]\n"
"ds_read_b64_tr_b16 a[128:129], v245 offset:1024\n"
"v_mfma_f32_16x16x32_bf16 v[68:71], a[92:95], v[168:171], v[68:71]\n"
"ds_read_b64_tr_b16 a[130:131], v245 offset:1280\n"
"v_mfma_f32_16x16x32_bf16 v[88:91], a[96:99], v[168:171], v[88:91]\n"
"ds_read_b64_tr_b16 a[132:133], v245 offset:1032\n"
"v_mfma_f32_16x16x32_bf16 v[108:111], a[100:103], v[168:171], v[108:111]\n"
"ds_read_b64_tr_b16 a[134:135], v245 offset:1288\n"
"v_mfma_f32_16x16x32_bf16 v[128:131], a[104:107], v[168:171], v[128:131]\n"
"ds_read_b64_tr_b16 a[136:137], v245 offset:1536\n"
"v_mfma_f32_16x16x32_bf16 v[148:151], a[108:111], v[168:171], v[148:151]\n"
"ds_read_b64_tr_b16 a[138:139], v245 offset:1792\n"
"v_mfma_f32_16x16x32_bf16 v[12:15], a[80:83], v[172:175], v[12:15]\n"
"ds_read_b64_tr_b16 a[140:141], v245 offset:1544\n"
"v_mfma_f32_16x16x32_bf16 v[32:35], a[84:87], v[172:175], v[32:35]\n"
"ds_read_b64_tr_b16 a[142:143], v245 offset:1800\n"
"v_mfma_f32_16x16x32_bf16 v[52:55], a[88:91], v[172:175], v[52:55]\n"
"s_cmp_eq_u32 s16, 5242880\n"
"s_cselect_b32 s16, 0, s16\n"
"v_mfma_f32_16x16x32_bf16 v[72:75], a[92:95], v[172:175], v[72:75]\n"
"v_mfma_f32_16x16x32_bf16 v[92:95], a[96:99], v[172:175], v[92:95]\n"
"v_mfma_f32_16x16x32_bf16 v[112:115], a[100:103], v[172:175], v[112:115]\n"
"v_mfma_f32_16x16x32_bf16 v[132:135], a[104:107], v[172:175], v[132:135]\n"
"v_mfma_f32_16x16x32_bf16 v[152:155], a[108:111], v[172:175], v[152:155]\n"
"v_mfma_f32_16x16x32_bf16 v[16:19], a[80:83], v[176:179], v[16:19]\n"
"v_mfma_f32_16x16x32_bf16 v[36:39], a[84:87], v[176:179], v[36:39]\n"
"v_mfma_f32_16x16x32_bf16 v[56:59], a[88:91], v[176:179], v[56:59]\n"
"v_mfma_f32_16x16x32_bf16 v[76:79], a[92:95], v[176:179], v[76:79]\n"
"v_mfma_f32_16x16x32_bf16 v[96:99], a[96:99], v[176:179], v[96:99]\n"
"v_mfma_f32_16x16x32_bf16 v[116:119], a[100:103], v[176:179], v[116:119]\n"
"v_mfma_f32_16x16x32_bf16 v[136:139], a[104:107], v[176:179], v[136:139]\n"
"v_mfma_f32_16x16x32_bf16 v[156:159], a[108:111], v[176:179], v[156:159]\n"
"s_waitcnt vmcnt(12) lgkmcnt(0)\n"
"s_barrier\n"
"v_mfma_f32_16x16x32_bf16 v[0:3], a[112:115], v[180:183], v[0:3]\n"
"v_add_u32 v245, 32768, v249\n"
"v_add_u32 v246, 65536, v250\n"
"v_mfma_f32_16x16x32_bf16 v[20:23], a[116:119], v[180:183], v[20:23]\n"
"ds_read_b128 v[160:163], v246 offset:0\n"
"s_add_u32 m0, s81, 102400\n"
"v_mfma_f32_16x16x32_bf16 v[40:43], a[120:123], v[180:183], v[40:43]\n"
"buffer_load_dwordx4 v248, s[36:39], 0 offen lds\n"
"v_mfma_f32_16x16x32_bf16 v[60:63], a[124:127], v[180:183], v[60:63]\n"
"ds_read_b128 v[164:167], v246 offset:2048\n"
"s_add_u32 m0, s81, 106496\n"
"v_mfma_f32_16x16x32_bf16 v[80:83], a[128:131], v[180:183], v[80:83]\n"
"ds_read_b128 v[168:171], v246 offset:4096\n"
"v_mfma_f32_16x16x32_bf16 v[100:103], a[132:135], v[180:183], v[100:103]\n"
"buffer_load_dwordx4 v248, s[36:39], s69 offen lds\n"
"v_mfma_f32_16x16x32_bf16 v[120:123], a[136:139], v[180:183], v[120:123]\n"
"ds_read_b128 v[172:175], v246 offset:6144\n"
"s_add_u32 m0, s81, 110592\n"
"v_mfma_f32_16x16x32_bf16 v[140:143], a[140:143], v[180:183], v[140:143]\n"
"ds_read_b128 v[176:179], v246 offset:8192\n"
"v_mfma_f32_16x16x32_bf16 v[4:7], a[112:115], v[184:187], v[4:7]\n"
"buffer_load_dwordx4 v248, s[36:39], s70 offen lds\n"
"v_mfma_f32_16x16x32_bf16 v[24:27], a[116:119], v[184:187], v[24:27]\n"
"ds_read_b64_tr_b16 a[80:81], v245 offset:0\n"
"s_add_u32 m0, s81, 114688\n"
"v_mfma_f32_16x16x32_bf16 v[44:47], a[120:123], v[184:187], v[44:47]\n"
"ds_read_b64_tr_b16 a[82:83], v245 offset:256\n"
"v_mfma_f32_16x16x32_bf16 v[64:67], a[124:127], v[184:187], v[64:67]\n"
"buffer_load_dwordx4 v248, s[36:39], s71 offen lds\n"
"v_mfma_f32_16x16x32_bf16 v[84:87], a[128:131], v[184:187], v[84:87]\n"
"ds_read_b64_tr_b16 a[84:85], v245 offset:8\n"
"s_add_u32 m0, s81, 118784\n"
"v_mfma_f32_16x16x32_bf16 v[104:107], a[132:135], v[184:187], v[104:107]\n"
"ds_read_b64_tr_b16 a[86:87], v245 offset:264\n"
"v_mfma_f32_16x16x32_bf16 v[124:127], a[136:139], v[184:187], v[124:127]\n"
"buffer_load_dwordx4 v248, s[36:39], s72 offen lds\n"
"v_mfma_f32_16x16x32_bf16 v[144:147], a[140:143], v[184:187], v[144:147]\n"
"ds_read_b64_tr_b16 a[88:89], v245 offset:512\n"
"s_add_u32 m0, s80, 147456\n"
"v_mfma_f32_16x16x32_bf16 v[8:11], a[112:115], v[188:191], v[8:11]\n"
"ds_read_b64_tr_b16 a[90:91], v245 offset:768\n"
"s_add_u32 s17, s17, 128\n"
"v_mfma_f32_16x16x32_bf16 v[28:31], a[116:119], v[188:191], v[28:31]\n"
"buffer_load_dwordx4 v247, s[40:43], s16 offen lds\n"
"v_mfma_f32_16x16x32_bf16 v[48:51], a[120:123], v[188:191], v[48:51]\n"
"ds_read_b64_tr_b16 a[92:93], v245 offset:520\n"
"s_add_u32 m0, s80, 148352\n"
"v_mfma_f32_16x16x32_bf16 v[68:71], a[124:127], v[188:191], v[68:71]\n"
"ds_read_b64_tr_b16 a[94:95], v245 offset:776\n"
"v_mfma_f32_16x16x32_bf16 v[88:91], a[128:131], v[188:191], v[88:91]\n"
"buffer_load_dwordx4 v247, s[40:43], s16 offen offset:128 lds\n"
"v_mfma_f32_16x16x32_bf16 v[108:111], a[132:135], v[188:191], v[108:111]\n"
"ds_read_b64_tr_b16 a[96:97], v245 offset:1024\n"
"s_add_u32 m0, s80, 155392\n"
"v_mfma_f32_16x16x32_bf16 v[128:131], a[136:139], v[188:191], v[128:131]\n"
"ds_read_b64_tr_b16 a[98:99], v245 offset:1280\n"
"v_mfma_f32_16x16x32_bf16 v[148:151], a[140:143], v[188:191], v[148:151]\n"
"buffer_load_dwordx4 v247, s[40:43], s16 offen offset:256 lds\n"
"v_mfma_f32_16x16x32_bf16 v[12:15], a[112:115], v[192:195], v[12:15]\n"
"ds_read_b64_tr_b16 a[100:101], v245 offset:1032\n"
"s_add_u32 m0, s80, 156288\n"
"v_mfma_f32_16x16x32_bf16 v[32:35], a[116:119], v[192:195], v[32:35]\n"
"ds_read_b64_tr_b16 a[102:103], v245 offset:1288\n"
"v_mfma_f32_16x16x32_bf16 v[52:55], a[120:123], v[192:195], v[52:55]\n"
"buffer_load_dwordx4 v247, s[40:43], s16 offen offset:384 lds\n"
"v_mfma_f32_16x16x32_bf16 v[72:75], a[124:127], v[192:195], v[72:75]\n"
"ds_read_b64_tr_b16 a[104:105], v245 offset:1536\n"
"s_add_u32 m0, s80, 16384\n"
"v_mfma_f32_16x16x32_bf16 v[92:95], a[128:131], v[192:195], v[92:95]\n"
"ds_read_b64_tr_b16 a[106:107], v245 offset:1792\n"
"s_add_u32 s16, s16, 262144\n"
"v_mfma_f32_16x16x32_bf16 v[112:115], a[132:135], v[192:195], v[112:115]\n"
"buffer_load_dwordx4 v243, s[44:47], s22 offen nt lds\n"
"v_mfma_f32_16x16x32_bf16 v[132:135], a[136:139], v[192:195], v[132:135]\n"
"ds_read_b64_tr_b16 a[108:109], v245 offset:1544\n"
"s_add_u32 m0, s80, 17280\n"
"v_mfma_f32_16x16x32_bf16 v[152:155], a[140:143], v[192:195], v[152:155]\n"
"ds_read_b64_tr_b16 a[110:111], v245 offset:1800\n"
"v_mfma_f32_16x16x32_bf16 v[16:19], a[112:115], v[196:199], v[16:19]\n"
"buffer_load_dwordx4 v243, s[44:47], s22 offen offset:128 nt lds\n"
"v_mfma_f32_16x16x32_bf16 v[36:39], a[116:119], v[196:199], v[36:39]\n"
"s_add_u32 m0, s80, 24576\n"
"v_mfma_f32_16x16x32_bf16 v[56:59], a[120:123], v[196:199], v[56:59]\n"
"s_cmp_eq_u32 s17, 1280\n"
"s_cselect_b32 s18, -1152, s24\n"
"s_cselect_b32 s17, 0, s17\n"
"v_mfma_f32_16x16x32_bf16 v[76:79], a[124:127], v[196:199], v[76:79]\n"
"buffer_load_dwordx4 v243, s[44:47], s23 offen nt lds\n"
"v_mfma_f32_16x16x32_bf16 v[96:99], a[128:131], v[196:199], v[96:99]\n"
"s_add_u32 m0, s80, 25472\n"
"v_add_u32 v248, s18, v248\n"
"v_mfma_f32_16x16x32_bf16 v[116:119], a[132:135], v[196:199], v[116:119]\n"
"s_cmp_eq_u32 s16, 5242880\n"
"s_cselect_b32 s16, 0, s16\n"
"v_mfma_f32_16x16x32_bf16 v[136:139], a[136:139], v[196:199], v[136:139]\n"
"buffer_load_dwordx4 v243, s[44:47], s23 offen offset:128 nt lds\n"
"v_mfma_f32_16x16x32_bf16 v[156:159], a[140:143], v[196:199], v[156:159]\n"
"s_waitcnt vmcnt(21) lgkmcnt(0)\n"
"s_barrier\n"
"v_mfma_f32_16x16x32_bf16 v[0:3], a[80:83], v[160:163], v[0:3]\n"
"v_add_u32 v245, 49152, v249\n"
"v_mfma_f32_16x16x32_bf16 v[20:23], a[84:87], v[160:163], v[20:23]\n"
"ds_read_b128 v[180:183], v246 offset:512\n"
"v_mfma_f32_16x16x32_bf16 v[40:43], a[88:91], v[160:163], v[40:43]\n"
"ds_read_b128 v[184:187], v246 offset:2560\n"
"v_mfma_f32_16x16x32_bf16 v[60:63], a[92:95], v[160:163], v[60:63]\n"
"ds_read_b128 v[188:191], v246 offset:4608\n"
"v_mfma_f32_16x16x32_bf16 v[80:83], a[96:99], v[160:163], v[80:83]\n"
"ds_read_b128 v[192:195], v246 offset:6656\n"
"v_mfma_f32_16x16x32_bf16 v[100:103], a[100:103], v[160:163], v[100:103]\n"
"ds_read_b128 v[196:199], v246 offset:8704\n"
"v_mfma_f32_16x16x32_bf16 v[120:123], a[104:107], v[160:163], v[120:123]\n"
"ds_read_b64_tr_b16 a[112:113], v245 offset:0\n"
"v_mfma_f32_16x16x32_bf16 v[140:143], a[108:111], v[160:163], v[140:143]\n"
"ds_read_b64_tr_b16 a[114:115], v245 offset:256\n"
"v_mfma_f32_16x16x32_bf16 v[4:7], a[80:83], v[164:167], v[4:7]\n"
"ds_read_b64_tr_b16 a[116:117], v245 offset:8\n"
"v_mfma_f32_16x16x32_bf16 v[24:27], a[84:87], v[164:167], v[24:27]\n"
"ds_read_b64_tr_b16 a[118:119], v245 offset:264\n"
"v_mfma_f32_16x16x32_bf16 v[44:47], a[88:91], v[164:167], v[44:47]\n"
"ds_read_b64_tr_b16 a[120:121], v245 offset:512\n"
"v_mfma_f32_16x16x32_bf16 v[64:67], a[92:95], v[164:167], v[64:67]\n"
"ds_read_b64_tr_b16 a[122:123], v245 offset:768\n"
"v_mfma_f32_16x16x32_bf16 v[84:87], a[96:99], v[164:167], v[84:87]\n"
"ds_read_b64_tr_b16 a[124:125], v245 offset:520\n"
"v_mfma_f32_16x16x32_bf16 v[104:107], a[100:103], v[164:167], v[104:107]\n"
"ds_read_b64_tr_b16 a[126:127], v245 offset:776\n"
"v_mfma_f32_16x16x32_bf16 v[124:127], a[104:107], v[164:167], v[124:127]\n"
"ds_read_b64_tr_b16 a[128:129], v245 offset:1024\n"
"v_mfma_f32_16x16x32_bf16 v[144:147], a[108:111], v[164:167], v[144:147]\n"
"ds_read_b64_tr_b16 a[130:131], v245 offset:1280\n"
"v_mfma_f32_16x16x32_bf16 v[8:11], a[80:83], v[168:171], v[8:11]\n"
"ds_read_b64_tr_b16 a[132:133], v245 offset:1032\n"
"v_mfma_f32_16x16x32_bf16 v[28:31], a[84:87], v[168:171], v[28:31]\n"
"ds_read_b64_tr_b16 a[134:135], v245 offset:1288\n"
"v_mfma_f32_16x16x32_bf16 v[48:51], a[88:91], v[168:171], v[48:51]\n"
"ds_read_b64_tr_b16 a[136:137], v245 offset:1536\n"
"v_mfma_f32_16x16x32_bf16 v[68:71], a[92:95], v[168:171], v[68:71]\n"
"ds_read_b64_tr_b16 a[138:139], v245 offset:1792\n"
"v_mfma_f32_16x16x32_bf16 v[88:91], a[96:99], v[168:171], v[88:91]\n"
"ds_read_b64_tr_b16 a[140:141], v245 offset:1544\n"
"v_mfma_f32_16x16x32_bf16 v[108:111], a[100:103], v[168:171], v[108:111]\n"
"ds_read_b64_tr_b16 a[142:143], v245 offset:1800\n"
"v_mfma_f32_16x16x32_bf16 v[128:131], a[104:107], v[168:171], v[128:131]\n"
"v_mfma_f32_16x16x32_bf16 v[148:151], a[108:111], v[168:171], v[148:151]\n"
"v_mfma_f32_16x16x32_bf16 v[12:15], a[80:83], v[172:175], v[12:15]\n"
"v_mfma_f32_16x16x32_bf16 v[32:35], a[84:87], v[172:175], v[32:35]\n"
"v_mfma_f32_16x16x32_bf16 v[52:55], a[88:91], v[172:175], v[52:55]\n"
"v_mfma_f32_16x16x32_bf16 v[72:75], a[92:95], v[172:175], v[72:75]\n"
"v_mfma_f32_16x16x32_bf16 v[92:95], a[96:99], v[172:175], v[92:95]\n"
"v_mfma_f32_16x16x32_bf16 v[112:115], a[100:103], v[172:175], v[112:115]\n"
"v_mfma_f32_16x16x32_bf16 v[132:135], a[104:107], v[172:175], v[132:135]\n"
"v_mfma_f32_16x16x32_bf16 v[152:155], a[108:111], v[172:175], v[152:155]\n"
"v_mfma_f32_16x16x32_bf16 v[16:19], a[80:83], v[176:179], v[16:19]\n"
"v_mfma_f32_16x16x32_bf16 v[36:39], a[84:87], v[176:179], v[36:39]\n"
"v_mfma_f32_16x16x32_bf16 v[56:59], a[88:91], v[176:179], v[56:59]\n"
"v_mfma_f32_16x16x32_bf16 v[76:79], a[92:95], v[176:179], v[76:79]\n"
"v_mfma_f32_16x16x32_bf16 v[96:99], a[96:99], v[176:179], v[96:99]\n"
"v_mfma_f32_16x16x32_bf16 v[116:119], a[100:103], v[176:179], v[116:119]\n"
"v_mfma_f32_16x16x32_bf16 v[136:139], a[104:107], v[176:179], v[136:139]\n"
"v_mfma_f32_16x16x32_bf16 v[156:159], a[108:111], v[176:179], v[156:159]\n"
"s_waitcnt vmcnt(8) lgkmcnt(0)\n"
"s_barrier\n"
"v_mfma_f32_16x16x32_bf16 v[0:3], a[112:115], v[180:183], v[0:3]\n"
"v_add_u32 v245, 86016, v249\n"
"v_add_u32 v246, 102400, v250\n"
"v_mfma_f32_16x16x32_bf16 v[20:23], a[116:119], v[180:183], v[20:23]\n"
"ds_read_b128 v[160:163], v246 offset:0\n"
"s_add_u32 m0, s80, 65536\n"
"v_mfma_f32_16x16x32_bf16 v[40:43], a[120:123], v[180:183], v[40:43]\n"
"buffer_load_dwordx4 v243, s[44:47], s94 offen nt lds\n"
"v_mfma_f32_16x16x32_bf16 v[60:63], a[124:127], v[180:183], v[60:63]\n"
"ds_read_b128 v[164:167], v246 offset:2048\n"
"v_mfma_f32_16x16x32_bf16 v[80:83], a[128:131], v[180:183], v[80:83]\n"
"ds_read_b128 v[168:171], v246 offset:4096\n"
"s_add_u32 m0, s80, 66432\n"
"v_mfma_f32_16x16x32_bf16 v[100:103], a[132:135], v[180:183], v[100:103]\n"
"ds_read_b128 v[172:175], v246 offset:6144\n"
"v_mfma_f32_16x16x32_bf16 v[120:123], a[136:139], v[180:183], v[120:123]\n"
"buffer_load_dwordx4 v243, s[44:47], s94 offen offset:128 nt lds\n"
"v_mfma_f32_16x16x32_bf16 v[140:143], a[140:143], v[180:183], v[140:143]\n"
"ds_read_b128 v[176:179], v246 offset:8192\n"
"v_mfma_f32_16x16x32_bf16 v[4:7], a[112:115], v[184:187], v[4:7]\n"
"ds_read_b64_tr_b16 a[80:81], v245 offset:0\n"
"s_add_u32 m0, s80, 73728\n"
"v_mfma_f32_16x16x32_bf16 v[24:27], a[116:119], v[184:187], v[24:27]\n"
"ds_read_b64_tr_b16 a[82:83], v245 offset:256\n"
"v_mfma_f32_16x16x32_bf16 v[44:47], a[120:123], v[184:187], v[44:47]\n"
"buffer_load_dwordx4 v243, s[44:47], s95 offen nt lds\n"
"v_mfma_f32_16x16x32_bf16 v[64:67], a[124:127], v[184:187], v[64:67]\n"
"ds_read_b64_tr_b16 a[84:85], v245 offset:8\n"
"v_mfma_f32_16x16x32_bf16 v[84:87], a[128:131], v[184:187], v[84:87]\n"
"ds_read_b64_tr_b16 a[86:87], v245 offset:264\n"
"s_add_u32 m0, s80, 74624\n"
"v_mfma_f32_16x16x32_bf16 v[104:107], a[132:135], v[184:187], v[104:107]\n"
"ds_read_b64_tr_b16 a[88:89], v245 offset:512\n"
"v_mfma_f32_16x16x32_bf16 v[124:127], a[136:139], v[184:187], v[124:127]\n"
"buffer_load_dwordx4 v243, s[44:47], s95 offen offset:128 nt lds\n"
"v_mfma_f32_16x16x32_bf16 v[144:147], a[140:143], v[184:187], v[144:147]\n"
"ds_read_b64_tr_b16 a[90:91], v245 offset:768\n"
"v_mfma_f32_16x16x32_bf16 v[8:11], a[112:115], v[188:191], v[8:11]\n"
"ds_read_b64_tr_b16 a[92:93], v245 offset:520\n"
"v_mfma_f32_16x16x32_bf16 v[28:31], a[116:119], v[188:191], v[28:31]\n"
"ds_read_b64_tr_b16 a[94:95], v245 offset:776\n"
"v_mfma_f32_16x16x32_bf16 v[48:51], a[120:123], v[188:191], v[48:51]\n"
"ds_read_b64_tr_b16 a[96:97], v245 offset:1024\n"
"v_mfma_f32_16x16x32_bf16 v[68:71], a[124:127], v[188:191], v[68:71]\n"
"ds_read_b64_tr_b16 a[98:99], v245 offset:1280\n"
"v_mfma_f32_16x16x32_bf16 v[88:91], a[128:131], v[188:191], v[88:91]\n"
"ds_read_b64_tr_b16 a[100:101], v245 offset:1032\n"
"v_mfma_f32_16x16x32_bf16 v[108:111], a[132:135], v[188:191], v[108:111]\n"
"ds_read_b64_tr_b16 a[102:103], v245 offset:1288\n"
"v_mfma_f32_16x16x32_bf16 v[128:131], a[136:139], v[188:191], v[128:131]\n"
"ds_read_b64_tr_b16 a[104:105], v245 offset:1536\n"
"v_mfma_f32_16x16x32_bf16 v[148:151], a[140:143], v[188:191], v[148:151]\n"
"ds_read_b64_tr_b16 a[106:107], v245 offset:1792\n"
"v_mfma_f32_16x16x32_bf16 v[12:15], a[112:115], v[192:195], v[12:15]\n"
"ds_read_b64_tr_b16 a[108:109], v245 offset:1544\n"
"v_mfma_f32_16x16x32_bf16 v[32:35], a[116:119], v[192:195], v[32:35]\n"
"ds_read_b64_tr_b16 a[110:111], v245 offset:1800\n"
"v_mfma_f32_16x16x32_bf16 v[52:55], a[120:123], v[192:195], v[52:55]\n"
"v_mfma_f32_16x16x32_bf16 v[72:75], a[124:127], v[192:195], v[72:75]\n"
"v_mfma_f32_16x16x32_bf16 v[92:95], a[128:131], v[192:195], v[92:95]\n"
"v_mfma_f32_16x16x32_bf16 v[112:115], a[132:135], v[192:195], v[112:115]\n"
"v_mfma_f32_16x16x32_bf16 v[132:135], a[136:139], v[192:195], v[132:135]\n"
"v_mfma_f32_16x16x32_bf16 v[152:155], a[140:143], v[192:195], v[152:155]\n"
"v_mfma_f32_16x16x32_bf16 v[16:19], a[112:115], v[196:199], v[16:19]\n"
"v_mfma_f32_16x16x32_bf16 v[36:39], a[116:119], v[196:199], v[36:39]\n"
"v_mfma_f32_16x16x32_bf16 v[56:59], a[120:123], v[196:199], v[56:59]\n"
"v_mfma_f32_16x16x32_bf16 v[76:79], a[124:127], v[196:199], v[76:79]\n"
"v_mfma_f32_16x16x32_bf16 v[96:99], a[128:131], v[196:199], v[96:99]\n"
"v_mfma_f32_16x16x32_bf16 v[116:119], a[132:135], v[196:199], v[116:119]\n"
"v_mfma_f32_16x16x32_bf16 v[136:139], a[136:139], v[196:199], v[136:139]\n"
"v_mfma_f32_16x16x32_bf16 v[156:159], a[140:143], v[196:199], v[156:159]\n"
"s_waitcnt vmcnt(8) lgkmcnt(0)\n"
"s_barrier\n"
"v_mfma_f32_16x16x32_bf16 v[0:3], a[80:83], v[160:163], v[0:3]\n"
"v_add_u32 v245, 147456, v249\n"
"s_add_u32 m0, s80, 81920\n"
"v_mfma_f32_16x16x32_bf16 v[20:23], a[84:87], v[160:163], v[20:23]\n"
"ds_read_b128 v[180:183], v246 offset:512\n"
"v_mfma_f32_16x16x32_bf16 v[40:43], a[88:91], v[160:163], v[40:43]\n"
"buffer_load_dwordx4 v244, s[48:51], s88 offen nt lds\n"
"v_mfma_f32_16x16x32_bf16 v[60:63], a[92:95], v[160:163], v[60:63]\n"
"ds_read_b128 v[184:187], v246 offset:2560\n"
"v_mfma_f32_16x16x32_bf16 v[80:83], a[96:99], v[160:163], v[80:83]\n"
"ds_read_b128 v[188:191], v246 offset:4608\n"
"s_add_u32 m0, s80, 82816\n"
"v_mfma_f32_16x16x32_bf16 v[100:103], a[100:103], v[160:163], v[100:103]\n"
"ds_read_b128 v[192:195], v246 offset:6656\n"
"v_mfma_f32_16x16x32_bf16 v[120:123], a[104:107], v[160:163], v[120:123]\n"
"buffer_load_dwordx4 v244, s[48:51], s88 offen offset:128 nt lds\n"
"v_mfma_f32_16x16x32_bf16 v[140:143], a[108:111], v[160:163], v[140:143]\n"
"ds_read_b128 v[196:199], v246 offset:8704\n"
"v_mfma_f32_16x16x32_bf16 v[4:7], a[80:83], v[164:167], v[4:7]\n"
"ds_read_b64_tr_b16 a[112:113], v245 offset:0\n"
"s_add_u32 m0, s80, 90112\n"
"v_mfma_f32_16x16x32_bf16 v[24:27], a[84:87], v[164:167], v[24:27]\n"
"ds_read_b64_tr_b16 a[114:115], v245 offset:256\n"
"v_mfma_f32_16x16x32_bf16 v[44:47], a[88:91], v[164:167], v[44:47]\n"
"buffer_load_dwordx4 v244, s[48:51], s89 offen nt lds\n"
"v_mfma_f32_16x16x32_bf16 v[64:67], a[92:95], v[164:167], v[64:67]\n"
"ds_read_b64_tr_b16 a[116:117], v245 offset:8\n"
"v_mfma_f32_16x16x32_bf16 v[84:87], a[96:99], v[164:167], v[84:87]\n"
"ds_read_b64_tr_b16 a[118:119], v245 offset:264\n"
"s_add_u32 m0, s80, 91008\n"
"v_mfma_f32_16x16x32_bf16 v[104:107], a[100:103], v[164:167], v[104:107]\n"
"ds_read_b64_tr_b16 a[120:121], v245 offset:512\n"
"v_mfma_f32_16x16x32_bf16 v[124:127], a[104:107], v[164:167], v[124:127]\n"
"buffer_load_dwordx4 v244, s[48:51], s89 offen offset:128 nt lds\n"
"v_mfma_f32_16x16x32_bf16 v[144:147], a[108:111], v[164:167], v[144:147]\n"
"ds_read_b64_tr_b16 a[122:123], v245 offset:768\n"
"v_mfma_f32_16x16x32_bf16 v[8:11], a[80:83], v[168:171], v[8:11]\n"
"ds_read_b64_tr_b16 a[124:125], v245 offset:520\n"
"v_mfma_f32_16x16x32_bf16 v[28:31], a[84:87], v[168:171], v[28:31]\n"
"ds_read_b64_tr_b16 a[126:127], v245 offset:776\n"
"v_mfma_f32_16x16x32_bf16 v[48:51], a[88:91], v[168:171], v[48:51]\n"
"ds_read_b64_tr_b16 a[128:129], v245 offset:1024\n"
"v_mfma_f32_16x16x32_bf16 v[68:71], a[92:95], v[168:171], v[68:71]\n"
"ds_read_b64_tr_b16 a[130:131], v245 offset:1280\n"
"v_mfma_f32_16x16x32_bf16 v[88:91], a[96:99], v[168:171], v[88:91]\n"
"ds_read_b64_tr_b16 a[132:133], v245 offset:1032\n"
"v_mfma_f32_16x16x32_bf16 v[108:111], a[100:103], v[168:171], v[108:111]\n"
"ds_read_b64_tr_b16 a[134:135], v245 offset:1288\n"
"v_mfma_f32_16x16x32_bf16 v[128:131], a[104:107], v[168:171], v[128:131]\n"
"ds_read_b64_tr_b16 a[136:137], v245 offset:1536\n"
"v_mfma_f32_16x16x32_bf16 v[148:151], a[108:111], v[168:171], v[148:151]\n"
"ds_read_b64_tr_b16 a[138:139], v245 offset:1792\n"
"v_mfma_f32_16x16x32_bf16 v[12:15], a[80:83], v[172:175], v[12:15]\n"
"ds_read_b64_tr_b16 a[140:141], v245 offset:1544\n"
"v_mfma_f32_16x16x32_bf16 v[32:35], a[84:87], v[172:175], v[32:35]\n"
"ds_read_b64_tr_b16 a[142:143], v245 offset:1800\n"
"v_mfma_f32_16x16x32_bf16 v[52:55], a[88:91], v[172:175], v[52:55]\n"
"v_mfma_f32_16x16x32_bf16 v[72:75], a[92:95], v[172:175], v[72:75]\n"
"v_mfma_f32_16x16x32_bf16 v[92:95], a[96:99], v[172:175], v[92:95]\n"
"v_mfma_f32_16x16x32_bf16 v[112:115], a[100:103], v[172:175], v[112:115]\n"
"v_mfma_f32_16x16x32_bf16 v[132:135], a[104:107], v[172:175], v[132:135]\n"
"v_mfma_f32_16x16x32_bf16 v[152:155], a[108:111], v[172:175], v[152:155]\n"
"v_mfma_f32_16x16x32_bf16 v[16:19], a[80:83], v[176:179], v[16:19]\n"
"v_mfma_f32_16x16x32_bf16 v[36:39], a[84:87], v[176:179], v[36:39]\n"
"v_mfma_f32_16x16x32_bf16 v[56:59], a[88:91], v[176:179], v[56:59]\n"
"v_mfma_f32_16x16x32_bf16 v[76:79], a[92:95], v[176:179], v[76:79]\n"
"v_mfma_f32_16x16x32_bf16 v[96:99], a[96:99], v[176:179], v[96:99]\n"
"v_mfma_f32_16x16x32_bf16 v[116:119], a[100:103], v[176:179], v[116:119]\n"
"v_mfma_f32_16x16x32_bf16 v[136:139], a[104:107], v[176:179], v[136:139]\n"
"v_mfma_f32_16x16x32_bf16 v[156:159], a[108:111], v[176:179], v[156:159]\n"
"s_waitcnt vmcnt(63) lgkmcnt(0)\n"
"s_barrier\n"
"v_mfma_f32_16x16x32_bf16 v[0:3], a[112:115], v[180:183], v[0:3]\n"
"v_accvgpr_read_b32 v240, a168\n"
"v_accvgpr_read_b32 v243, a170\n"
"v_mfma_f32_16x16x32_bf16 v[20:23], a[116:119], v[180:183], v[20:23]\n"
"v_accvgpr_read_b32 v245, a168\n"
"v_accvgpr_read_b32 v246, a169\n"
"v_mfma_f32_16x16x32_bf16 v[40:43], a[120:123], v[180:183], v[40:43]\n"
"ds_read_b128 a[80:83], v240 offset:0\n"
"v_mfma_f32_16x16x32_bf16 v[60:63], a[124:127], v[180:183], v[60:63]\n"
"ds_read_b128 a[84:87], v240 offset:512\n"
"v_accvgpr_read_b32 v244, a171\n"
"v_mfma_f32_16x16x32_bf16 v[80:83], a[128:131], v[180:183], v[80:83]\n"
"s_add_u32 s78, s84, 40960\n"
"ds_read_b128 a[88:91], v240 offset:1024\n"
"v_mfma_f32_16x16x32_bf16 v[100:103], a[132:135], v[180:183], v[100:103]\n"
"ds_read_b128 a[92:95], v240 offset:1536\n"
"v_add_u32 v243, s78, v243\n"
"v_mfma_f32_16x16x32_bf16 v[120:123], a[136:139], v[180:183], v[120:123]\n"
"v_add_u32 v244, s78, v244\n"
"v_mfma_f32_16x16x32_bf16 v[140:143], a[140:143], v[180:183], v[140:143]\n"
"ds_read_b128 a[96:99], v240 offset:4096\n"
"v_mfma_f32_16x16x32_bf16 v[4:7], a[112:115], v[184:187], v[4:7]\n"
"ds_read_b128 a[100:103], v240 offset:4608\n"
"v_accvgpr_read_b32 v241, a169\n"
"v_mfma_f32_16x16x32_bf16 v[24:27], a[116:119], v[184:187], v[24:27]\n"
"s_add_u32 s76, s80, 0\n"
"ds_read_b128 a[104:107], v240 offset:5120\n"
"v_mfma_f32_16x16x32_bf16 v[44:47], a[120:123], v[184:187], v[44:47]\n"
"ds_read_b128 a[108:111], v240 offset:5632\n"
"s_add_u32 s77, s80, 131072\n"
"v_mfma_f32_16x16x32_bf16 v[64:67], a[124:127], v[184:187], v[64:67]\n"
"v_add_u32 v241, 131072, v241\n"
"v_add_u32 v216, s80, v251\n"
"v_mfma_f32_16x16x32_bf16 v[84:87], a[128:131], v[184:187], v[84:87]\n"
"s_waitcnt vmcnt(63)\n"
"v_add_u32 v217, 98304, v216\n"
"v_mfma_f32_16x16x32_bf16 v[104:107], a[132:135], v[184:187], v[104:107]\n"
"v_xor_b32 v242, 32, v241\n"
"v_mfma_f32_16x16x32_bf16 v[124:127], a[136:139], v[184:187], v[124:127]\n"
"ds_write_b128 v216, a[0:3] offset:32768\n"
"v_mfma_f32_16x16x32_bf16 v[144:147], a[140:143], v[184:187], v[144:147]\n"
"ds_write_b128 v216, a[20:23] offset:33792\n"
"v_mfma_f32_16x16x32_bf16 v[8:11], a[112:115], v[188:191], v[8:11]\n"
"ds_write_b128 v216, a[40:43] offset:40960\n"
"v_mfma_f32_16x16x32_bf16 v[28:31], a[116:119], v[188:191], v[28:31]\n"
"ds_write_b128 v216, a[60:63] offset:41984\n"
"v_mfma_f32_16x16x32_bf16 v[48:51], a[120:123], v[188:191], v[48:51]\n"
"v_mul_f32 v0, s82, v0\n"
"v_mul_f32 v1, s82, v1\n"
"v_mfma_f32_16x16x32_bf16 v[68:71], a[124:127], v[188:191], v[68:71]\n"
"v_mul_f32 v2, s82, v2\n"
"v_mul_f32 v3, s82, v3\n"
"v_mfma_f32_16x16x32_bf16 v[88:91], a[128:131], v[188:191], v[88:91]\n"
"v_mul_f32 v20, s82, v20\n"
"v_mul_f32 v21, s82, v21\n"
"v_mfma_f32_16x16x32_bf16 v[108:111], a[132:135], v[188:191], v[108:111]\n"
"v_mul_f32 v22, s82, v22\n"
"v_mul_f32 v23, s82, v23\n"
"v_mfma_f32_16x16x32_bf16 v[128:131], a[136:139], v[188:191], v[128:131]\n"
"v_cvt_pk_bf16_f32 v208, v0, v1\n"
"v_cvt_pk_bf16_f32 v209, v2, v3\n"
"v_mfma_f32_16x16x32_bf16 v[148:151], a[140:143], v[188:191], v[148:151]\n"
"v_cvt_pk_bf16_f32 v210, v20, v21\n"
"v_cvt_pk_bf16_f32 v211, v22, v23\n"
"v_mfma_f32_16x16x32_bf16 v[12:15], a[112:115], v[192:195], v[12:15]\n"
"v_mul_f32 v40, s82, v40\n"
"v_mul_f32 v41, s82, v41\n"
"v_mfma_f32_16x16x32_bf16 v[32:35], a[116:119], v[192:195], v[32:35]\n"
"v_mul_f32 v42, s82, v42\n"
"v_mul_f32 v43, s82, v43\n"
"v_mfma_f32_16x16x32_bf16 v[52:55], a[120:123], v[192:195], v[52:55]\n"
"v_mul_f32 v60, s82, v60\n"
"v_mul_f32 v61, s82, v61\n"
"v_mfma_f32_16x16x32_bf16 v[72:75], a[124:127], v[192:195], v[72:75]\n"
"v_mul_f32 v62, s82, v62\n"
"v_mul_f32 v63, s82, v63\n"
"v_mfma_f32_16x16x32_bf16 v[92:95], a[128:131], v[192:195], v[92:95]\n"
"s_waitcnt lgkmcnt(0)\n"
"v_accvgpr_write_b32 a0, v208\n"
"v_mfma_f32_16x16x32_bf16 v[112:115], a[132:135], v[192:195], v[112:115]\n"
"ds_write_b128 v217, a[4:7] offset:0\n"
"v_mfma_f32_16x16x32_bf16 v[132:135], a[136:139], v[192:195], v[132:135]\n"
"v_accvgpr_write_b32 a1, v209\n"
"v_accvgpr_write_b32 a2, v210\n"
"v_mfma_f32_16x16x32_bf16 v[152:155], a[140:143], v[192:195], v[152:155]\n"
"v_accvgpr_write_b32 a3, v211\n"
"v_cvt_pk_bf16_f32 v212, v40, v41\n"
"v_mfma_f32_16x16x32_bf16 v[16:19], a[112:115], v[196:199], v[16:19]\n"
"v_cvt_pk_bf16_f32 v213, v42, v43\n"
"v_cvt_pk_bf16_f32 v214, v60, v61\n"
"v_mfma_f32_16x16x32_bf16 v[36:39], a[116:119], v[196:199], v[36:39]\n"
"v_cvt_pk_bf16_f32 v215, v62, v63\n"
"v_mfma_f32_16x16x32_bf16 v[56:59], a[120:123], v[196:199], v[56:59]\n"
"v_mul_f32 v80, s82, v80\n"
"v_mul_f32 v81, s82, v81\n"
"v_mfma_f32_16x16x32_bf16 v[76:79], a[124:127], v[196:199], v[76:79]\n"
"ds_write_b128 v217, a[24:27] offset:1024\n"
"v_mfma_f32_16x16x32_bf16 v[96:99], a[128:131], v[196:199], v[96:99]\n"
"v_mul_f32 v82, s82, v82\n"
"v_mul_f32 v83, s82, v83\n"
"v_mfma_f32_16x16x32_bf16 v[116:119], a[132:135], v[196:199], v[116:119]\n"
"v_mul_f32 v100, s82, v100\n"
"v_mul_f32 v101, s82, v101\n"
"v_mfma_f32_16x16x32_bf16 v[136:139], a[136:139], v[196:199], v[136:139]\n"
"v_mul_f32 v102, s82, v102\n"
"v_mul_f32 v103, s82, v103\n"
"v_mfma_f32_16x16x32_bf16 v[156:159], a[140:143], v[196:199], v[156:159]\n"
"v_accvgpr_write_b32 a20, v212\n"
"v_accvgpr_write_b32 a21, v213\n"
"v_accvgpr_write_b32 a22, v214\n"
"v_accvgpr_write_b32 a23, v215\n"
"v_cvt_pk_bf16_f32 v208, v80, v81\n"
"ds_write_b128 v217, a[44:47] offset:8192\n"
"v_cvt_pk_bf16_f32 v209, v82, v83\n"
"v_cvt_pk_bf16_f32 v210, v100, v101\n"
"v_cvt_pk_bf16_f32 v211, v102, v103\n"
"v_pk_mul_f32 v[120:121], v[120:121], s[82:83] op_sel:[0,0] op_sel_hi:[1,0]\n"
"v_pk_mul_f32 v[122:123], v[122:123], s[82:83] op_sel:[0,0] op_sel_hi:[1,0]\n"
"v_pk_mul_f32 v[140:141], v[140:141], s[82:83] op_sel:[0,0] op_sel_hi:[1,0]\n"
"v_pk_mul_f32 v[142:143], v[142:143], s[82:83] op_sel:[0,0] op_sel_hi:[1,0]\n"
"v_accvgpr_write_b32 a40, v208\n"
"ds_write_b128 v217, a[64:67] offset:9216\n"
"v_accvgpr_write_b32 a41, v209\n"
"v_accvgpr_write_b32 a42, v210\n"
"v_accvgpr_write_b32 a43, v211\n"
"v_cvt_pk_bf16_f32 v212, v120, v121\n"
"v_cvt_pk_bf16_f32 v213, v122, v123\n"
"v_cvt_pk_bf16_f32 v214, v140, v141\n"
"v_cvt_pk_bf16_f32 v215, v142, v143\n"
"v_accvgpr_write_b32 a60, v212\n"
"v_accvgpr_write_b32 a61, v213\n"
"v_accvgpr_write_b32 a62, v214\n"
"v_accvgpr_write_b32 a63, v215\n"
"s_waitcnt vmcnt(63)\n"
"s_barrier\n"
"s_add_u32 m0, s76, 0\n"
"s_nop 0\n"
"buffer_load_dwordx4 v243, s[44:47], 0 offen nt lds\n"
"s_add_u32 m0, s76, 896\n"
"s_nop 0\n"
"buffer_load_dwordx4 v243, s[44:47], 0 offen offset:128 nt lds\n"
"s_add_u32 m0, s76, 8192\n"
"s_nop 0\n"
"buffer_load_dwordx4 v243, s[44:47], s68 offen nt lds\n"
"s_add_u32 m0, s76, 9088\n"
"s_nop 0\n"
"buffer_load_dwordx4 v243, s[44:47], s68 offen offset:128 nt lds\n"
"v_add_u32 v243, 8192, v243\n"
"s_nop 1\n"
"v_mfma_f32_16x16x32_bf16 v[160:163], a[80:83], a[0:3], 0\n"
"v_mul_f32 v4, s82, v4\n"
"v_mul_f32 v5, s82, v5\n"
"v_mfma_f32_16x16x32_bf16 v[160:163], a[84:87], a[20:23], v[160:163]\n"
"v_mul_f32 v6, s82, v6\n"
"v_mul_f32 v7, s82, v7\n"
"v_mfma_f32_16x16x32_bf16 v[160:163], a[88:91], a[40:43], v[160:163]\n"
"v_mul_f32 v24, s82, v24\n"
"v_mul_f32 v25, s82, v25\n"
"v_mfma_f32_16x16x32_bf16 v[160:163], a[92:95], a[60:63], v[160:163]\n"
"v_mul_f32 v26, s82, v26\n"
"v_mul_f32 v27, s82, v27\n"
"v_mfma_f32_16x16x32_bf16 v[164:167], a[96:99], a[0:3], 0\n"
"v_cvt_pk_bf16_f32 v208, v4, v5\n"
"v_cvt_pk_bf16_f32 v209, v6, v7\n"
"v_mfma_f32_16x16x32_bf16 v[164:167], a[100:103], a[20:23], v[164:167]\n"
"v_cvt_pk_bf16_f32 v210, v24, v25\n"
"v_cvt_pk_bf16_f32 v211, v26, v27\n"
"v_mfma_f32_16x16x32_bf16 v[164:167], a[104:107], a[40:43], v[164:167]\n"
"v_mul_f32 v44, s82, v44\n"
"v_mul_f32 v45, s82, v45\n"
"v_mfma_f32_16x16x32_bf16 v[164:167], a[108:111], a[60:63], v[164:167]\n"
"v_mul_f32 v46, s82, v46\n"
"v_mul_f32 v47, s82, v47\n"
"v_mul_f32 v64, s82, v64\n"
"v_mul_f32 v65, s82, v65\n"
"v_pk_mul_f32 v[66:67], v[66:67], s[82:83] op_sel:[0,0] op_sel_hi:[1,0]\n"
"s_waitcnt lgkmcnt(0)\n"
"v_accvgpr_write_b32 a4, v208\n"
"ds_write_b128 v216, a[8:11] offset:49152\n"
"v_accvgpr_write_b32 a5, v209\n"
"v_accvgpr_write_b32 a6, v210\n"
"v_accvgpr_write_b32 a7, v211\n"
"v_cvt_pk_bf16_f32 v212, v44, v45\n"
"v_cvt_pk_bf16_f32 v213, v46, v47\n"
"v_cvt_pk_bf16_f32 v214, v64, v65\n"
"v_cvt_pk_bf16_f32 v215, v66, v67\n"
"v_pk_mul_f32 v[84:85], v[84:85], s[82:83] op_sel:[0,0] op_sel_hi:[1,0]\n"
"ds_write_b128 v216, a[28:31] offset:50176\n"
"v_pk_mul_f32 v[86:87], v[86:87], s[82:83] op_sel:[0,0] op_sel_hi:[1,0]\n"
"v_pk_mul_f32 v[104:105], v[104:105], s[82:83] op_sel:[0,0] op_sel_hi:[1,0]\n"
"v_pk_mul_f32 v[106:107], v[106:107], s[82:83] op_sel:[0,0] op_sel_hi:[1,0]\n"
"v_accvgpr_write_b32 a24, v212\n"
"v_accvgpr_write_b32 a25, v213\n"
"v_accvgpr_write_b32 a26, v214\n"
"v_accvgpr_write_b32 a27, v215\n"
"v_cvt_pk_bf16_f32 v208, v84, v85\n"
"ds_write_b128 v216, a[48:51] offset:57344\n"
"v_cvt_pk_bf16_f32 v209, v86, v87\n"
"v_cvt_pk_bf16_f32 v210, v104, v105\n"
"v_cvt_pk_bf16_f32 v211, v106, v107\n"
"v_pk_mul_f32 v[124:125], v[124:125], s[82:83] op_sel:[0,0] op_sel_hi:[1,0]\n"
"v_pk_mul_f32 v[126:127], v[126:127], s[82:83] op_sel:[0,0] op_sel_hi:[1,0]\n"
"v_pk_mul_f32 v[144:145], v[144:145], s[82:83] op_sel:[0,0] op_sel_hi:[1,0]\n"
"v_pk_mul_f32 v[146:147], v[146:147], s[82:83] op_sel:[0,0] op_sel_hi:[1,0]\n"
"v_accvgpr_write_b32 a44, v208\n"
"ds_write_b128 v216, a[68:71] offset:58368\n"
"v_accvgpr_write_b32 a45, v209\n"
"v_accvgpr_write_b32 a46, v210\n"
"v_accvgpr_write_b32 a47, v211\n"
"v_cvt_pk_bf16_f32 v212, v124, v125\n"
"v_cvt_pk_bf16_f32 v213, v126, v127\n"
"v_cvt_pk_bf16_f32 v214, v144, v145\n"
"v_cvt_pk_bf16_f32 v215, v146, v147\n"
"v_accvgpr_write_b32 a64, v212\n"
"v_accvgpr_write_b32 a65, v213\n"
"v_accvgpr_write_b32 a66, v214\n"
"v_accvgpr_write_b32 a67, v215\n"
"s_nop 1\n"
"v_mfma_f32_16x16x32_bf16 v[168:171], a[80:83], a[4:7], 0\n"
"ds_read_b64_tr_b16 a[112:113], v241 offset:0\n"
"v_mfma_f32_16x16x32_bf16 v[168:171], a[84:87], a[24:27], v[168:171]\n"
"v_mul_f32 v8, s82, v8\n"
"v_mul_f32 v9, s82, v9\n"
"v_mfma_f32_16x16x32_bf16 v[168:171], a[88:91], a[44:47], v[168:171]\n"
"v_mul_f32 v10, s82, v10\n"
"v_mul_f32 v11, s82, v11\n"
"v_mfma_f32_16x16x32_bf16 v[168:171], a[92:95], a[64:67], v[168:171]\n"
"ds_read_b64_tr_b16 a[114:115], v241 offset:4096\n"
"v_mfma_f32_16x16x32_bf16 v[172:175], a[96:99], a[4:7], 0\n"
"v_mul_f32 v28, s82, v28\n"
"v_mul_f32 v29, s82, v29\n"
"v_mfma_f32_16x16x32_bf16 v[172:175], a[100:103], a[24:27], v[172:175]\n"
"v_mul_f32 v30, s82, v30\n"
"v_mul_f32 v31, s82, v31\n"
"v_mfma_f32_16x16x32_bf16 v[172:175], a[104:107], a[44:47], v[172:175]\n"
"ds_read_b64_tr_b16 a[116:117], v242 offset:0\n"
"v_cvt_pk_bf16_f32 v208, v8, v9\n"
"v_mfma_f32_16x16x32_bf16 v[172:175], a[108:111], a[64:67], v[172:175]\n"
"v_cvt_pk_bf16_f32 v209, v10, v11\n"
"ds_read_b64_tr_b16 a[118:119], v242 offset:4096\n"
"v_cvt_pk_bf16_f32 v210, v28, v29\n"
"v_cvt_pk_bf16_f32 v211, v30, v31\n"
"ds_read_b64_tr_b16 a[120:121], v241 offset:512\n"
"v_pk_mul_f32 v[48:49], v[48:49], s[82:83] op_sel:[0,0] op_sel_hi:[1,0]\n"
"v_pk_mul_f32 v[50:51], v[50:51], s[82:83] op_sel:[0,0] op_sel_hi:[1,0]\n"
"ds_read_b64_tr_b16 a[122:123], v241 offset:4608\n"
"v_pk_mul_f32 v[68:69], v[68:69], s[82:83] op_sel:[0,0] op_sel_hi:[1,0]\n"
"v_pk_mul_f32 v[70:71], v[70:71], s[82:83] op_sel:[0,0] op_sel_hi:[1,0]\n"
"ds_read_b64_tr_b16 a[124:125], v242 offset:512\n"
"s_waitcnt lgkmcnt(0)\n"
"v_accvgpr_write_b32 a8, v208\n"
"ds_read_b64_tr_b16 a[126:127], v242 offset:4608\n"
"ds_write_b128 v217, a[12:15] offset:16384\n"
"v_accvgpr_write_b32 a9, v209\n"
"ds_read_b64_tr_b16 a[128:129], v241 offset:1024\n"
"v_accvgpr_write_b32 a10, v210\n"
"v_accvgpr_write_b32 a11, v211\n"
"ds_read_b64_tr_b16 a[130:131], v241 offset:5120\n"
"v_cvt_pk_bf16_f32 v212, v48, v49\n"
"v_cvt_pk_bf16_f32 v213, v50, v51\n"
"ds_read_b64_tr_b16 a[132:133], v242 offset:1024\n"
"v_cvt_pk_bf16_f32 v214, v68, v69\n"
"v_cvt_pk_bf16_f32 v215, v70, v71\n"
"ds_read_b64_tr_b16 a[134:135], v242 offset:5120\n"
"v_pk_mul_f32 v[88:89], v[88:89], s[82:83] op_sel:[0,0] op_sel_hi:[1,0]\n"
"ds_write_b128 v217, a[32:35] offset:17408\n"
"ds_read_b64_tr_b16 a[136:137], v241 offset:1536\n"
"v_pk_mul_f32 v[90:91], v[90:91], s[82:83] op_sel:[0,0] op_sel_hi:[1,0]\n"
"v_pk_mul_f32 v[108:109], v[108:109], s[82:83] op_sel:[0,0] op_sel_hi:[1,0]\n"
"ds_read_b64_tr_b16 a[138:139], v241 offset:5632\n"
"v_pk_mul_f32 v[110:111], v[110:111], s[82:83] op_sel:[0,0] op_sel_hi:[1,0]\n"
"v_accvgpr_write_b32 a28, v212\n"
"ds_read_b64_tr_b16 a[140:141], v242 offset:1536\n"
"v_accvgpr_write_b32 a29, v213\n"
"v_accvgpr_write_b32 a30, v214\n"
"ds_read_b64_tr_b16 a[142:143], v242 offset:5632\n"
"v_accvgpr_write_b32 a31, v215\n"
"v_cvt_pk_bf16_f32 v208, v88, v89\n"
"ds_write_b128 v217, a[52:55] offset:24576\n"
"v_cvt_pk_bf16_f32 v209, v90, v91\n"
"v_cvt_pk_bf16_f32 v210, v108, v109\n"
"v_cvt_pk_bf16_f32 v211, v110, v111\n"
"v_pk_mul_f32 v[128:129], v[128:129], s[82:83] op_sel:[0,0] op_sel_hi:[1,0]\n"
"v_pk_mul_f32 v[130:131], v[130:131], s[82:83] op_sel:[0,0] op_sel_hi:[1,0]\n"
"v_pk_mul_f32 v[148:149], v[148:149], s[82:83] op_sel:[0,0] op_sel_hi:[1,0]\n"
"v_pk_mul_f32 v[150:151], v[150:151], s[82:83] op_sel:[0,0] op_sel_hi:[1,0]\n"
"v_accvgpr_write_b32 a48, v208\n"
"ds_write_b128 v217, a[72:75] offset:25600\n"
"v_accvgpr_write_b32 a49, v209\n"
"v_accvgpr_write_b32 a50, v210\n"
"v_accvgpr_write_b32 a51, v211\n"
"v_cvt_pk_bf16_f32 v212, v128, v129\n"
"v_cvt_pk_bf16_f32 v213, v130, v131\n"
"v_cvt_pk_bf16_f32 v214, v148, v149\n"
"v_cvt_pk_bf16_f32 v215, v150, v151\n"
"v_accvgpr_write_b32 a68, v212\n"
"v_accvgpr_write_b32 a69, v213\n"
"v_accvgpr_write_b32 a70, v214\n"
"v_accvgpr_write_b32 a71, v215\n"
"s_nop 1\n"
"v_mfma_f32_16x16x32_bf16 v[176:179], a[80:83], a[8:11], 0\n"
"v_max3_f32 v254, v160, v161, v162\n"
"v_max3_f32 v255, v163, v164, v165\n"
"v_mfma_f32_16x16x32_bf16 v[176:179], a[84:87], a[28:31], v[176:179]\n"
"v_max3_f32 v254, v254, v166, v167\n"
"v_max_f32 v254, v254, v255\n"
"v_mfma_f32_16x16x32_bf16 v[176:179], a[88:91], a[48:51], v[176:179]\n"
"v_mov_b32 v255, v254\n"
"v_mfma_f32_16x16x32_bf16 v[176:179], a[92:95], a[68:71], v[176:179]\n"
"s_nop 1\n"
"v_mfma_f32_16x16x32_bf16 v[180:183], a[96:99], a[8:11], 0\n"
"v_permlane32_swap_b32 v255, v254\n"
"v_max_f32 v254, v254, v255\n"
"v_mfma_f32_16x16x32_bf16 v[180:183], a[100:103], a[28:31], v[180:183]\n"
"v_mov_b32 v255, v254\n"
"v_mfma_f32_16x16x32_bf16 v[180:183], a[104:107], a[48:51], v[180:183]\n"
"s_nop 1\n"
"v_mfma_f32_16x16x32_bf16 v[180:183], a[108:111], a[68:71], v[180:183]\n"
"v_permlane16_swap_b32 v255, v254\n"
"v_max_f32 v254, v254, v255\n"
"v_sub_f32 v220, 0, v254\n"
"v_sub_f32 v221, 0, v254\n"
"v_pk_add_f32 v[160:161], v[160:161], v[220:221]\n"
"v_pk_add_f32 v[162:163], v[162:163], v[220:221]\n"
"v_pk_add_f32 v[164:165], v[164:165], v[220:221]\n"
"v_pk_add_f32 v[166:167], v[166:167], v[220:221]\n"
"v_mov_b64 v[222:223], v[220:221]\n"
"v_pk_mul_f32 v[12:13], v[12:13], s[82:83] op_sel:[0,0] op_sel_hi:[1,0]\n"
"v_pk_mul_f32 v[14:15], v[14:15], s[82:83] op_sel:[0,0] op_sel_hi:[1,0]\n"
"v_pk_mul_f32 v[32:33], v[32:33], s[82:83] op_sel:[0,0] op_sel_hi:[1,0]\n"
"v_pk_mul_f32 v[34:35], v[34:35], s[82:83] op_sel:[0,0] op_sel_hi:[1,0]\n"
"v_cvt_pk_bf16_f32 v208, v12, v13\n"
"v_cvt_pk_bf16_f32 v209, v14, v15\n"
"v_cvt_pk_bf16_f32 v210, v32, v33\n"
"v_cvt_pk_bf16_f32 v211, v34, v35\n"
"v_pk_mul_f32 v[52:53], v[52:53], s[82:83] op_sel:[0,0] op_sel_hi:[1,0]\n"
"v_pk_mul_f32 v[54:55], v[54:55], s[82:83] op_sel:[0,0] op_sel_hi:[1,0]\n"
"v_pk_mul_f32 v[72:73], v[72:73], s[82:83] op_sel:[0,0] op_sel_hi:[1,0]\n"
"v_pk_mul_f32 v[74:75], v[74:75], s[82:83] op_sel:[0,0] op_sel_hi:[1,0]\n"
"s_waitcnt lgkmcnt(0)\n"
"v_accvgpr_write_b32 a12, v208\n"
"ds_write_b128 v217, a[16:19] offset:49152\n"
"v_accvgpr_write_b32 a13, v209\n"
"v_accvgpr_write_b32 a14, v210\n"
"v_accvgpr_write_b32 a15, v211\n"
"v_cvt_pk_bf16_f32 v212, v52, v53\n"
"v_cvt_pk_bf16_f32 v213, v54, v55\n"
"v_cvt_pk_bf16_f32 v214, v72, v73\n"
"v_cvt_pk_bf16_f32 v215, v74, v75\n"
"v_pk_mul_f32 v[92:93], v[92:93], s[82:83] op_sel:[0,0] op_sel_hi:[1,0]\n"
"ds_write_b128 v217, a[36:39] offset:50176\n"
"v_pk_mul_f32 v[94:95], v[94:95], s[82:83] op_sel:[0,0] op_sel_hi:[1,0]\n"
"v_pk_mul_f32 v[112:113], v[112:113], s[82:83] op_sel:[0,0] op_sel_hi:[1,0]\n"
"v_pk_mul_f32 v[114:115], v[114:115], s[82:83] op_sel:[0,0] op_sel_hi:[1,0]\n"
"v_accvgpr_write_b32 a32, v212\n"
"v_accvgpr_write_b32 a33, v213\n"
"v_accvgpr_write_b32 a34, v214\n"
"v_accvgpr_write_b32 a35, v215\n"
"v_cvt_pk_bf16_f32 v208, v92, v93\n"
"ds_write_b128 v217, a[56:59] offset:57344\n"
"v_cvt_pk_bf16_f32 v209, v94, v95\n"
"v_cvt_pk_bf16_f32 v210, v112, v113\n"
"v_cvt_pk_bf16_f32 v211, v114, v115\n"
"v_pk_mul_f32 v[132:133], v[132:133], s[82:83] op_sel:[0,0] op_sel_hi:[1,0]\n"
"v_pk_mul_f32 v[134:135], v[134:135], s[82:83] op_sel:[0,0] op_sel_hi:[1,0]\n"
"v_pk_mul_f32 v[152:153], v[152:153], s[82:83] op_sel:[0,0] op_sel_hi:[1,0]\n"
"v_pk_mul_f32 v[154:155], v[154:155], s[82:83] op_sel:[0,0] op_sel_hi:[1,0]\n"
"v_accvgpr_write_b32 a52, v208\n"
"ds_write_b128 v217, a[76:79] offset:58368\n"
"v_accvgpr_write_b32 a53, v209\n"
"v_accvgpr_write_b32 a54, v210\n"
"v_accvgpr_write_b32 a55, v211\n"
"v_cvt_pk_bf16_f32 v212, v132, v133\n"
"v_cvt_pk_bf16_f32 v213, v134, v135\n"
"v_cvt_pk_bf16_f32 v214, v152, v153\n"
"v_cvt_pk_bf16_f32 v215, v154, v155\n"
"v_accvgpr_write_b32 a72, v212\n"
"v_accvgpr_write_b32 a73, v213\n"
"v_accvgpr_write_b32 a74, v214\n"
"v_accvgpr_write_b32 a75, v215\n"
"s_nop 1\n"
"v_mfma_f32_16x16x32_bf16 v[184:187], a[80:83], a[12:15], 0\n"
"v_max3_f32 v254, v168, v169, v170\n"
"v_max3_f32 v255, v171, v172, v173\n"
"v_mfma_f32_16x16x32_bf16 v[184:187], a[84:87], a[32:35], v[184:187]\n"
"v_max3_f32 v254, v254, v174, v175\n"
"v_max_f32 v254, v254, v255\n"
"v_mfma_f32_16x16x32_bf16 v[184:187], a[88:91], a[52:55], v[184:187]\n"
"v_mov_b32 v255, v254\n"
"v_mfma_f32_16x16x32_bf16 v[184:187], a[92:95], a[72:75], v[184:187]\n"
"s_nop 1\n"
"v_mfma_f32_16x16x32_bf16 v[188:191], a[96:99], a[12:15], 0\n"
"v_permlane32_swap_b32 v255, v254\n"
"v_max_f32 v254, v254, v255\n"
"v_mfma_f32_16x16x32_bf16 v[188:191], a[100:103], a[32:35], v[188:191]\n"
"v_mov_b32 v255, v254\n"
"v_mfma_f32_16x16x32_bf16 v[188:191], a[104:107], a[52:55], v[188:191]\n"
"s_nop 1\n"
"v_mfma_f32_16x16x32_bf16 v[188:191], a[108:111], a[72:75], v[188:191]\n"
"v_permlane16_swap_b32 v255, v254\n"
"v_max_f32 v254, v254, v255\n"
"v_sub_f32 v224, 0, v254\n"
"v_sub_f32 v225, 0, v254\n"
"v_pk_add_f32 v[168:169], v[168:169], v[224:225]\n"
"v_pk_add_f32 v[170:171], v[170:171], v[224:225]\n"
"v_pk_add_f32 v[172:173], v[172:173], v[224:225]\n"
"v_pk_add_f32 v[174:175], v[174:175], v[224:225]\n"
"v_mov_b64 v[226:227], v[224:225]\n"
"v_pk_mul_f32 v[16:17], v[16:17], s[82:83] op_sel:[0,0] op_sel_hi:[1,0]\n"
"v_pk_mul_f32 v[18:19], v[18:19], s[82:83] op_sel:[0,0] op_sel_hi:[1,0]\n"
"v_pk_mul_f32 v[36:37], v[36:37], s[82:83] op_sel:[0,0] op_sel_hi:[1,0]\n"
"v_pk_mul_f32 v[38:39], v[38:39], s[82:83] op_sel:[0,0] op_sel_hi:[1,0]\n"
"v_cvt_pk_bf16_f32 v208, v16, v17\n"
"v_cvt_pk_bf16_f32 v209, v18, v19\n"
"v_cvt_pk_bf16_f32 v210, v36, v37\n"
"v_cvt_pk_bf16_f32 v211, v38, v39\n"
"v_pk_mul_f32 v[56:57], v[56:57], s[82:83] op_sel:[0,0] op_sel_hi:[1,0]\n"
"v_pk_mul_f32 v[58:59], v[58:59], s[82:83] op_sel:[0,0] op_sel_hi:[1,0]\n"
"v_pk_mul_f32 v[76:77], v[76:77], s[82:83] op_sel:[0,0] op_sel_hi:[1,0]\n"
"v_pk_mul_f32 v[78:79], v[78:79], s[82:83] op_sel:[0,0] op_sel_hi:[1,0]\n"
"s_waitcnt lgkmcnt(0)\n"
"v_accvgpr_write_b32 a16, v208\n"
"v_accvgpr_write_b32 a17, v209\n"
"v_accvgpr_write_b32 a18, v210\n"
"v_accvgpr_write_b32 a19, v211\n"
"v_cvt_pk_bf16_f32 v212, v56, v57\n"
"v_cvt_pk_bf16_f32 v213, v58, v59\n"
"v_cvt_pk_bf16_f32 v214, v76, v77\n"
"v_cvt_pk_bf16_f32 v215, v78, v79\n"
"v_pk_mul_f32 v[96:97], v[96:97], s[82:83] op_sel:[0,0] op_sel_hi:[1,0]\n"
"v_pk_mul_f32 v[98:99], v[98:99], s[82:83] op_sel:[0,0] op_sel_hi:[1,0]\n"
"v_pk_mul_f32 v[116:117], v[116:117], s[82:83] op_sel:[0,0] op_sel_hi:[1,0]\n"
"v_pk_mul_f32 v[118:119], v[118:119], s[82:83] op_sel:[0,0] op_sel_hi:[1,0]\n"
"v_accvgpr_write_b32 a36, v212\n"
"v_accvgpr_write_b32 a37, v213\n"
"v_accvgpr_write_b32 a38, v214\n"
"v_accvgpr_write_b32 a39, v215\n"
"v_cvt_pk_bf16_f32 v208, v96, v97\n"
"v_cvt_pk_bf16_f32 v209, v98, v99\n"
"v_cvt_pk_bf16_f32 v210, v116, v117\n"
"v_cvt_pk_bf16_f32 v211, v118, v119\n"
"v_pk_mul_f32 v[136:137], v[136:137], s[82:83] op_sel:[0,0] op_sel_hi:[1,0]\n"
"v_pk_mul_f32 v[138:139], v[138:139], s[82:83] op_sel:[0,0] op_sel_hi:[1,0]\n"
"v_pk_mul_f32 v[156:157], v[156:157], s[82:83] op_sel:[0,0] op_sel_hi:[1,0]\n"
"v_pk_mul_f32 v[158:159], v[158:159], s[82:83] op_sel:[0,0] op_sel_hi:[1,0]\n"
"v_accvgpr_write_b32 a56, v208\n"
"v_accvgpr_write_b32 a57, v209\n"
"v_accvgpr_write_b32 a58, v210\n"
"v_accvgpr_write_b32 a59, v211\n"
"v_cvt_pk_bf16_f32 v212, v136, v137\n"
"v_cvt_pk_bf16_f32 v213, v138, v139\n"
"v_cvt_pk_bf16_f32 v214, v156, v157\n"
"v_cvt_pk_bf16_f32 v215, v158, v159\n"
"v_accvgpr_write_b32 a76, v212\n"
"v_accvgpr_write_b32 a77, v213\n"
"v_accvgpr_write_b32 a78, v214\n"
"v_accvgpr_write_b32 a79, v215\n"
"s_nop 1\n"
"v_mfma_f32_16x16x32_bf16 v[192:195], a[80:83], a[16:19], 0\n"
"v_max3_f32 v254, v176, v177, v178\n"
"v_max3_f32 v255, v179, v180, v181\n"
"v_mfma_f32_16x16x32_bf16 v[192:195], a[84:87], a[36:39], v[192:195]\n"
"v_max3_f32 v254, v254, v182, v183\n"
"v_max_f32 v254, v254, v255\n"
"v_mfma_f32_16x16x32_bf16 v[192:195], a[88:91], a[56:59], v[192:195]\n"
"v_mov_b32 v255, v254\n"
"v_mfma_f32_16x16x32_bf16 v[192:195], a[92:95], a[76:79], v[192:195]\n"
"s_nop 1\n"
"v_mfma_f32_16x16x32_bf16 v[196:199], a[96:99], a[16:19], 0\n"
"v_permlane32_swap_b32 v255, v254\n"
"v_max_f32 v254, v254, v255\n"
"v_mfma_f32_16x16x32_bf16 v[196:199], a[100:103], a[36:39], v[196:199]\n"
"v_mov_b32 v255, v254\n"
"v_mfma_f32_16x16x32_bf16 v[196:199], a[104:107], a[56:59], v[196:199]\n"
"s_nop 1\n"
"v_mfma_f32_16x16x32_bf16 v[196:199], a[108:111], a[76:79], v[196:199]\n"
"v_permlane16_swap_b32 v255, v254\n"
"v_max_f32 v254, v254, v255\n"
"v_sub_f32 v228, 0, v254\n"
"v_sub_f32 v229, 0, v254\n"
"v_pk_add_f32 v[176:177], v[176:177], v[228:229]\n"
"v_pk_add_f32 v[178:179], v[178:179], v[228:229]\n"
"v_pk_add_f32 v[180:181], v[180:181], v[228:229]\n"
"v_pk_add_f32 v[182:183], v[182:183], v[228:229]\n"
"v_mov_b64 v[230:231], v[228:229]\n"
"v_exp_f32 v160, v160\n"
"v_exp_f32 v161, v161\n"
"v_exp_f32 v162, v162\n"
"v_exp_f32 v163, v163\n"
"s_waitcnt vmcnt(12) lgkmcnt(0)\n"
"s_barrier\n"
"ds_read_b128 a[80:83], v240 offset:16384\n"
"ds_read_b128 a[84:87], v240 offset:16896\n"
"ds_read_b128 a[88:91], v240 offset:17408\n"
"ds_read_b128 a[92:95], v240 offset:17920\n"
"ds_read_b128 a[96:99], v240 offset:20480\n"
"ds_read_b128 a[100:103], v240 offset:20992\n"
"ds_read_b128 a[104:107], v240 offset:21504\n"
"ds_read_b128 a[108:111], v240 offset:22016\n"
"s_add_u32 m0, s77, 0\n"
"s_nop 0\n"
"buffer_load_dwordx4 v244, s[48:51], 0 offen nt lds\n"
"s_add_u32 m0, s77, 896\n"
"s_nop 0\n"
"buffer_load_dwordx4 v244, s[48:51], 0 offen offset:128 nt lds\n"
"s_add_u32 m0, s77, 8192\n"
"s_nop 0\n"
"buffer_load_dwordx4 v244, s[48:51], s68 offen nt lds\n"
"s_add_u32 m0, s77, 9088\n"
"s_nop 0\n"
"buffer_load_dwordx4 v244, s[48:51], s68 offen offset:128 nt lds\n"
"v_add_u32 v244, 8192, v244\n"
"v_exp_f32 v164, v164\n"
"v_exp_f32 v165, v165\n"
"v_exp_f32 v166, v166\n"
"v_exp_f32 v167, v167\n"
"v_max3_f32 v254, v184, v185, v186\n"
"v_max3_f32 v255, v187, v188, v189\n"
"v_max3_f32 v254, v254, v190, v191\n"
"v_max_f32 v254, v254, v255\n"
"v_mov_b32 v255, v254\n"
"s_nop 1\n"
"v_permlane32_swap_b32 v255, v254\n"
"v_max_f32 v254, v254, v255\n"
"v_mov_b32 v255, v254\n"
"s_nop 1\n"
"v_permlane16_swap_b32 v255, v254\n"
"v_max_f32 v254, v254, v255\n"
"v_sub_f32 v232, 0, v254\n"
"v_sub_f32 v233, 0, v254\n"
"v_pk_add_f32 v[184:185], v[184:185], v[232:233]\n"
"v_pk_add_f32 v[186:187], v[186:187], v[232:233]\n"
"v_pk_add_f32 v[188:189], v[188:189], v[232:233]\n"
"v_pk_add_f32 v[190:191], v[190:191], v[232:233]\n"
"v_mov_b64 v[234:235], v[232:233]\n"
"v_cvt_pk_bf16_f32 v200, v160, v161\n"
"v_cvt_pk_bf16_f32 v201, v162, v163\n"
"v_cvt_pk_bf16_f32 v202, v164, v165\n"
"v_cvt_pk_bf16_f32 v203, v166, v167\n"
"v_max3_f32 v254, v192, v193, v194\n"
"v_max3_f32 v255, v195, v196, v197\n"
"v_max3_f32 v254, v254, v198, v199\n"
"v_max_f32 v254, v254, v255\n"
"v_mov_b32 v255, v254\n"
"s_nop 1\n"
"v_permlane32_swap_b32 v255, v254\n"
"v_max_f32 v254, v254, v255\n"
"v_mov_b32 v255, v254\n"
"s_nop 1\n"
"v_permlane16_swap_b32 v255, v254\n"
"v_max_f32 v254, v254, v255\n"
"v_sub_f32 v236, 0, v254\n"
"v_sub_f32 v237, 0, v254\n"
"v_pk_add_f32 v[192:193], v[192:193], v[236:237]\n"
"v_pk_add_f32 v[194:195], v[194:195], v[236:237]\n"
"v_pk_add_f32 v[196:197], v[196:197], v[236:237]\n"
"v_pk_add_f32 v[198:199], v[198:199], v[236:237]\n"
"v_mov_b64 v[238:239], v[236:237]\n"
"s_waitcnt lgkmcnt(0)\n"
"s_nop 1\n"
"v_mfma_f32_16x16x32_bf16 v[160:163], a[80:83], a[0:3], v[220:223]\n"
"v_exp_f32 v168, v168\n"
"v_mfma_f32_16x16x32_bf16 v[160:163], a[84:87], a[20:23], v[160:163]\n"
"v_exp_f32 v169, v169\n"
"v_mfma_f32_16x16x32_bf16 v[160:163], a[88:91], a[40:43], v[160:163]\n"
"v_exp_f32 v170, v170\n"
"v_mfma_f32_16x16x32_bf16 v[160:163], a[92:95], a[60:63], v[160:163]\n"
"v_exp_f32 v171, v171\n"
"v_mfma_f32_16x16x32_bf16 v[164:167], a[96:99], a[0:3], v[220:223]\n"
"v_exp_f32 v172, v172\n"
"v_mfma_f32_16x16x32_bf16 v[164:167], a[100:103], a[20:23], v[164:167]\n"
"v_exp_f32 v173, v173\n"
"v_mfma_f32_16x16x32_bf16 v[164:167], a[104:107], a[40:43], v[164:167]\n"
"v_exp_f32 v174, v174\n"
"v_mfma_f32_16x16x32_bf16 v[164:167], a[108:111], a[60:63], v[164:167]\n"
"v_exp_f32 v175, v175\n"
"s_add_u32 s76, s80, 16384\n"
"v_cvt_pk_bf16_f32 v204, v168, v169\n"
"v_cvt_pk_bf16_f32 v205, v170, v171\n"
"v_cvt_pk_bf16_f32 v206, v172, v173\n"
"v_cvt_pk_bf16_f32 v207, v174, v175\n"
"s_nop 1\n"
"v_mfma_f32_16x16x32_bf16 v[168:171], a[80:83], a[4:7], v[224:227]\n"
"v_exp_f32 v176, v176\n"
"v_mfma_f32_16x16x32_bf16 v[168:171], a[84:87], a[24:27], v[168:171]\n"
"v_exp_f32 v177, v177\n"
"v_mfma_f32_16x16x32_bf16 v[168:171], a[88:91], a[44:47], v[168:171]\n"
"v_exp_f32 v178, v178\n"
"v_mfma_f32_16x16x32_bf16 v[168:171], a[92:95], a[64:67], v[168:171]\n"
"v_exp_f32 v179, v179\n"
"v_mfma_f32_16x16x32_bf16 v[172:175], a[96:99], a[4:7], v[224:227]\n"
"v_exp_f32 v180, v180\n"
"v_mfma_f32_16x16x32_bf16 v[172:175], a[100:103], a[24:27], v[172:175]\n"
"v_exp_f32 v181, v181\n"
"v_mfma_f32_16x16x32_bf16 v[172:175], a[104:107], a[44:47], v[172:175]\n"
"v_exp_f32 v182, v182\n"
"v_mfma_f32_16x16x32_bf16 v[172:175], a[108:111], a[64:67], v[172:175]\n"
"v_exp_f32 v183, v183\n"
"v_add_u32 v241, 16384, v241\n"
"v_cvt_pk_bf16_f32 v208, v176, v177\n"
"v_cvt_pk_bf16_f32 v209, v178, v179\n"
"v_cvt_pk_bf16_f32 v210, v180, v181\n"
"v_cvt_pk_bf16_f32 v211, v182, v183\n"
"s_nop 1\n"
"v_mfma_f32_16x16x32_bf16 v[176:179], a[80:83], a[8:11], v[228:231]\n"
"v_exp_f32 v184, v184\n"
"v_mfma_f32_16x16x32_bf16 v[176:179], a[84:87], a[28:31], v[176:179]\n"
"v_exp_f32 v185, v185\n"
"v_mfma_f32_16x16x32_bf16 v[176:179], a[88:91], a[48:51], v[176:179]\n"
"v_exp_f32 v186, v186\n"
"v_mfma_f32_16x16x32_bf16 v[176:179], a[92:95], a[68:71], v[176:179]\n"
"v_exp_f32 v187, v187\n"
"v_mfma_f32_16x16x32_bf16 v[180:183], a[96:99], a[8:11], v[228:231]\n"
"v_exp_f32 v188, v188\n"
"v_mfma_f32_16x16x32_bf16 v[180:183], a[100:103], a[28:31], v[180:183]\n"
"v_exp_f32 v189, v189\n"
"v_mfma_f32_16x16x32_bf16 v[180:183], a[104:107], a[48:51], v[180:183]\n"
"v_exp_f32 v190, v190\n"
"v_mfma_f32_16x16x32_bf16 v[180:183], a[108:111], a[68:71], v[180:183]\n"
"v_exp_f32 v191, v191\n"
"v_add_u32 v242, 16384, v242\n"
"v_cvt_pk_bf16_f32 v212, v184, v185\n"
"v_cvt_pk_bf16_f32 v213, v186, v187\n"
"v_cvt_pk_bf16_f32 v214, v188, v189\n"
"v_cvt_pk_bf16_f32 v215, v190, v191\n"
"s_nop 1\n"
"v_mfma_f32_16x16x32_bf16 v[184:187], a[80:83], a[12:15], v[232:235]\n"
"v_exp_f32 v192, v192\n"
"v_mfma_f32_16x16x32_bf16 v[184:187], a[84:87], a[32:35], v[184:187]\n"
"v_exp_f32 v193, v193\n"
"v_mfma_f32_16x16x32_bf16 v[184:187], a[88:91], a[52:55], v[184:187]\n"
"v_exp_f32 v194, v194\n"
"v_mfma_f32_16x16x32_bf16 v[184:187], a[92:95], a[72:75], v[184:187]\n"
"v_exp_f32 v195, v195\n"
"v_mfma_f32_16x16x32_bf16 v[188:191], a[96:99], a[12:15], v[232:235]\n"
"v_exp_f32 v196, v196\n"
"v_mfma_f32_16x16x32_bf16 v[188:191], a[100:103], a[32:35], v[188:191]\n"
"v_exp_f32 v197, v197\n"
"v_mfma_f32_16x16x32_bf16 v[188:191], a[104:107], a[52:55], v[188:191]\n"
"v_exp_f32 v198, v198\n"
"v_mfma_f32_16x16x32_bf16 v[188:191], a[108:111], a[72:75], v[188:191]\n"
"v_exp_f32 v199, v199\n"
"s_nop 0\n"
"v_cvt_pk_bf16_f32 v216, v192, v193\n"
"v_cvt_pk_bf16_f32 v217, v194, v195\n"
"v_cvt_pk_bf16_f32 v218, v196, v197\n"
"v_cvt_pk_bf16_f32 v219, v198, v199\n"
"s_nop 1\n"
"v_mfma_f32_16x16x32_bf16 v[192:195], a[80:83], a[16:19], v[236:239]\n"
"v_exp_f32 v160, v160\n"
"v_mfma_f32_16x16x32_bf16 v[192:195], a[84:87], a[36:39], v[192:195]\n"
"v_exp_f32 v161, v161\n"
"v_mfma_f32_16x16x32_bf16 v[192:195], a[88:91], a[56:59], v[192:195]\n"
"v_exp_f32 v162, v162\n"
"v_mfma_f32_16x16x32_bf16 v[192:195], a[92:95], a[76:79], v[192:195]\n"
"v_exp_f32 v163, v163\n"
"v_mfma_f32_16x16x32_bf16 v[196:199], a[96:99], a[16:19], v[236:239]\n"
"v_exp_f32 v164, v164\n"
"v_mfma_f32_16x16x32_bf16 v[196:199], a[100:103], a[36:39], v[196:199]\n"
"v_exp_f32 v165, v165\n"
"v_mfma_f32_16x16x32_bf16 v[196:199], a[104:107], a[56:59], v[196:199]\n"
"s_waitcnt vmcnt(63) lgkmcnt(0)\n"
"s_barrier\n"
"v_mfma_f32_16x16x32_bf16 v[196:199], a[108:111], a[76:79], v[196:199]\n"
"ds_read_b128 a[80:83], v240 offset:32768\n"
"v_mfma_f32_16x16x32_bf16 v[0:3], a[112:115], v[200:203], 0\n"
"v_exp_f32 v166, v166\n"
"v_mfma_f32_16x16x32_bf16 v[20:23], a[116:119], v[200:203], 0\n"
"v_exp_f32 v167, v167\n"
"v_mfma_f32_16x16x32_bf16 v[40:43], a[120:123], v[200:203], 0\n"
"v_exp_f32 v168, v168\n"
"v_mfma_f32_16x16x32_bf16 v[60:63], a[124:127], v[200:203], 0\n"
"ds_read_b128 a[84:87], v240 offset:33280\n"
"v_mfma_f32_16x16x32_bf16 v[80:83], a[128:131], v[200:203], 0\n"
"v_exp_f32 v169, v169\n"
"v_mfma_f32_16x16x32_bf16 v[100:103], a[132:135], v[200:203], 0\n"
"v_exp_f32 v170, v170\n"
"v_mfma_f32_16x16x32_bf16 v[120:123], a[136:139], v[200:203], 0\n"
"v_exp_f32 v171, v171\n"
"v_mfma_f32_16x16x32_bf16 v[140:143], a[140:143], v[200:203], 0\n"
"ds_read_b128 a[88:91], v240 offset:33792\n"
"v_mfma_f32_16x16x32_bf16 a[144:147], a[164:167], v[200:203], 0\n"
"v_exp_f32 v172, v172\n"
"v_mfma_f32_16x16x32_bf16 v[4:7], a[112:115], v[204:207], 0\n"
"v_cvt_pk_bf16_f32 v200, v160, v161\n"
"v_cvt_pk_bf16_f32 v201, v162, v163\n"
"v_mfma_f32_16x16x32_bf16 v[24:27], a[116:119], v[204:207], 0\n"
"v_cvt_pk_bf16_f32 v202, v164, v165\n"
"v_cvt_pk_bf16_f32 v203, v166, v167\n"
"v_mfma_f32_16x16x32_bf16 v[44:47], a[120:123], v[204:207], 0\n"
"v_exp_f32 v173, v173\n"
"v_mfma_f32_16x16x32_bf16 v[64:67], a[124:127], v[204:207], 0\n"
"v_exp_f32 v174, v174\n"
"v_mfma_f32_16x16x32_bf16 v[84:87], a[128:131], v[204:207], 0\n"
"ds_read_b128 a[92:95], v240 offset:34304\n"
"v_mfma_f32_16x16x32_bf16 v[104:107], a[132:135], v[204:207], 0\n"
"v_exp_f32 v175, v175\n"
"v_mfma_f32_16x16x32_bf16 v[124:127], a[136:139], v[204:207], 0\n"
"v_exp_f32 v176, v176\n"
"v_mfma_f32_16x16x32_bf16 v[144:147], a[140:143], v[204:207], 0\n"
"v_exp_f32 v177, v177\n"
"v_mfma_f32_16x16x32_bf16 a[148:151], a[164:167], v[204:207], 0\n"
"ds_read_b128 a[96:99], v240 offset:36864\n"
"v_mfma_f32_16x16x32_bf16 v[8:11], a[112:115], v[208:211], 0\n"
"v_exp_f32 v178, v178\n"
"v_mfma_f32_16x16x32_bf16 v[28:31], a[116:119], v[208:211], 0\n"
"v_exp_f32 v179, v179\n"
"v_mfma_f32_16x16x32_bf16 v[48:51], a[120:123], v[208:211], 0\n"
"v_exp_f32 v180, v180\n"
"v_mfma_f32_16x16x32_bf16 v[68:71], a[124:127], v[208:211], 0\n"
"ds_read_b128 a[100:103], v240 offset:37376\n"
"v_mfma_f32_16x16x32_bf16 v[88:91], a[128:131], v[208:211], 0\n"
"v_exp_f32 v181, v181\n"
"v_mfma_f32_16x16x32_bf16 v[108:111], a[132:135], v[208:211], 0\n"
"v_cvt_pk_bf16_f32 v204, v168, v169\n"
"v_cvt_pk_bf16_f32 v205, v170, v171\n"
"v_mfma_f32_16x16x32_bf16 v[128:131], a[136:139], v[208:211], 0\n"
"v_cvt_pk_bf16_f32 v206, v172, v173\n"
"v_cvt_pk_bf16_f32 v207, v174, v175\n"
"v_mfma_f32_16x16x32_bf16 v[148:151], a[140:143], v[208:211], 0\n"
"v_exp_f32 v182, v182\n"
"v_mfma_f32_16x16x32_bf16 a[152:155], a[164:167], v[208:211], 0\n"
"v_exp_f32 v183, v183\n"
"v_mfma_f32_16x16x32_bf16 v[12:15], a[112:115], v[212:215], 0\n"
"ds_read_b128 a[104:107], v240 offset:37888\n"
"v_mfma_f32_16x16x32_bf16 v[32:35], a[116:119], v[212:215], 0\n"
"v_exp_f32 v184, v184\n"
"v_mfma_f32_16x16x32_bf16 v[52:55], a[120:123], v[212:215], 0\n"
"v_exp_f32 v185, v185\n"
"v_mfma_f32_16x16x32_bf16 v[72:75], a[124:127], v[212:215], 0\n"
"v_exp_f32 v186, v186\n"
"v_mfma_f32_16x16x32_bf16 v[92:95], a[128:131], v[212:215], 0\n"
"ds_read_b128 a[108:111], v240 offset:38400\n"
"v_mfma_f32_16x16x32_bf16 v[112:115], a[132:135], v[212:215], 0\n"
"v_exp_f32 v187, v187\n"
"v_mfma_f32_16x16x32_bf16 v[132:135], a[136:139], v[212:215], 0\n"
"v_exp_f32 v188, v188\n"
"v_mfma_f32_16x16x32_bf16 v[152:155], a[140:143], v[212:215], 0\n"
"v_exp_f32 v189, v189\n"
"v_mfma_f32_16x16x32_bf16 a[156:159], a[164:167], v[212:215], 0\n"
"v_exp_f32 v190, v190\n"
"v_mfma_f32_16x16x32_bf16 v[16:19], a[112:115], v[216:219], 0\n"
"v_cvt_pk_bf16_f32 v208, v176, v177\n"
"v_cvt_pk_bf16_f32 v209, v178, v179\n"
"v_mfma_f32_16x16x32_bf16 v[36:39], a[116:119], v[216:219], 0\n"
"v_cvt_pk_bf16_f32 v210, v180, v181\n"
"v_cvt_pk_bf16_f32 v211, v182, v183\n"
"v_mfma_f32_16x16x32_bf16 v[56:59], a[120:123], v[216:219], 0\n"
"v_exp_f32 v191, v191\n"
"v_mfma_f32_16x16x32_bf16 v[76:79], a[124:127], v[216:219], 0\n"
"v_exp_f32 v192, v192\n"
"v_mfma_f32_16x16x32_bf16 v[96:99], a[128:131], v[216:219], 0\n"
"v_exp_f32 v193, v193\n"
"v_mfma_f32_16x16x32_bf16 v[116:119], a[132:135], v[216:219], 0\n"
"v_cvt_pk_bf16_f32 v212, v184, v185\n"
"v_cvt_pk_bf16_f32 v213, v186, v187\n"
"v_mfma_f32_16x16x32_bf16 v[136:139], a[136:139], v[216:219], 0\n"
"v_mfma_f32_16x16x32_bf16 v[156:159], a[140:143], v[216:219], 0\n"
"v_mfma_f32_16x16x32_bf16 a[160:163], a[164:167], v[216:219], 0\n"
"s_mov_b32 s75, 13\n"
"s_mov_b32 s27, 49152\n"
"s_mov_b32 s28, 147456\n"
"s_mov_b32 s19, 32768\n"
"s_waitcnt lgkmcnt(0)\n"
"v_mfma_f32_16x16x32_bf16 v[160:163], a[80:83], a[0:3], v[220:223]\n"
"ds_read_b64_tr_b16 a[112:113], v241 offset:0\n"
"s_add_u32 m0, s80, 16384\n"
"v_mfma_f32_16x16x32_bf16 v[160:163], a[84:87], a[20:23], v[160:163]\n"
"v_exp_f32 v194, v194\n"
"v_mfma_f32_16x16x32_bf16 v[160:163], a[88:91], a[40:43], v[160:163]\n"
"ds_read_b64_tr_b16 a[114:115], v241 offset:4096\n"
"v_cvt_pk_bf16_f32 v214, v188, v189\n"
"v_mfma_f32_16x16x32_bf16 v[160:163], a[92:95], a[60:63], v[160:163]\n"
"v_exp_f32 v195, v195\n"
"v_mfma_f32_16x16x32_bf16 v[164:167], a[96:99], a[0:3], v[220:223]\n"
"ds_read_b64_tr_b16 a[116:117], v242 offset:0\n"
"v_cvt_pk_bf16_f32 v215, v190, v191\n"
"v_mfma_f32_16x16x32_bf16 v[164:167], a[100:103], a[20:23], v[164:167]\n"
"v_exp_f32 v196, v196\n"
"v_mfma_f32_16x16x32_bf16 v[164:167], a[104:107], a[40:43], v[164:167]\n"
"ds_read_b64_tr_b16 a[118:119], v242 offset:4096\n"
"v_cvt_pk_bf16_f32 v216, v192, v193\n"
"v_mfma_f32_16x16x32_bf16 v[164:167], a[108:111], a[60:63], v[164:167]\n"
"v_exp_f32 v197, v197\n"
"v_mfma_f32_16x16x32_bf16 v[168:171], a[80:83], a[4:7], v[224:227]\n"
"ds_read_b64_tr_b16 a[120:121], v241 offset:512\n"
"v_cvt_pk_bf16_f32 v217, v194, v195\n"
"v_mfma_f32_16x16x32_bf16 v[168:171], a[84:87], a[24:27], v[168:171]\n"
"v_exp_f32 v198, v198\n"
"v_mfma_f32_16x16x32_bf16 v[168:171], a[88:91], a[44:47], v[168:171]\n"
"ds_read_b64_tr_b16 a[122:123], v241 offset:4608\n"
"v_cvt_pk_bf16_f32 v218, v196, v197\n"
"v_mfma_f32_16x16x32_bf16 v[168:171], a[92:95], a[64:67], v[168:171]\n"
"v_exp_f32 v199, v199\n"
"v_mfma_f32_16x16x32_bf16 v[172:175], a[96:99], a[4:7], v[224:227]\n"
"ds_read_b64_tr_b16 a[124:125], v242 offset:512\n"
"v_cvt_pk_bf16_f32 v219, v198, v199\n"
"v_mfma_f32_16x16x32_bf16 v[172:175], a[100:103], a[24:27], v[172:175]\n"
"buffer_load_dwordx4 v243, s[44:47], 0 offen nt lds\n"
"v_mfma_f32_16x16x32_bf16 v[172:175], a[104:107], a[44:47], v[172:175]\n"
"ds_read_b64_tr_b16 a[126:127], v242 offset:4608\n"
"s_add_u32 m0, s80, 17280\n"
"v_mfma_f32_16x16x32_bf16 v[172:175], a[108:111], a[64:67], v[172:175]\n"
"ds_read_b64_tr_b16 a[128:129], v241 offset:1024\n"
"v_add_u32 v240, 49152, v245\n"
"v_mfma_f32_16x16x32_bf16 v[176:179], a[80:83], a[8:11], v[228:231]\n"
"v_exp_f32 v160, v160\n"
"v_mfma_f32_16x16x32_bf16 v[176:179], a[84:87], a[28:31], v[176:179]\n"
"ds_read_b64_tr_b16 a[130:131], v241 offset:5120\n"
"v_mfma_f32_16x16x32_bf16 v[176:179], a[88:91], a[48:51], v[176:179]\n"
"buffer_load_dwordx4 v243, s[44:47], 0 offen offset:128 nt lds\n"
"v_mfma_f32_16x16x32_bf16 v[176:179], a[92:95], a[68:71], v[176:179]\n"
"ds_read_b64_tr_b16 a[132:133], v242 offset:1024\n"
"s_add_u32 m0, s80, 24576\n"
"v_mfma_f32_16x16x32_bf16 v[180:183], a[96:99], a[8:11], v[228:231]\n"
"v_exp_f32 v161, v161\n"
"v_mfma_f32_16x16x32_bf16 v[180:183], a[100:103], a[28:31], v[180:183]\n"
"ds_read_b64_tr_b16 a[134:135], v242 offset:5120\n"
"v_mfma_f32_16x16x32_bf16 v[180:183], a[104:107], a[48:51], v[180:183]\n"
"v_exp_f32 v162, v162\n"
"v_mfma_f32_16x16x32_bf16 v[180:183], a[108:111], a[68:71], v[180:183]\n"
"buffer_load_dwordx4 v243, s[44:47], s68 offen nt lds\n"
"v_mfma_f32_16x16x32_bf16 v[184:187], a[80:83], a[12:15], v[232:235]\n"
"ds_read_b64_tr_b16 a[136:137], v241 offset:1536\n"
"s_add_u32 m0, s80, 25472\n"
"v_mfma_f32_16x16x32_bf16 v[184:187], a[84:87], a[32:35], v[184:187]\n"
"v_exp_f32 v163, v163\n"
"v_mfma_f32_16x16x32_bf16 v[184:187], a[88:91], a[52:55], v[184:187]\n"
"ds_read_b64_tr_b16 a[138:139], v241 offset:5632\n"
"v_mfma_f32_16x16x32_bf16 v[184:187], a[92:95], a[72:75], v[184:187]\n"
"v_exp_f32 v164, v164\n"
"v_mfma_f32_16x16x32_bf16 v[188:191], a[96:99], a[12:15], v[232:235]\n"
"buffer_load_dwordx4 v243, s[44:47], s68 offen offset:128 nt lds\n"
"v_mfma_f32_16x16x32_bf16 v[188:191], a[100:103], a[32:35], v[188:191]\n"
"ds_read_b64_tr_b16 a[140:141], v242 offset:1536\n"
"v_mfma_f32_16x16x32_bf16 v[188:191], a[104:107], a[52:55], v[188:191]\n"
"v_exp_f32 v165, v165\n"
"v_mfma_f32_16x16x32_bf16 v[188:191], a[108:111], a[72:75], v[188:191]\n"
"ds_read_b64_tr_b16 a[142:143], v242 offset:5632\n"
"v_mfma_f32_16x16x32_bf16 v[192:195], a[80:83], a[16:19], v[236:239]\n"
"v_exp_f32 v166, v166\n"
"v_mfma_f32_16x16x32_bf16 v[192:195], a[84:87], a[36:39], v[192:195]\n"
"v_exp_f32 v167, v167\n"
"v_mfma_f32_16x16x32_bf16 v[192:195], a[88:91], a[56:59], v[192:195]\n"
"v_exp_f32 v168, v168\n"
"v_mfma_f32_16x16x32_bf16 v[192:195], a[92:95], a[76:79], v[192:195]\n"
"v_mfma_f32_16x16x32_bf16 v[196:199], a[96:99], a[16:19], v[236:239]\n"
"v_exp_f32 v169, v169\n"
"v_mfma_f32_16x16x32_bf16 v[196:199], a[100:103], a[36:39], v[196:199]\n"
"v_mfma_f32_16x16x32_bf16 v[196:199], a[104:107], a[56:59], v[196:199]\n"
"v_exp_f32 v170, v170\n"
"v_mfma_f32_16x16x32_bf16 v[196:199], a[108:111], a[76:79], v[196:199]\n"
"s_waitcnt vmcnt(12) lgkmcnt(0)\n"
"s_barrier\n"
"v_mfma_f32_16x16x32_bf16 v[0:3], a[112:115], v[200:203], v[0:3]\n"
"ds_read_b128 a[80:83], v240 offset:0\n"
"s_add_u32 m0, s80, 147456\n"
"v_mfma_f32_16x16x32_bf16 v[20:23], a[116:119], v[200:203], v[20:23]\n"
"v_exp_f32 v171, v171\n"
"v_mfma_f32_16x16x32_bf16 v[40:43], a[120:123], v[200:203], v[40:43]\n"
"buffer_load_dwordx4 v244, s[48:51], 0 offen nt lds\n"
"v_mfma_f32_16x16x32_bf16 v[60:63], a[124:127], v[200:203], v[60:63]\n"
"v_exp_f32 v172, v172\n"
"v_mfma_f32_16x16x32_bf16 v[80:83], a[128:131], v[200:203], v[80:83]\n"
"ds_read_b128 a[84:87], v240 offset:512\n"
"s_add_u32 m0, s80, 148352\n"
"v_mfma_f32_16x16x32_bf16 v[100:103], a[132:135], v[200:203], v[100:103]\n"
"v_exp_f32 v173, v173\n"
"v_mfma_f32_16x16x32_bf16 v[120:123], a[136:139], v[200:203], v[120:123]\n"
"v_exp_f32 v174, v174\n"
"v_mfma_f32_16x16x32_bf16 v[140:143], a[140:143], v[200:203], v[140:143]\n"
"v_exp_f32 v175, v175\n"
"v_mfma_f32_16x16x32_bf16 a[144:147], a[164:167], v[200:203], a[144:147]\n"
"buffer_load_dwordx4 v244, s[48:51], 0 offen offset:128 nt lds\n"
"v_mfma_f32_16x16x32_bf16 v[4:7], a[112:115], v[204:207], v[4:7]\n"
"ds_read_b128 a[88:91], v240 offset:1024\n"
"s_add_u32 m0, s80, 155648\n"
"v_mfma_f32_16x16x32_bf16 v[24:27], a[116:119], v[204:207], v[24:27]\n"
"v_cvt_pk_bf16_f32 v200, v160, v161\n"
"v_cvt_pk_bf16_f32 v201, v162, v163\n"
"v_mfma_f32_16x16x32_bf16 v[44:47], a[120:123], v[204:207], v[44:47]\n"
"v_exp_f32 v176, v176\n"
"v_mfma_f32_16x16x32_bf16 v[64:67], a[124:127], v[204:207], v[64:67]\n"
"v_exp_f32 v177, v177\n"
"v_mfma_f32_16x16x32_bf16 v[84:87], a[128:131], v[204:207], v[84:87]\n"
"ds_read_b128 a[92:95], v240 offset:1536\n"
"v_cvt_pk_bf16_f32 v202, v164, v165\n"
"v_mfma_f32_16x16x32_bf16 v[104:107], a[132:135], v[204:207], v[104:107]\n"
"buffer_load_dwordx4 v244, s[48:51], s68 offen nt lds\n"
"v_mfma_f32_16x16x32_bf16 v[124:127], a[136:139], v[204:207], v[124:127]\n"
"s_add_u32 m0, s80, 156544\n"
"v_cvt_pk_bf16_f32 v203, v166, v167\n"
"v_mfma_f32_16x16x32_bf16 v[144:147], a[140:143], v[204:207], v[144:147]\n"
"v_exp_f32 v178, v178\n"
"v_mfma_f32_16x16x32_bf16 a[148:151], a[164:167], v[204:207], a[148:151]\n"
"ds_read_b128 a[96:99], v240 offset:4096\n"
"v_add_u32 v243, s85, v243\n"
"v_mfma_f32_16x16x32_bf16 v[8:11], a[112:115], v[208:211], v[8:11]\n"
"v_exp_f32 v179, v179\n"
"v_mfma_f32_16x16x32_bf16 v[28:31], a[116:119], v[208:211], v[28:31]\n"
"v_cvt_pk_bf16_f32 v204, v168, v169\n"
"v_cvt_pk_bf16_f32 v205, v170, v171\n"
"v_mfma_f32_16x16x32_bf16 v[48:51], a[120:123], v[208:211], v[48:51]\n"
"buffer_load_dwordx4 v244, s[48:51], s68 offen offset:128 nt lds\n"
"v_mfma_f32_16x16x32_bf16 v[68:71], a[124:127], v[208:211], v[68:71]\n"
"ds_read_b128 a[100:103], v240 offset:4608\n"
"v_cvt_pk_bf16_f32 v206, v172, v173\n"
"v_mfma_f32_16x16x32_bf16 v[88:91], a[128:131], v[208:211], v[88:91]\n"
"v_exp_f32 v180, v180\n"
"v_mfma_f32_16x16x32_bf16 v[108:111], a[132:135], v[208:211], v[108:111]\n"
"v_cvt_pk_bf16_f32 v207, v174, v175\n"
"v_add_u32 v244, s85, v244\n"
"v_mfma_f32_16x16x32_bf16 v[128:131], a[136:139], v[208:211], v[128:131]\n"
"v_exp_f32 v181, v181\n"
"v_mfma_f32_16x16x32_bf16 v[148:151], a[140:143], v[208:211], v[148:151]\n"
"ds_read_b128 a[104:107], v240 offset:5120\n"
"v_add_u32 v241, 81920, v246\n"
"v_mfma_f32_16x16x32_bf16 a[152:155], a[164:167], v[208:211], a[152:155]\n"
"v_exp_f32 v182, v182\n"
"v_mfma_f32_16x16x32_bf16 v[12:15], a[112:115], v[212:215], v[12:15]\n"
"v_cvt_pk_bf16_f32 v208, v176, v177\n"
"v_cvt_pk_bf16_f32 v209, v178, v179\n"
"v_mfma_f32_16x16x32_bf16 v[32:35], a[116:119], v[212:215], v[32:35]\n"
"v_exp_f32 v183, v183\n"
"v_mfma_f32_16x16x32_bf16 v[52:55], a[120:123], v[212:215], v[52:55]\n"
"ds_read_b128 a[108:111], v240 offset:5632\n"
"v_cvt_pk_bf16_f32 v210, v180, v181\n"
"v_mfma_f32_16x16x32_bf16 v[72:75], a[124:127], v[212:215], v[72:75]\n"
"v_exp_f32 v184, v184\n"
"v_mfma_f32_16x16x32_bf16 v[92:95], a[128:131], v[212:215], v[92:95]\n"
"v_cvt_pk_bf16_f32 v211, v182, v183\n"
"v_xor_b32 v242, 32, v241\n"
"v_mfma_f32_16x16x32_bf16 v[112:115], a[132:135], v[212:215], v[112:115]\n"
"v_exp_f32 v185, v185\n"
"v_mfma_f32_16x16x32_bf16 v[132:135], a[136:139], v[212:215], v[132:135]\n"
"v_exp_f32 v186, v186\n"
"v_mfma_f32_16x16x32_bf16 v[152:155], a[140:143], v[212:215], v[152:155]\n"
"v_exp_f32 v187, v187\n"
"v_mfma_f32_16x16x32_bf16 a[156:159], a[164:167], v[212:215], a[156:159]\n"
"v_mfma_f32_16x16x32_bf16 v[16:19], a[112:115], v[216:219], v[16:19]\n"
"v_exp_f32 v188, v188\n"
"v_mfma_f32_16x16x32_bf16 v[36:39], a[116:119], v[216:219], v[36:39]\n"
"v_exp_f32 v189, v189\n"
"v_mfma_f32_16x16x32_bf16 v[56:59], a[120:123], v[216:219], v[56:59]\n"
"v_exp_f32 v190, v190\n"
"v_mfma_f32_16x16x32_bf16 v[76:79], a[124:127], v[216:219], v[76:79]\n"
"v_cvt_pk_bf16_f32 v212, v184, v185\n"
"v_cvt_pk_bf16_f32 v213, v186, v187\n"
"v_mfma_f32_16x16x32_bf16 v[96:99], a[128:131], v[216:219], v[96:99]\n"
"v_exp_f32 v191, v191\n"
"v_mfma_f32_16x16x32_bf16 v[116:119], a[132:135], v[216:219], v[116:119]\n"
"v_exp_f32 v192, v192\n"
"v_mfma_f32_16x16x32_bf16 v[136:139], a[136:139], v[216:219], v[136:139]\n"
"v_mfma_f32_16x16x32_bf16 v[156:159], a[140:143], v[216:219], v[156:159]\n"
"v_exp_f32 v193, v193\n"
"v_mfma_f32_16x16x32_bf16 a[160:163], a[164:167], v[216:219], a[160:163]\n"
"s_waitcnt lgkmcnt(0)\n"
"v_mfma_f32_16x16x32_bf16 v[160:163], a[80:83], a[0:3], v[220:223]\n"
"ds_read_b64_tr_b16 a[112:113], v241 offset:0\n"
"s_add_u32 m0, s80, 32768\n"
"v_mfma_f32_16x16x32_bf16 v[160:163], a[84:87], a[20:23], v[160:163]\n"
"v_exp_f32 v194, v194\n"
"v_mfma_f32_16x16x32_bf16 v[160:163], a[88:91], a[40:43], v[160:163]\n"
"ds_read_b64_tr_b16 a[114:115], v241 offset:4096\n"
"v_cvt_pk_bf16_f32 v214, v188, v189\n"
"v_mfma_f32_16x16x32_bf16 v[160:163], a[92:95], a[60:63], v[160:163]\n"
"v_exp_f32 v195, v195\n"
"v_mfma_f32_16x16x32_bf16 v[164:167], a[96:99], a[0:3], v[220:223]\n"
"ds_read_b64_tr_b16 a[116:117], v242 offset:0\n"
"v_cvt_pk_bf16_f32 v215, v190, v191\n"
"v_mfma_f32_16x16x32_bf16 v[164:167], a[100:103], a[20:23], v[164:167]\n"
"v_exp_f32 v196, v196\n"
"v_mfma_f32_16x16x32_bf16 v[164:167], a[104:107], a[40:43], v[164:167]\n"
"ds_read_b64_tr_b16 a[118:119], v242 offset:4096\n"
"v_cvt_pk_bf16_f32 v216, v192, v193\n"
"v_mfma_f32_16x16x32_bf16 v[164:167], a[108:111], a[60:63], v[164:167]\n"
"v_exp_f32 v197, v197\n"
"v_mfma_f32_16x16x32_bf16 v[168:171], a[80:83], a[4:7], v[224:227]\n"
"ds_read_b64_tr_b16 a[120:121], v241 offset:512\n"
"v_cvt_pk_bf16_f32 v217, v194, v195\n"
"v_mfma_f32_16x16x32_bf16 v[168:171], a[84:87], a[24:27], v[168:171]\n"
"v_exp_f32 v198, v198\n"
"v_mfma_f32_16x16x32_bf16 v[168:171], a[88:91], a[44:47], v[168:171]\n"
"ds_read_b64_tr_b16 a[122:123], v241 offset:4608\n"
"v_cvt_pk_bf16_f32 v218, v196, v197\n"
"v_mfma_f32_16x16x32_bf16 v[168:171], a[92:95], a[64:67], v[168:171]\n"
"v_exp_f32 v199, v199\n"
"v_mfma_f32_16x16x32_bf16 v[172:175], a[96:99], a[4:7], v[224:227]\n"
"ds_read_b64_tr_b16 a[124:125], v242 offset:512\n"
"v_cvt_pk_bf16_f32 v219, v198, v199\n"
"v_mfma_f32_16x16x32_bf16 v[172:175], a[100:103], a[24:27], v[172:175]\n"
"buffer_load_dwordx4 v243, s[44:47], 0 offen nt lds\n"
"v_mfma_f32_16x16x32_bf16 v[172:175], a[104:107], a[44:47], v[172:175]\n"
"ds_read_b64_tr_b16 a[126:127], v242 offset:4608\n"
"s_add_u32 m0, s80, 33664\n"
"v_mfma_f32_16x16x32_bf16 v[172:175], a[108:111], a[64:67], v[172:175]\n"
"ds_read_b64_tr_b16 a[128:129], v241 offset:1024\n"
"v_add_u32 v240, 65536, v245\n"
"v_mfma_f32_16x16x32_bf16 v[176:179], a[80:83], a[8:11], v[228:231]\n"
"v_exp_f32 v160, v160\n"
"v_mfma_f32_16x16x32_bf16 v[176:179], a[84:87], a[28:31], v[176:179]\n"
"ds_read_b64_tr_b16 a[130:131], v241 offset:5120\n"
"v_mfma_f32_16x16x32_bf16 v[176:179], a[88:91], a[48:51], v[176:179]\n"
"buffer_load_dwordx4 v243, s[44:47], 0 offen offset:128 nt lds\n"
"v_mfma_f32_16x16x32_bf16 v[176:179], a[92:95], a[68:71], v[176:179]\n"
"ds_read_b64_tr_b16 a[132:133], v242 offset:1024\n"
"s_add_u32 m0, s80, 40960\n"
"v_mfma_f32_16x16x32_bf16 v[180:183], a[96:99], a[8:11], v[228:231]\n"
"v_exp_f32 v161, v161\n"
"v_mfma_f32_16x16x32_bf16 v[180:183], a[100:103], a[28:31], v[180:183]\n"
"ds_read_b64_tr_b16 a[134:135], v242 offset:5120\n"
"v_mfma_f32_16x16x32_bf16 v[180:183], a[104:107], a[48:51], v[180:183]\n"
"v_exp_f32 v162, v162\n"
"v_mfma_f32_16x16x32_bf16 v[180:183], a[108:111], a[68:71], v[180:183]\n"
"buffer_load_dwordx4 v243, s[44:47], s68 offen nt lds\n"
"v_mfma_f32_16x16x32_bf16 v[184:187], a[80:83], a[12:15], v[232:235]\n"
"ds_read_b64_tr_b16 a[136:137], v241 offset:1536\n"
"s_add_u32 m0, s80, 41856\n"
"v_mfma_f32_16x16x32_bf16 v[184:187], a[84:87], a[32:35], v[184:187]\n"
"v_exp_f32 v163, v163\n"
"v_mfma_f32_16x16x32_bf16 v[184:187], a[88:91], a[52:55], v[184:187]\n"
"ds_read_b64_tr_b16 a[138:139], v241 offset:5632\n"
"v_mfma_f32_16x16x32_bf16 v[184:187], a[92:95], a[72:75], v[184:187]\n"
"v_exp_f32 v164, v164\n"
"v_mfma_f32_16x16x32_bf16 v[188:191], a[96:99], a[12:15], v[232:235]\n"
"buffer_load_dwordx4 v243, s[44:47], s68 offen offset:128 nt lds\n"
"v_mfma_f32_16x16x32_bf16 v[188:191], a[100:103], a[32:35], v[188:191]\n"
"ds_read_b64_tr_b16 a[140:141], v242 offset:1536\n"
"v_mfma_f32_16x16x32_bf16 v[188:191], a[104:107], a[52:55], v[188:191]\n"
"v_exp_f32 v165, v165\n"
"v_mfma_f32_16x16x32_bf16 v[188:191], a[108:111], a[72:75], v[188:191]\n"
"ds_read_b64_tr_b16 a[142:143], v242 offset:5632\n"
"v_mfma_f32_16x16x32_bf16 v[192:195], a[80:83], a[16:19], v[236:239]\n"
"v_exp_f32 v166, v166\n"
"v_mfma_f32_16x16x32_bf16 v[192:195], a[84:87], a[36:39], v[192:195]\n"
"v_exp_f32 v167, v167\n"
"v_mfma_f32_16x16x32_bf16 v[192:195], a[88:91], a[56:59], v[192:195]\n"
"v_exp_f32 v168, v168\n"
"v_mfma_f32_16x16x32_bf16 v[192:195], a[92:95], a[76:79], v[192:195]\n"
"v_mfma_f32_16x16x32_bf16 v[196:199], a[96:99], a[16:19], v[236:239]\n"
"v_exp_f32 v169, v169\n"
"v_mfma_f32_16x16x32_bf16 v[196:199], a[100:103], a[36:39], v[196:199]\n"
"v_mfma_f32_16x16x32_bf16 v[196:199], a[104:107], a[56:59], v[196:199]\n"
"v_exp_f32 v170, v170\n"
"v_mfma_f32_16x16x32_bf16 v[196:199], a[108:111], a[76:79], v[196:199]\n"
"s_waitcnt vmcnt(63) lgkmcnt(0)\n"
"s_barrier\n"
"v_mfma_f32_16x16x32_bf16 v[0:3], a[112:115], v[200:203], v[0:3]\n"
"ds_read_b128 a[80:83], v240 offset:0\n"
"s_add_u32 m0, s80, 81920\n"
"v_mfma_f32_16x16x32_bf16 v[20:23], a[116:119], v[200:203], v[20:23]\n"
"v_exp_f32 v171, v171\n"
"v_mfma_f32_16x16x32_bf16 v[40:43], a[120:123], v[200:203], v[40:43]\n"
"buffer_load_dwordx4 v244, s[48:51], 0 offen nt lds\n"
"v_mfma_f32_16x16x32_bf16 v[60:63], a[124:127], v[200:203], v[60:63]\n"
"v_exp_f32 v172, v172\n"
"v_mfma_f32_16x16x32_bf16 v[80:83], a[128:131], v[200:203], v[80:83]\n"
"ds_read_b128 a[84:87], v240 offset:512\n"
"s_add_u32 m0, s80, 82816\n"
"v_mfma_f32_16x16x32_bf16 v[100:103], a[132:135], v[200:203], v[100:103]\n"
"v_exp_f32 v173, v173\n"
"v_mfma_f32_16x16x32_bf16 v[120:123], a[136:139], v[200:203], v[120:123]\n"
"v_exp_f32 v174, v174\n"
"v_mfma_f32_16x16x32_bf16 v[140:143], a[140:143], v[200:203], v[140:143]\n"
"v_exp_f32 v175, v175\n"
"v_mfma_f32_16x16x32_bf16 a[144:147], a[164:167], v[200:203], a[144:147]\n"
"buffer_load_dwordx4 v244, s[48:51], 0 offen offset:128 nt lds\n"
"v_mfma_f32_16x16x32_bf16 v[4:7], a[112:115], v[204:207], v[4:7]\n"
"ds_read_b128 a[88:91], v240 offset:1024\n"
"s_add_u32 m0, s80, 90112\n"
"v_mfma_f32_16x16x32_bf16 v[24:27], a[116:119], v[204:207], v[24:27]\n"
"v_cvt_pk_bf16_f32 v200, v160, v161\n"
"v_cvt_pk_bf16_f32 v201, v162, v163\n"
"v_mfma_f32_16x16x32_bf16 v[44:47], a[120:123], v[204:207], v[44:47]\n"
"v_exp_f32 v176, v176\n"
"v_mfma_f32_16x16x32_bf16 v[64:67], a[124:127], v[204:207], v[64:67]\n"
"v_exp_f32 v177, v177\n"
"v_mfma_f32_16x16x32_bf16 v[84:87], a[128:131], v[204:207], v[84:87]\n"
"ds_read_b128 a[92:95], v240 offset:1536\n"
"v_cvt_pk_bf16_f32 v202, v164, v165\n"
"v_mfma_f32_16x16x32_bf16 v[104:107], a[132:135], v[204:207], v[104:107]\n"
"buffer_load_dwordx4 v244, s[48:51], s68 offen nt lds\n"
"v_mfma_f32_16x16x32_bf16 v[124:127], a[136:139], v[204:207], v[124:127]\n"
"s_add_u32 m0, s80, 91008\n"
"v_cvt_pk_bf16_f32 v203, v166, v167\n"
"v_mfma_f32_16x16x32_bf16 v[144:147], a[140:143], v[204:207], v[144:147]\n"
"v_exp_f32 v178, v178\n"
"v_mfma_f32_16x16x32_bf16 a[148:151], a[164:167], v[204:207], a[148:151]\n"
"ds_read_b128 a[96:99], v240 offset:4096\n"
"v_add_u32 v243, s85, v243\n"
"v_mfma_f32_16x16x32_bf16 v[8:11], a[112:115], v[208:211], v[8:11]\n"
"v_exp_f32 v179, v179\n"
"v_mfma_f32_16x16x32_bf16 v[28:31], a[116:119], v[208:211], v[28:31]\n"
"v_cvt_pk_bf16_f32 v204, v168, v169\n"
"v_cvt_pk_bf16_f32 v205, v170, v171\n"
"v_mfma_f32_16x16x32_bf16 v[48:51], a[120:123], v[208:211], v[48:51]\n"
"buffer_load_dwordx4 v244, s[48:51], s68 offen offset:128 nt lds\n"
"v_mfma_f32_16x16x32_bf16 v[68:71], a[124:127], v[208:211], v[68:71]\n"
"ds_read_b128 a[100:103], v240 offset:4608\n"
"v_cvt_pk_bf16_f32 v206, v172, v173\n"
"v_mfma_f32_16x16x32_bf16 v[88:91], a[128:131], v[208:211], v[88:91]\n"
"v_exp_f32 v180, v180\n"
"v_mfma_f32_16x16x32_bf16 v[108:111], a[132:135], v[208:211], v[108:111]\n"
"v_cvt_pk_bf16_f32 v207, v174, v175\n"
"v_add_u32 v244, s85, v244\n"
"v_mfma_f32_16x16x32_bf16 v[128:131], a[136:139], v[208:211], v[128:131]\n"
"v_exp_f32 v181, v181\n"
"v_mfma_f32_16x16x32_bf16 v[148:151], a[140:143], v[208:211], v[148:151]\n"
"ds_read_b128 a[104:107], v240 offset:5120\n"
"v_add_u32 v241, 98304, v246\n"
"v_mfma_f32_16x16x32_bf16 a[152:155], a[164:167], v[208:211], a[152:155]\n"
"v_exp_f32 v182, v182\n"
"v_mfma_f32_16x16x32_bf16 v[12:15], a[112:115], v[212:215], v[12:15]\n"
"v_cvt_pk_bf16_f32 v208, v176, v177\n"
"v_cvt_pk_bf16_f32 v209, v178, v179\n"
"v_mfma_f32_16x16x32_bf16 v[32:35], a[116:119], v[212:215], v[32:35]\n"
"v_exp_f32 v183, v183\n"
"v_mfma_f32_16x16x32_bf16 v[52:55], a[120:123], v[212:215], v[52:55]\n"
"ds_read_b128 a[108:111], v240 offset:5632\n"
"v_cvt_pk_bf16_f32 v210, v180, v181\n"
"v_mfma_f32_16x16x32_bf16 v[72:75], a[124:127], v[212:215], v[72:75]\n"
"v_exp_f32 v184, v184\n"
"v_mfma_f32_16x16x32_bf16 v[92:95], a[128:131], v[212:215], v[92:95]\n"
"v_cvt_pk_bf16_f32 v211, v182, v183\n"
"v_xor_b32 v242, 32, v241\n"
"v_mfma_f32_16x16x32_bf16 v[112:115], a[132:135], v[212:215], v[112:115]\n"
"v_exp_f32 v185, v185\n"
"v_mfma_f32_16x16x32_bf16 v[132:135], a[136:139], v[212:215], v[132:135]\n"
"v_exp_f32 v186, v186\n"
"v_mfma_f32_16x16x32_bf16 v[152:155], a[140:143], v[212:215], v[152:155]\n"
"v_exp_f32 v187, v187\n"
"v_mfma_f32_16x16x32_bf16 a[156:159], a[164:167], v[212:215], a[156:159]\n"
"v_mfma_f32_16x16x32_bf16 v[16:19], a[112:115], v[216:219], v[16:19]\n"
"v_exp_f32 v188, v188\n"
"v_mfma_f32_16x16x32_bf16 v[36:39], a[116:119], v[216:219], v[36:39]\n"
"v_exp_f32 v189, v189\n"
"v_mfma_f32_16x16x32_bf16 v[56:59], a[120:123], v[216:219], v[56:59]\n"
"v_exp_f32 v190, v190\n"
"v_mfma_f32_16x16x32_bf16 v[76:79], a[124:127], v[216:219], v[76:79]\n"
"v_cvt_pk_bf16_f32 v212, v184, v185\n"
"v_cvt_pk_bf16_f32 v213, v186, v187\n"
"v_mfma_f32_16x16x32_bf16 v[96:99], a[128:131], v[216:219], v[96:99]\n"
"v_exp_f32 v191, v191\n"
"v_mfma_f32_16x16x32_bf16 v[116:119], a[132:135], v[216:219], v[116:119]\n"
"v_exp_f32 v192, v192\n"
"v_mfma_f32_16x16x32_bf16 v[136:139], a[136:139], v[216:219], v[136:139]\n"
"v_mfma_f32_16x16x32_bf16 v[156:159], a[140:143], v[216:219], v[156:159]\n"
"v_exp_f32 v193, v193\n"
"v_mfma_f32_16x16x32_bf16 a[160:163], a[164:167], v[216:219], a[160:163]\n"
"s_waitcnt lgkmcnt(0)\n"
"v_mfma_f32_16x16x32_bf16 v[160:163], a[80:83], a[0:3], v[220:223]\n"
"ds_read_b64_tr_b16 a[112:113], v241 offset:0\n"
"s_add_u32 m0, s80, 49152\n"
"v_mfma_f32_16x16x32_bf16 v[160:163], a[84:87], a[20:23], v[160:163]\n"
"v_exp_f32 v194, v194\n"
"v_mfma_f32_16x16x32_bf16 v[160:163], a[88:91], a[40:43], v[160:163]\n"
"ds_read_b64_tr_b16 a[114:115], v241 offset:4096\n"
"v_cvt_pk_bf16_f32 v214, v188, v189\n"
"v_mfma_f32_16x16x32_bf16 v[160:163], a[92:95], a[60:63], v[160:163]\n"
"v_exp_f32 v195, v195\n"
"v_mfma_f32_16x16x32_bf16 v[164:167], a[96:99], a[0:3], v[220:223]\n"
"ds_read_b64_tr_b16 a[116:117], v242 offset:0\n"
"v_cvt_pk_bf16_f32 v215, v190, v191\n"
"v_mfma_f32_16x16x32_bf16 v[164:167], a[100:103], a[20:23], v[164:167]\n"
"v_exp_f32 v196, v196\n"
"v_mfma_f32_16x16x32_bf16 v[164:167], a[104:107], a[40:43], v[164:167]\n"
"ds_read_b64_tr_b16 a[118:119], v242 offset:4096\n"
"v_cvt_pk_bf16_f32 v216, v192, v193\n"
"v_mfma_f32_16x16x32_bf16 v[164:167], a[108:111], a[60:63], v[164:167]\n"
"v_exp_f32 v197, v197\n"
"v_mfma_f32_16x16x32_bf16 v[168:171], a[80:83], a[4:7], v[224:227]\n"
"ds_read_b64_tr_b16 a[120:121], v241 offset:512\n"
"v_cvt_pk_bf16_f32 v217, v194, v195\n"
"v_mfma_f32_16x16x32_bf16 v[168:171], a[84:87], a[24:27], v[168:171]\n"
"v_exp_f32 v198, v198\n"
"v_mfma_f32_16x16x32_bf16 v[168:171], a[88:91], a[44:47], v[168:171]\n"
"ds_read_b64_tr_b16 a[122:123], v241 offset:4608\n"
"v_cvt_pk_bf16_f32 v218, v196, v197\n"
"v_mfma_f32_16x16x32_bf16 v[168:171], a[92:95], a[64:67], v[168:171]\n"
"v_exp_f32 v199, v199\n"
"v_mfma_f32_16x16x32_bf16 v[172:175], a[96:99], a[4:7], v[224:227]\n"
"ds_read_b64_tr_b16 a[124:125], v242 offset:512\n"
"v_cvt_pk_bf16_f32 v219, v198, v199\n"
"v_mfma_f32_16x16x32_bf16 v[172:175], a[100:103], a[24:27], v[172:175]\n"
"buffer_load_dwordx4 v243, s[44:47], 0 offen nt lds\n"
"v_mfma_f32_16x16x32_bf16 v[172:175], a[104:107], a[44:47], v[172:175]\n"
"ds_read_b64_tr_b16 a[126:127], v242 offset:4608\n"
"s_add_u32 m0, s80, 50048\n"
"v_mfma_f32_16x16x32_bf16 v[172:175], a[108:111], a[64:67], v[172:175]\n"
"ds_read_b64_tr_b16 a[128:129], v241 offset:1024\n"
"v_add_u32 v240, 0, v245\n"
"v_mfma_f32_16x16x32_bf16 v[176:179], a[80:83], a[8:11], v[228:231]\n"
"v_exp_f32 v160, v160\n"
"v_mfma_f32_16x16x32_bf16 v[176:179], a[84:87], a[28:31], v[176:179]\n"
"ds_read_b64_tr_b16 a[130:131], v241 offset:5120\n"
"v_mfma_f32_16x16x32_bf16 v[176:179], a[88:91], a[48:51], v[176:179]\n"
"buffer_load_dwordx4 v243, s[44:47], 0 offen offset:128 nt lds\n"
"v_mfma_f32_16x16x32_bf16 v[176:179], a[92:95], a[68:71], v[176:179]\n"
"ds_read_b64_tr_b16 a[132:133], v242 offset:1024\n"
"s_add_u32 m0, s80, 57344\n"
"v_mfma_f32_16x16x32_bf16 v[180:183], a[96:99], a[8:11], v[228:231]\n"
"v_exp_f32 v161, v161\n"
"v_mfma_f32_16x16x32_bf16 v[180:183], a[100:103], a[28:31], v[180:183]\n"
"ds_read_b64_tr_b16 a[134:135], v242 offset:5120\n"
"v_mfma_f32_16x16x32_bf16 v[180:183], a[104:107], a[48:51], v[180:183]\n"
"v_exp_f32 v162, v162\n"
"v_mfma_f32_16x16x32_bf16 v[180:183], a[108:111], a[68:71], v[180:183]\n"
"buffer_load_dwordx4 v243, s[44:47], s68 offen nt lds\n"
"v_mfma_f32_16x16x32_bf16 v[184:187], a[80:83], a[12:15], v[232:235]\n"
"ds_read_b64_tr_b16 a[136:137], v241 offset:1536\n"
"s_add_u32 m0, s80, 58240\n"
"v_mfma_f32_16x16x32_bf16 v[184:187], a[84:87], a[32:35], v[184:187]\n"
"v_exp_f32 v163, v163\n"
"v_mfma_f32_16x16x32_bf16 v[184:187], a[88:91], a[52:55], v[184:187]\n"
"ds_read_b64_tr_b16 a[138:139], v241 offset:5632\n"
"v_mfma_f32_16x16x32_bf16 v[184:187], a[92:95], a[72:75], v[184:187]\n"
"v_exp_f32 v164, v164\n"
"v_mfma_f32_16x16x32_bf16 v[188:191], a[96:99], a[12:15], v[232:235]\n"
"buffer_load_dwordx4 v243, s[44:47], s68 offen offset:128 nt lds\n"
"v_mfma_f32_16x16x32_bf16 v[188:191], a[100:103], a[32:35], v[188:191]\n"
"ds_read_b64_tr_b16 a[140:141], v242 offset:1536\n"
"v_mfma_f32_16x16x32_bf16 v[188:191], a[104:107], a[52:55], v[188:191]\n"
"v_exp_f32 v165, v165\n"
"v_mfma_f32_16x16x32_bf16 v[188:191], a[108:111], a[72:75], v[188:191]\n"
"ds_read_b64_tr_b16 a[142:143], v242 offset:5632\n"
"v_mfma_f32_16x16x32_bf16 v[192:195], a[80:83], a[16:19], v[236:239]\n"
"v_exp_f32 v166, v166\n"
"v_mfma_f32_16x16x32_bf16 v[192:195], a[84:87], a[36:39], v[192:195]\n"
"v_exp_f32 v167, v167\n"
"v_mfma_f32_16x16x32_bf16 v[192:195], a[88:91], a[56:59], v[192:195]\n"
"v_exp_f32 v168, v168\n"
"v_mfma_f32_16x16x32_bf16 v[192:195], a[92:95], a[76:79], v[192:195]\n"
"v_mfma_f32_16x16x32_bf16 v[196:199], a[96:99], a[16:19], v[236:239]\n"
"v_exp_f32 v169, v169\n"
"v_mfma_f32_16x16x32_bf16 v[196:199], a[100:103], a[36:39], v[196:199]\n"
"v_mfma_f32_16x16x32_bf16 v[196:199], a[104:107], a[56:59], v[196:199]\n"
"v_exp_f32 v170, v170\n"
"v_mfma_f32_16x16x32_bf16 v[196:199], a[108:111], a[76:79], v[196:199]\n"
"s_waitcnt vmcnt(24) lgkmcnt(0)\n"
"s_barrier\n"
"v_mfma_f32_16x16x32_bf16 v[0:3], a[112:115], v[200:203], v[0:3]\n"
"ds_read_b128 a[80:83], v240 offset:0\n"
"s_add_u32 m0, s80, 98304\n"
"v_mfma_f32_16x16x32_bf16 v[20:23], a[116:119], v[200:203], v[20:23]\n"
"v_exp_f32 v171, v171\n"
"v_mfma_f32_16x16x32_bf16 v[40:43], a[120:123], v[200:203], v[40:43]\n"
"buffer_load_dwordx4 v244, s[48:51], 0 offen nt lds\n"
"v_mfma_f32_16x16x32_bf16 v[60:63], a[124:127], v[200:203], v[60:63]\n"
"v_exp_f32 v172, v172\n"
"v_mfma_f32_16x16x32_bf16 v[80:83], a[128:131], v[200:203], v[80:83]\n"
"ds_read_b128 a[84:87], v240 offset:512\n"
"s_add_u32 m0, s80, 99200\n"
"v_mfma_f32_16x16x32_bf16 v[100:103], a[132:135], v[200:203], v[100:103]\n"
"v_exp_f32 v173, v173\n"
"v_mfma_f32_16x16x32_bf16 v[120:123], a[136:139], v[200:203], v[120:123]\n"
"v_exp_f32 v174, v174\n"
"v_mfma_f32_16x16x32_bf16 v[140:143], a[140:143], v[200:203], v[140:143]\n"
"v_exp_f32 v175, v175\n"
"v_mfma_f32_16x16x32_bf16 a[144:147], a[164:167], v[200:203], a[144:147]\n"
"buffer_load_dwordx4 v244, s[48:51], 0 offen offset:128 nt lds\n"
"v_mfma_f32_16x16x32_bf16 v[4:7], a[112:115], v[204:207], v[4:7]\n"
"ds_read_b128 a[88:91], v240 offset:1024\n"
"s_add_u32 m0, s80, 106496\n"
"v_mfma_f32_16x16x32_bf16 v[24:27], a[116:119], v[204:207], v[24:27]\n"
"v_cvt_pk_bf16_f32 v200, v160, v161\n"
"v_cvt_pk_bf16_f32 v201, v162, v163\n"
"v_mfma_f32_16x16x32_bf16 v[44:47], a[120:123], v[204:207], v[44:47]\n"
"v_exp_f32 v176, v176\n"
"v_mfma_f32_16x16x32_bf16 v[64:67], a[124:127], v[204:207], v[64:67]\n"
"v_exp_f32 v177, v177\n"
"v_mfma_f32_16x16x32_bf16 v[84:87], a[128:131], v[204:207], v[84:87]\n"
"ds_read_b128 a[92:95], v240 offset:1536\n"
"v_cvt_pk_bf16_f32 v202, v164, v165\n"
"v_mfma_f32_16x16x32_bf16 v[104:107], a[132:135], v[204:207], v[104:107]\n"
"buffer_load_dwordx4 v244, s[48:51], s68 offen nt lds\n"
"v_mfma_f32_16x16x32_bf16 v[124:127], a[136:139], v[204:207], v[124:127]\n"
"s_add_u32 m0, s80, 107392\n"
"v_cvt_pk_bf16_f32 v203, v166, v167\n"
"v_mfma_f32_16x16x32_bf16 v[144:147], a[140:143], v[204:207], v[144:147]\n"
"v_exp_f32 v178, v178\n"
"v_mfma_f32_16x16x32_bf16 a[148:151], a[164:167], v[204:207], a[148:151]\n"
"ds_read_b128 a[96:99], v240 offset:4096\n"
"v_add_u32 v243, s85, v243\n"
"v_mfma_f32_16x16x32_bf16 v[8:11], a[112:115], v[208:211], v[8:11]\n"
"v_exp_f32 v179, v179\n"
"v_mfma_f32_16x16x32_bf16 v[28:31], a[116:119], v[208:211], v[28:31]\n"
"v_cvt_pk_bf16_f32 v204, v168, v169\n"
"v_cvt_pk_bf16_f32 v205, v170, v171\n"
"v_mfma_f32_16x16x32_bf16 v[48:51], a[120:123], v[208:211], v[48:51]\n"
"buffer_load_dwordx4 v244, s[48:51], s68 offen offset:128 nt lds\n"
"v_mfma_f32_16x16x32_bf16 v[68:71], a[124:127], v[208:211], v[68:71]\n"
"ds_read_b128 a[100:103], v240 offset:4608\n"
"v_cvt_pk_bf16_f32 v206, v172, v173\n"
"v_mfma_f32_16x16x32_bf16 v[88:91], a[128:131], v[208:211], v[88:91]\n"
"v_exp_f32 v180, v180\n"
"v_mfma_f32_16x16x32_bf16 v[108:111], a[132:135], v[208:211], v[108:111]\n"
"v_cvt_pk_bf16_f32 v207, v174, v175\n"
"v_add_u32 v244, s85, v244\n"
"v_mfma_f32_16x16x32_bf16 v[128:131], a[136:139], v[208:211], v[128:131]\n"
"v_exp_f32 v181, v181\n"
"v_mfma_f32_16x16x32_bf16 v[148:151], a[140:143], v[208:211], v[148:151]\n"
"ds_read_b128 a[104:107], v240 offset:5120\n"
"v_add_u32 v241, 114688, v246\n"
"v_mfma_f32_16x16x32_bf16 a[152:155], a[164:167], v[208:211], a[152:155]\n"
"v_exp_f32 v182, v182\n"
"v_mfma_f32_16x16x32_bf16 v[12:15], a[112:115], v[212:215], v[12:15]\n"
"v_cvt_pk_bf16_f32 v208, v176, v177\n"
"v_cvt_pk_bf16_f32 v209, v178, v179\n"
"v_mfma_f32_16x16x32_bf16 v[32:35], a[116:119], v[212:215], v[32:35]\n"
"v_exp_f32 v183, v183\n"
"v_mfma_f32_16x16x32_bf16 v[52:55], a[120:123], v[212:215], v[52:55]\n"
"ds_read_b128 a[108:111], v240 offset:5632\n"
"v_cvt_pk_bf16_f32 v210, v180, v181\n"
"v_mfma_f32_16x16x32_bf16 v[72:75], a[124:127], v[212:215], v[72:75]\n"
"v_exp_f32 v184, v184\n"
"v_mfma_f32_16x16x32_bf16 v[92:95], a[128:131], v[212:215], v[92:95]\n"
"v_cvt_pk_bf16_f32 v211, v182, v183\n"
"v_xor_b32 v242, 32, v241\n"
"v_mfma_f32_16x16x32_bf16 v[112:115], a[132:135], v[212:215], v[112:115]\n"
"v_exp_f32 v185, v185\n"
"v_mfma_f32_16x16x32_bf16 v[132:135], a[136:139], v[212:215], v[132:135]\n"
"v_exp_f32 v186, v186\n"
"v_mfma_f32_16x16x32_bf16 v[152:155], a[140:143], v[212:215], v[152:155]\n"
"v_exp_f32 v187, v187\n"
"v_mfma_f32_16x16x32_bf16 a[156:159], a[164:167], v[212:215], a[156:159]\n"
"v_mfma_f32_16x16x32_bf16 v[16:19], a[112:115], v[216:219], v[16:19]\n"
"v_exp_f32 v188, v188\n"
"v_mfma_f32_16x16x32_bf16 v[36:39], a[116:119], v[216:219], v[36:39]\n"
"v_exp_f32 v189, v189\n"
"v_mfma_f32_16x16x32_bf16 v[56:59], a[120:123], v[216:219], v[56:59]\n"
"v_exp_f32 v190, v190\n"
"v_mfma_f32_16x16x32_bf16 v[76:79], a[124:127], v[216:219], v[76:79]\n"
"v_cvt_pk_bf16_f32 v212, v184, v185\n"
"v_cvt_pk_bf16_f32 v213, v186, v187\n"
"v_mfma_f32_16x16x32_bf16 v[96:99], a[128:131], v[216:219], v[96:99]\n"
"v_exp_f32 v191, v191\n"
"v_mfma_f32_16x16x32_bf16 v[116:119], a[132:135], v[216:219], v[116:119]\n"
"v_exp_f32 v192, v192\n"
"v_mfma_f32_16x16x32_bf16 v[136:139], a[136:139], v[216:219], v[136:139]\n"
"v_mfma_f32_16x16x32_bf16 v[156:159], a[140:143], v[216:219], v[156:159]\n"
"v_exp_f32 v193, v193\n"
"v_mfma_f32_16x16x32_bf16 a[160:163], a[164:167], v[216:219], a[160:163]\n"
"s_waitcnt lgkmcnt(0)\n"
"v_mfma_f32_16x16x32_bf16 v[160:163], a[80:83], a[0:3], v[220:223]\n"
"ds_read_b64_tr_b16 a[112:113], v241 offset:0\n"
"s_add_u32 m0, s80, 65536\n"
"v_mfma_f32_16x16x32_bf16 v[160:163], a[84:87], a[20:23], v[160:163]\n"
"v_exp_f32 v194, v194\n"
"v_mfma_f32_16x16x32_bf16 v[160:163], a[88:91], a[40:43], v[160:163]\n"
"ds_read_b64_tr_b16 a[114:115], v241 offset:4096\n"
"v_cvt_pk_bf16_f32 v214, v188, v189\n"
"v_mfma_f32_16x16x32_bf16 v[160:163], a[92:95], a[60:63], v[160:163]\n"
"v_exp_f32 v195, v195\n"
"v_mfma_f32_16x16x32_bf16 v[164:167], a[96:99], a[0:3], v[220:223]\n"
"ds_read_b64_tr_b16 a[116:117], v242 offset:0\n"
"v_cvt_pk_bf16_f32 v215, v190, v191\n"
"v_mfma_f32_16x16x32_bf16 v[164:167], a[100:103], a[20:23], v[164:167]\n"
"v_exp_f32 v196, v196\n"
"v_mfma_f32_16x16x32_bf16 v[164:167], a[104:107], a[40:43], v[164:167]\n"
"ds_read_b64_tr_b16 a[118:119], v242 offset:4096\n"
"v_cvt_pk_bf16_f32 v216, v192, v193\n"
"v_mfma_f32_16x16x32_bf16 v[164:167], a[108:111], a[60:63], v[164:167]\n"
"v_exp_f32 v197, v197\n"
"v_mfma_f32_16x16x32_bf16 v[168:171], a[80:83], a[4:7], v[224:227]\n"
"ds_read_b64_tr_b16 a[120:121], v241 offset:512\n"
"v_cvt_pk_bf16_f32 v217, v194, v195\n"
"v_mfma_f32_16x16x32_bf16 v[168:171], a[84:87], a[24:27], v[168:171]\n"
"v_exp_f32 v198, v198\n"
"v_mfma_f32_16x16x32_bf16 v[168:171], a[88:91], a[44:47], v[168:171]\n"
"ds_read_b64_tr_b16 a[122:123], v241 offset:4608\n"
"v_cvt_pk_bf16_f32 v218, v196, v197\n"
"v_mfma_f32_16x16x32_bf16 v[168:171], a[92:95], a[64:67], v[168:171]\n"
"v_exp_f32 v199, v199\n"
"v_mfma_f32_16x16x32_bf16 v[172:175], a[96:99], a[4:7], v[224:227]\n"
"ds_read_b64_tr_b16 a[124:125], v242 offset:512\n"
"v_cvt_pk_bf16_f32 v219, v198, v199\n"
"v_mfma_f32_16x16x32_bf16 v[172:175], a[100:103], a[24:27], v[172:175]\n"
"buffer_load_dwordx4 v243, s[44:47], 0 offen nt lds\n"
"v_mfma_f32_16x16x32_bf16 v[172:175], a[104:107], a[44:47], v[172:175]\n"
"ds_read_b64_tr_b16 a[126:127], v242 offset:4608\n"
"s_add_u32 m0, s80, 66432\n"
"v_mfma_f32_16x16x32_bf16 v[172:175], a[108:111], a[64:67], v[172:175]\n"
"ds_read_b64_tr_b16 a[128:129], v241 offset:1024\n"
"v_add_u32 v240, 16384, v245\n"
"v_mfma_f32_16x16x32_bf16 v[176:179], a[80:83], a[8:11], v[228:231]\n"
"v_exp_f32 v160, v160\n"
"v_mfma_f32_16x16x32_bf16 v[176:179], a[84:87], a[28:31], v[176:179]\n"
"ds_read_b64_tr_b16 a[130:131], v241 offset:5120\n"
"v_mfma_f32_16x16x32_bf16 v[176:179], a[88:91], a[48:51], v[176:179]\n"
"buffer_load_dwordx4 v243, s[44:47], 0 offen offset:128 nt lds\n"
"v_mfma_f32_16x16x32_bf16 v[176:179], a[92:95], a[68:71], v[176:179]\n"
"ds_read_b64_tr_b16 a[132:133], v242 offset:1024\n"
"s_add_u32 m0, s80, 73728\n"
"v_mfma_f32_16x16x32_bf16 v[180:183], a[96:99], a[8:11], v[228:231]\n"
"v_exp_f32 v161, v161\n"
"v_mfma_f32_16x16x32_bf16 v[180:183], a[100:103], a[28:31], v[180:183]\n"
"ds_read_b64_tr_b16 a[134:135], v242 offset:5120\n"
"v_mfma_f32_16x16x32_bf16 v[180:183], a[104:107], a[48:51], v[180:183]\n"
"v_exp_f32 v162, v162\n"
"v_mfma_f32_16x16x32_bf16 v[180:183], a[108:111], a[68:71], v[180:183]\n"
"buffer_load_dwordx4 v243, s[44:47], s68 offen nt lds\n"
"v_mfma_f32_16x16x32_bf16 v[184:187], a[80:83], a[12:15], v[232:235]\n"
"ds_read_b64_tr_b16 a[136:137], v241 offset:1536\n"
"s_add_u32 m0, s80, 74624\n"
"v_mfma_f32_16x16x32_bf16 v[184:187], a[84:87], a[32:35], v[184:187]\n"
"v_exp_f32 v163, v163\n"
"v_mfma_f32_16x16x32_bf16 v[184:187], a[88:91], a[52:55], v[184:187]\n"
"ds_read_b64_tr_b16 a[138:139], v241 offset:5632\n"
"v_mfma_f32_16x16x32_bf16 v[184:187], a[92:95], a[72:75], v[184:187]\n"
"v_exp_f32 v164, v164\n"
"v_mfma_f32_16x16x32_bf16 v[188:191], a[96:99], a[12:15], v[232:235]\n"
"buffer_load_dwordx4 v243, s[44:47], s68 offen offset:128 nt lds\n"
"v_mfma_f32_16x16x32_bf16 v[188:191], a[100:103], a[32:35], v[188:191]\n"
"ds_read_b64_tr_b16 a[140:141], v242 offset:1536\n"
"v_mfma_f32_16x16x32_bf16 v[188:191], a[104:107], a[52:55], v[188:191]\n"
"v_exp_f32 v165, v165\n"
"v_mfma_f32_16x16x32_bf16 v[188:191], a[108:111], a[72:75], v[188:191]\n"
"ds_read_b64_tr_b16 a[142:143], v242 offset:5632\n"
"v_mfma_f32_16x16x32_bf16 v[192:195], a[80:83], a[16:19], v[236:239]\n"
"v_exp_f32 v166, v166\n"
"v_mfma_f32_16x16x32_bf16 v[192:195], a[84:87], a[36:39], v[192:195]\n"
"v_exp_f32 v167, v167\n"
"v_mfma_f32_16x16x32_bf16 v[192:195], a[88:91], a[56:59], v[192:195]\n"
"v_exp_f32 v168, v168\n"
"v_mfma_f32_16x16x32_bf16 v[192:195], a[92:95], a[76:79], v[192:195]\n"
"v_mfma_f32_16x16x32_bf16 v[196:199], a[96:99], a[16:19], v[236:239]\n"
"v_exp_f32 v169, v169\n"
"v_mfma_f32_16x16x32_bf16 v[196:199], a[100:103], a[36:39], v[196:199]\n"
"v_mfma_f32_16x16x32_bf16 v[196:199], a[104:107], a[56:59], v[196:199]\n"
"v_exp_f32 v170, v170\n"
"v_mfma_f32_16x16x32_bf16 v[196:199], a[108:111], a[76:79], v[196:199]\n"
"s_waitcnt vmcnt(24) lgkmcnt(0)\n"
"s_barrier\n"
"v_mfma_f32_16x16x32_bf16 v[0:3], a[112:115], v[200:203], v[0:3]\n"
"ds_read_b128 a[80:83], v240 offset:0\n"
"v_mfma_f32_16x16x32_bf16 v[20:23], a[116:119], v[200:203], v[20:23]\n"
"v_exp_f32 v171, v171\n"
"v_mfma_f32_16x16x32_bf16 v[40:43], a[120:123], v[200:203], v[40:43]\n"
"v_mfma_f32_16x16x32_bf16 v[60:63], a[124:127], v[200:203], v[60:63]\n"
"v_exp_f32 v172, v172\n"
"v_mfma_f32_16x16x32_bf16 v[80:83], a[128:131], v[200:203], v[80:83]\n"
"ds_read_b128 a[84:87], v240 offset:512\n"
"v_mfma_f32_16x16x32_bf16 v[100:103], a[132:135], v[200:203], v[100:103]\n"
"v_exp_f32 v173, v173\n"
"v_mfma_f32_16x16x32_bf16 v[120:123], a[136:139], v[200:203], v[120:123]\n"
"v_exp_f32 v174, v174\n"
"v_mfma_f32_16x16x32_bf16 v[140:143], a[140:143], v[200:203], v[140:143]\n"
"v_exp_f32 v175, v175\n"
"v_mfma_f32_16x16x32_bf16 a[144:147], a[164:167], v[200:203], a[144:147]\n"
"v_mfma_f32_16x16x32_bf16 v[4:7], a[112:115], v[204:207], v[4:7]\n"
"ds_read_b128 a[88:91], v240 offset:1024\n"
"v_mfma_f32_16x16x32_bf16 v[24:27], a[116:119], v[204:207], v[24:27]\n"
"v_cvt_pk_bf16_f32 v200, v160, v161\n"
"v_cvt_pk_bf16_f32 v201, v162, v163\n"
"v_mfma_f32_16x16x32_bf16 v[44:47], a[120:123], v[204:207], v[44:47]\n"
"v_exp_f32 v176, v176\n"
"v_mfma_f32_16x16x32_bf16 v[64:67], a[124:127], v[204:207], v[64:67]\n"
"v_exp_f32 v177, v177\n"
"v_mfma_f32_16x16x32_bf16 v[84:87], a[128:131], v[204:207], v[84:87]\n"
"ds_read_b128 a[92:95], v240 offset:1536\n"
"v_cvt_pk_bf16_f32 v202, v164, v165\n"
"v_mfma_f32_16x16x32_bf16 v[104:107], a[132:135], v[204:207], v[104:107]\n"
"v_mfma_f32_16x16x32_bf16 v[124:127], a[136:139], v[204:207], v[124:127]\n"
"v_cvt_pk_bf16_f32 v203, v166, v167\n"
"v_mfma_f32_16x16x32_bf16 v[144:147], a[140:143], v[204:207], v[144:147]\n"
"v_exp_f32 v178, v178\n"
"v_mfma_f32_16x16x32_bf16 a[148:151], a[164:167], v[204:207], a[148:151]\n"
"ds_read_b128 a[96:99], v240 offset:4096\n"
"v_add_u32 v243, s85, v243\n"
"v_mfma_f32_16x16x32_bf16 v[8:11], a[112:115], v[208:211], v[8:11]\n"
"v_exp_f32 v179, v179\n"
"v_mfma_f32_16x16x32_bf16 v[28:31], a[116:119], v[208:211], v[28:31]\n"
"v_cvt_pk_bf16_f32 v204, v168, v169\n"
"v_cvt_pk_bf16_f32 v205, v170, v171\n"
"v_mfma_f32_16x16x32_bf16 v[48:51], a[120:123], v[208:211], v[48:51]\n"
"v_mfma_f32_16x16x32_bf16 v[68:71], a[124:127], v[208:211], v[68:71]\n"
"ds_read_b128 a[100:103], v240 offset:4608\n"
"v_cvt_pk_bf16_f32 v206, v172, v173\n"
"v_mfma_f32_16x16x32_bf16 v[88:91], a[128:131], v[208:211], v[88:91]\n"
"v_exp_f32 v180, v180\n"
"v_mfma_f32_16x16x32_bf16 v[108:111], a[132:135], v[208:211], v[108:111]\n"
"v_cvt_pk_bf16_f32 v207, v174, v175\n"
"v_mfma_f32_16x16x32_bf16 v[128:131], a[136:139], v[208:211], v[128:131]\n"
"v_exp_f32 v181, v181\n"
"v_mfma_f32_16x16x32_bf16 v[148:151], a[140:143], v[208:211], v[148:151]\n"
"ds_read_b128 a[104:107], v240 offset:5120\n"
"v_add_u32 v241, 131072, v246\n"
"v_mfma_f32_16x16x32_bf16 a[152:155], a[164:167], v[208:211], a[152:155]\n"
"v_exp_f32 v182, v182\n"
"v_mfma_f32_16x16x32_bf16 v[12:15], a[112:115], v[212:215], v[12:15]\n"
"v_cvt_pk_bf16_f32 v208, v176, v177\n"
"v_cvt_pk_bf16_f32 v209, v178, v179\n"
"v_mfma_f32_16x16x32_bf16 v[32:35], a[116:119], v[212:215], v[32:35]\n"
"v_exp_f32 v183, v183\n"
"v_mfma_f32_16x16x32_bf16 v[52:55], a[120:123], v[212:215], v[52:55]\n"
"ds_read_b128 a[108:111], v240 offset:5632\n"
"v_cvt_pk_bf16_f32 v210, v180, v181\n"
"v_mfma_f32_16x16x32_bf16 v[72:75], a[124:127], v[212:215], v[72:75]\n"
"v_exp_f32 v184, v184\n"
"v_mfma_f32_16x16x32_bf16 v[92:95], a[128:131], v[212:215], v[92:95]\n"
"v_cvt_pk_bf16_f32 v211, v182, v183\n"
"v_xor_b32 v242, 32, v241\n"
"v_mfma_f32_16x16x32_bf16 v[112:115], a[132:135], v[212:215], v[112:115]\n"
"v_exp_f32 v185, v185\n"
"v_mfma_f32_16x16x32_bf16 v[132:135], a[136:139], v[212:215], v[132:135]\n"
"v_exp_f32 v186, v186\n"
"v_mfma_f32_16x16x32_bf16 v[152:155], a[140:143], v[212:215], v[152:155]\n"
"v_exp_f32 v187, v187\n"
"v_mfma_f32_16x16x32_bf16 a[156:159], a[164:167], v[212:215], a[156:159]\n"
"v_mfma_f32_16x16x32_bf16 v[16:19], a[112:115], v[216:219], v[16:19]\n"
"v_exp_f32 v188, v188\n"
"v_mfma_f32_16x16x32_bf16 v[36:39], a[116:119], v[216:219], v[36:39]\n"
"v_exp_f32 v189, v189\n"
"v_mfma_f32_16x16x32_bf16 v[56:59], a[120:123], v[216:219], v[56:59]\n"
"v_exp_f32 v190, v190\n"
"v_mfma_f32_16x16x32_bf16 v[76:79], a[124:127], v[216:219], v[76:79]\n"
"v_cvt_pk_bf16_f32 v212, v184, v185\n"
"v_cvt_pk_bf16_f32 v213, v186, v187\n"
"v_mfma_f32_16x16x32_bf16 v[96:99], a[128:131], v[216:219], v[96:99]\n"
"v_exp_f32 v191, v191\n"
"v_mfma_f32_16x16x32_bf16 v[116:119], a[132:135], v[216:219], v[116:119]\n"
"v_exp_f32 v192, v192\n"
"v_mfma_f32_16x16x32_bf16 v[136:139], a[136:139], v[216:219], v[136:139]\n"
"v_mfma_f32_16x16x32_bf16 v[156:159], a[140:143], v[216:219], v[156:159]\n"
"v_exp_f32 v193, v193\n"
"v_mfma_f32_16x16x32_bf16 a[160:163], a[164:167], v[216:219], a[160:163]\n"
"s_waitcnt lgkmcnt(0)\n"
"v_mfma_f32_16x16x32_bf16 v[160:163], a[80:83], a[0:3], v[220:223]\n"
"ds_read_b64_tr_b16 a[112:113], v241 offset:0\n"
"s_add_u32 m0, s80, 0\n"
"v_mfma_f32_16x16x32_bf16 v[160:163], a[84:87], a[20:23], v[160:163]\n"
"v_exp_f32 v194, v194\n"
"v_mfma_f32_16x16x32_bf16 v[160:163], a[88:91], a[40:43], v[160:163]\n"
"ds_read_b64_tr_b16 a[114:115], v241 offset:4096\n"
"v_cvt_pk_bf16_f32 v214, v188, v189\n"
"v_mfma_f32_16x16x32_bf16 v[160:163], a[92:95], a[60:63], v[160:163]\n"
"v_exp_f32 v195, v195\n"
"v_mfma_f32_16x16x32_bf16 v[164:167], a[96:99], a[0:3], v[220:223]\n"
"ds_read_b64_tr_b16 a[116:117], v242 offset:0\n"
"v_cvt_pk_bf16_f32 v215, v190, v191\n"
"v_mfma_f32_16x16x32_bf16 v[164:167], a[100:103], a[20:23], v[164:167]\n"
"v_exp_f32 v196, v196\n"
"v_mfma_f32_16x16x32_bf16 v[164:167], a[104:107], a[40:43], v[164:167]\n"
"ds_read_b64_tr_b16 a[118:119], v242 offset:4096\n"
"v_cvt_pk_bf16_f32 v216, v192, v193\n"
"v_mfma_f32_16x16x32_bf16 v[164:167], a[108:111], a[60:63], v[164:167]\n"
"v_exp_f32 v197, v197\n"
"v_mfma_f32_16x16x32_bf16 v[168:171], a[80:83], a[4:7], v[224:227]\n"
"ds_read_b64_tr_b16 a[120:121], v241 offset:512\n"
"v_cvt_pk_bf16_f32 v217, v194, v195\n"
"v_mfma_f32_16x16x32_bf16 v[168:171], a[84:87], a[24:27], v[168:171]\n"
"v_exp_f32 v198, v198\n"
"v_mfma_f32_16x16x32_bf16 v[168:171], a[88:91], a[44:47], v[168:171]\n"
"ds_read_b64_tr_b16 a[122:123], v241 offset:4608\n"
"v_cvt_pk_bf16_f32 v218, v196, v197\n"
"v_mfma_f32_16x16x32_bf16 v[168:171], a[92:95], a[64:67], v[168:171]\n"
"v_exp_f32 v199, v199\n"
"v_mfma_f32_16x16x32_bf16 v[172:175], a[96:99], a[4:7], v[224:227]\n"
"ds_read_b64_tr_b16 a[124:125], v242 offset:512\n"
"v_cvt_pk_bf16_f32 v219, v198, v199\n"
"v_mfma_f32_16x16x32_bf16 v[172:175], a[100:103], a[24:27], v[172:175]\n"
"buffer_load_dwordx4 v243, s[44:47], 0 offen nt lds\n"
"v_mfma_f32_16x16x32_bf16 v[172:175], a[104:107], a[44:47], v[172:175]\n"
"ds_read_b64_tr_b16 a[126:127], v242 offset:4608\n"
"s_add_u32 m0, s80, 896\n"
"v_mfma_f32_16x16x32_bf16 v[172:175], a[108:111], a[64:67], v[172:175]\n"
"ds_read_b64_tr_b16 a[128:129], v241 offset:1024\n"
"v_add_u32 v240, 32768, v245\n"
"v_mfma_f32_16x16x32_bf16 v[176:179], a[80:83], a[8:11], v[228:231]\n"
"v_exp_f32 v160, v160\n"
"v_mfma_f32_16x16x32_bf16 v[176:179], a[84:87], a[28:31], v[176:179]\n"
"ds_read_b64_tr_b16 a[130:131], v241 offset:5120\n"
"v_mfma_f32_16x16x32_bf16 v[176:179], a[88:91], a[48:51], v[176:179]\n"
"buffer_load_dwordx4 v243, s[44:47], 0 offen offset:128 nt lds\n"
"v_mfma_f32_16x16x32_bf16 v[176:179], a[92:95], a[68:71], v[176:179]\n"
"ds_read_b64_tr_b16 a[132:133], v242 offset:1024\n"
"s_add_u32 m0, s80, 8192\n"
"v_mfma_f32_16x16x32_bf16 v[180:183], a[96:99], a[8:11], v[228:231]\n"
"v_exp_f32 v161, v161\n"
"v_mfma_f32_16x16x32_bf16 v[180:183], a[100:103], a[28:31], v[180:183]\n"
"ds_read_b64_tr_b16 a[134:135], v242 offset:5120\n"
"v_mfma_f32_16x16x32_bf16 v[180:183], a[104:107], a[48:51], v[180:183]\n"
"v_exp_f32 v162, v162\n"
"v_mfma_f32_16x16x32_bf16 v[180:183], a[108:111], a[68:71], v[180:183]\n"
"buffer_load_dwordx4 v243, s[44:47], s68 offen nt lds\n"
"v_mfma_f32_16x16x32_bf16 v[184:187], a[80:83], a[12:15], v[232:235]\n"
"ds_read_b64_tr_b16 a[136:137], v241 offset:1536\n"
"s_add_u32 m0, s80, 9088\n"
"v_mfma_f32_16x16x32_bf16 v[184:187], a[84:87], a[32:35], v[184:187]\n"
"v_exp_f32 v163, v163\n"
"v_mfma_f32_16x16x32_bf16 v[184:187], a[88:91], a[52:55], v[184:187]\n"
"ds_read_b64_tr_b16 a[138:139], v241 offset:5632\n"
"v_mfma_f32_16x16x32_bf16 v[184:187], a[92:95], a[72:75], v[184:187]\n"
"v_exp_f32 v164, v164\n"
"v_mfma_f32_16x16x32_bf16 v[188:191], a[96:99], a[12:15], v[232:235]\n"
"buffer_load_dwordx4 v243, s[44:47], s68 offen offset:128 nt lds\n"
"v_mfma_f32_16x16x32_bf16 v[188:191], a[100:103], a[32:35], v[188:191]\n"
"ds_read_b64_tr_b16 a[140:141], v242 offset:1536\n"
"v_mfma_f32_16x16x32_bf16 v[188:191], a[104:107], a[52:55], v[188:191]\n"
"v_exp_f32 v165, v165\n"
"v_mfma_f32_16x16x32_bf16 v[188:191], a[108:111], a[72:75], v[188:191]\n"
"ds_read_b64_tr_b16 a[142:143], v242 offset:5632\n"
"v_mfma_f32_16x16x32_bf16 v[192:195], a[80:83], a[16:19], v[236:239]\n"
"v_exp_f32 v166, v166\n"
"v_mfma_f32_16x16x32_bf16 v[192:195], a[84:87], a[36:39], v[192:195]\n"
"v_exp_f32 v167, v167\n"
"v_mfma_f32_16x16x32_bf16 v[192:195], a[88:91], a[56:59], v[192:195]\n"
"v_exp_f32 v168, v168\n"
"v_mfma_f32_16x16x32_bf16 v[192:195], a[92:95], a[76:79], v[192:195]\n"
"v_mfma_f32_16x16x32_bf16 v[196:199], a[96:99], a[16:19], v[236:239]\n"
"v_exp_f32 v169, v169\n"
"v_mfma_f32_16x16x32_bf16 v[196:199], a[100:103], a[36:39], v[196:199]\n"
"v_mfma_f32_16x16x32_bf16 v[196:199], a[104:107], a[56:59], v[196:199]\n"
"v_exp_f32 v170, v170\n"
"v_mfma_f32_16x16x32_bf16 v[196:199], a[108:111], a[76:79], v[196:199]\n"
"s_waitcnt vmcnt(20) lgkmcnt(0)\n"
"s_barrier\n"
"v_mfma_f32_16x16x32_bf16 v[0:3], a[112:115], v[200:203], v[0:3]\n"
"ds_read_b128 a[80:83], v240 offset:0\n"
"s_add_u32 m0, s80, 114688\n"
"v_mfma_f32_16x16x32_bf16 v[20:23], a[116:119], v[200:203], v[20:23]\n"
"v_exp_f32 v171, v171\n"
"v_mfma_f32_16x16x32_bf16 v[40:43], a[120:123], v[200:203], v[40:43]\n"
"buffer_load_dwordx4 v244, s[48:51], 0 offen nt lds\n"
"v_mfma_f32_16x16x32_bf16 v[60:63], a[124:127], v[200:203], v[60:63]\n"
"v_exp_f32 v172, v172\n"
"v_mfma_f32_16x16x32_bf16 v[80:83], a[128:131], v[200:203], v[80:83]\n"
"ds_read_b128 a[84:87], v240 offset:512\n"
"s_add_u32 m0, s80, 115584\n"
"v_mfma_f32_16x16x32_bf16 v[100:103], a[132:135], v[200:203], v[100:103]\n"
"v_exp_f32 v173, v173\n"
"v_mfma_f32_16x16x32_bf16 v[120:123], a[136:139], v[200:203], v[120:123]\n"
"v_exp_f32 v174, v174\n"
"v_mfma_f32_16x16x32_bf16 v[140:143], a[140:143], v[200:203], v[140:143]\n"
"v_exp_f32 v175, v175\n"
"v_mfma_f32_16x16x32_bf16 a[144:147], a[164:167], v[200:203], a[144:147]\n"
"buffer_load_dwordx4 v244, s[48:51], 0 offen offset:128 nt lds\n"
"v_mfma_f32_16x16x32_bf16 v[4:7], a[112:115], v[204:207], v[4:7]\n"
"ds_read_b128 a[88:91], v240 offset:1024\n"
"s_add_u32 m0, s80, 122880\n"
"v_mfma_f32_16x16x32_bf16 v[24:27], a[116:119], v[204:207], v[24:27]\n"
"v_cvt_pk_bf16_f32 v200, v160, v161\n"
"v_cvt_pk_bf16_f32 v201, v162, v163\n"
"v_mfma_f32_16x16x32_bf16 v[44:47], a[120:123], v[204:207], v[44:47]\n"
"v_exp_f32 v176, v176\n"
"v_mfma_f32_16x16x32_bf16 v[64:67], a[124:127], v[204:207], v[64:67]\n"
"v_exp_f32 v177, v177\n"
"v_mfma_f32_16x16x32_bf16 v[84:87], a[128:131], v[204:207], v[84:87]\n"
"ds_read_b128 a[92:95], v240 offset:1536\n"
"v_cvt_pk_bf16_f32 v202, v164, v165\n"
"v_mfma_f32_16x16x32_bf16 v[104:107], a[132:135], v[204:207], v[104:107]\n"
"buffer_load_dwordx4 v244, s[48:51], s68 offen nt lds\n"
"v_mfma_f32_16x16x32_bf16 v[124:127], a[136:139], v[204:207], v[124:127]\n"
"s_add_u32 m0, s80, 123776\n"
"v_cvt_pk_bf16_f32 v203, v166, v167\n"
"v_mfma_f32_16x16x32_bf16 v[144:147], a[140:143], v[204:207], v[144:147]\n"
"v_exp_f32 v178, v178\n"
"v_mfma_f32_16x16x32_bf16 a[148:151], a[164:167], v[204:207], a[148:151]\n"
"ds_read_b128 a[96:99], v240 offset:4096\n"
"v_add_u32 v243, s85, v243\n"
"v_mfma_f32_16x16x32_bf16 v[8:11], a[112:115], v[208:211], v[8:11]\n"
"v_exp_f32 v179, v179\n"
"v_mfma_f32_16x16x32_bf16 v[28:31], a[116:119], v[208:211], v[28:31]\n"
"v_cvt_pk_bf16_f32 v204, v168, v169\n"
"v_cvt_pk_bf16_f32 v205, v170, v171\n"
"v_mfma_f32_16x16x32_bf16 v[48:51], a[120:123], v[208:211], v[48:51]\n"
"buffer_load_dwordx4 v244, s[48:51], s68 offen offset:128 nt lds\n"
"v_mfma_f32_16x16x32_bf16 v[68:71], a[124:127], v[208:211], v[68:71]\n"
"ds_read_b128 a[100:103], v240 offset:4608\n"
"v_cvt_pk_bf16_f32 v206, v172, v173\n"
"v_mfma_f32_16x16x32_bf16 v[88:91], a[128:131], v[208:211], v[88:91]\n"
"v_exp_f32 v180, v180\n"
"v_mfma_f32_16x16x32_bf16 v[108:111], a[132:135], v[208:211], v[108:111]\n"
"v_cvt_pk_bf16_f32 v207, v174, v175\n"
"v_add_u32 v244, s85, v244\n"
"v_mfma_f32_16x16x32_bf16 v[128:131], a[136:139], v[208:211], v[128:131]\n"
"v_exp_f32 v181, v181\n"
"v_mfma_f32_16x16x32_bf16 v[148:151], a[140:143], v[208:211], v[148:151]\n"
"ds_read_b128 a[104:107], v240 offset:5120\n"
"v_add_u32 v241, 147456, v246\n"
"v_mfma_f32_16x16x32_bf16 a[152:155], a[164:167], v[208:211], a[152:155]\n"
"v_exp_f32 v182, v182\n"
"v_mfma_f32_16x16x32_bf16 v[12:15], a[112:115], v[212:215], v[12:15]\n"
"v_cvt_pk_bf16_f32 v208, v176, v177\n"
"v_cvt_pk_bf16_f32 v209, v178, v179\n"
"v_mfma_f32_16x16x32_bf16 v[32:35], a[116:119], v[212:215], v[32:35]\n"
"v_exp_f32 v183, v183\n"
"v_mfma_f32_16x16x32_bf16 v[52:55], a[120:123], v[212:215], v[52:55]\n"
"ds_read_b128 a[108:111], v240 offset:5632\n"
"v_cvt_pk_bf16_f32 v210, v180, v181\n"
"v_mfma_f32_16x16x32_bf16 v[72:75], a[124:127], v[212:215], v[72:75]\n"
"v_exp_f32 v184, v184\n"
"v_mfma_f32_16x16x32_bf16 v[92:95], a[128:131], v[212:215], v[92:95]\n"
"v_cvt_pk_bf16_f32 v211, v182, v183\n"
"v_xor_b32 v242, 32, v241\n"
"v_mfma_f32_16x16x32_bf16 v[112:115], a[132:135], v[212:215], v[112:115]\n"
"v_exp_f32 v185, v185\n"
"v_mfma_f32_16x16x32_bf16 v[132:135], a[136:139], v[212:215], v[132:135]\n"
"v_exp_f32 v186, v186\n"
"v_mfma_f32_16x16x32_bf16 v[152:155], a[140:143], v[212:215], v[152:155]\n"
"v_exp_f32 v187, v187\n"
"v_mfma_f32_16x16x32_bf16 a[156:159], a[164:167], v[212:215], a[156:159]\n"
"v_mfma_f32_16x16x32_bf16 v[16:19], a[112:115], v[216:219], v[16:19]\n"
"v_exp_f32 v188, v188\n"
"v_mfma_f32_16x16x32_bf16 v[36:39], a[116:119], v[216:219], v[36:39]\n"
"v_exp_f32 v189, v189\n"
"v_mfma_f32_16x16x32_bf16 v[56:59], a[120:123], v[216:219], v[56:59]\n"
"v_exp_f32 v190, v190\n"
"v_mfma_f32_16x16x32_bf16 v[76:79], a[124:127], v[216:219], v[76:79]\n"
"v_cvt_pk_bf16_f32 v212, v184, v185\n"
"v_cvt_pk_bf16_f32 v213, v186, v187\n"
"v_mfma_f32_16x16x32_bf16 v[96:99], a[128:131], v[216:219], v[96:99]\n"
"v_exp_f32 v191, v191\n"
"v_mfma_f32_16x16x32_bf16 v[116:119], a[132:135], v[216:219], v[116:119]\n"
"v_exp_f32 v192, v192\n"
"v_mfma_f32_16x16x32_bf16 v[136:139], a[136:139], v[216:219], v[136:139]\n"
"v_mfma_f32_16x16x32_bf16 v[156:159], a[140:143], v[216:219], v[156:159]\n"
"v_exp_f32 v193, v193\n"
"v_mfma_f32_16x16x32_bf16 a[160:163], a[164:167], v[216:219], a[160:163]\n"
"s_waitcnt lgkmcnt(0)\n"
"v_mfma_f32_16x16x32_bf16 v[160:163], a[80:83], a[0:3], v[220:223]\n"
"ds_read_b64_tr_b16 a[112:113], v241 offset:0\n"
"s_add_u32 m0, s80, 16384\n"
"v_mfma_f32_16x16x32_bf16 v[160:163], a[84:87], a[20:23], v[160:163]\n"
"v_exp_f32 v194, v194\n"
"v_mfma_f32_16x16x32_bf16 v[160:163], a[88:91], a[40:43], v[160:163]\n"
"ds_read_b64_tr_b16 a[114:115], v241 offset:4096\n"
"v_cvt_pk_bf16_f32 v214, v188, v189\n"
"v_mfma_f32_16x16x32_bf16 v[160:163], a[92:95], a[60:63], v[160:163]\n"
"v_exp_f32 v195, v195\n"
"v_mfma_f32_16x16x32_bf16 v[164:167], a[96:99], a[0:3], v[220:223]\n"
"ds_read_b64_tr_b16 a[116:117], v242 offset:0\n"
"v_cvt_pk_bf16_f32 v215, v190, v191\n"
"v_mfma_f32_16x16x32_bf16 v[164:167], a[100:103], a[20:23], v[164:167]\n"
"v_exp_f32 v196, v196\n"
"v_mfma_f32_16x16x32_bf16 v[164:167], a[104:107], a[40:43], v[164:167]\n"
"ds_read_b64_tr_b16 a[118:119], v242 offset:4096\n"
"v_cvt_pk_bf16_f32 v216, v192, v193\n"
"v_mfma_f32_16x16x32_bf16 v[164:167], a[108:111], a[60:63], v[164:167]\n"
"v_exp_f32 v197, v197\n"
"v_mfma_f32_16x16x32_bf16 v[168:171], a[80:83], a[4:7], v[224:227]\n"
"ds_read_b64_tr_b16 a[120:121], v241 offset:512\n"
"v_cvt_pk_bf16_f32 v217, v194, v195\n"
"v_mfma_f32_16x16x32_bf16 v[168:171], a[84:87], a[24:27], v[168:171]\n"
"v_exp_f32 v198, v198\n"
"v_mfma_f32_16x16x32_bf16 v[168:171], a[88:91], a[44:47], v[168:171]\n"
"ds_read_b64_tr_b16 a[122:123], v241 offset:4608\n"
"v_cvt_pk_bf16_f32 v218, v196, v197\n"
"v_mfma_f32_16x16x32_bf16 v[168:171], a[92:95], a[64:67], v[168:171]\n"
"v_exp_f32 v199, v199\n"
"v_mfma_f32_16x16x32_bf16 v[172:175], a[96:99], a[4:7], v[224:227]\n"
"ds_read_b64_tr_b16 a[124:125], v242 offset:512\n"
"v_cvt_pk_bf16_f32 v219, v198, v199\n"
"v_mfma_f32_16x16x32_bf16 v[172:175], a[100:103], a[24:27], v[172:175]\n"
"buffer_load_dwordx4 v243, s[44:47], 0 offen nt lds\n"
"v_mfma_f32_16x16x32_bf16 v[172:175], a[104:107], a[44:47], v[172:175]\n"
"ds_read_b64_tr_b16 a[126:127], v242 offset:4608\n"
"s_add_u32 m0, s80, 17280\n"
"v_mfma_f32_16x16x32_bf16 v[172:175], a[108:111], a[64:67], v[172:175]\n"
"ds_read_b64_tr_b16 a[128:129], v241 offset:1024\n"
"v_add_u32 v240, 49152, v245\n"
"v_mfma_f32_16x16x32_bf16 v[176:179], a[80:83], a[8:11], v[228:231]\n"
"v_exp_f32 v160, v160\n"
"v_mfma_f32_16x16x32_bf16 v[176:179], a[84:87], a[28:31], v[176:179]\n"
"ds_read_b64_tr_b16 a[130:131], v241 offset:5120\n"
"v_mfma_f32_16x16x32_bf16 v[176:179], a[88:91], a[48:51], v[176:179]\n"
"buffer_load_dwordx4 v243, s[44:47], 0 offen offset:128 nt lds\n"
"v_mfma_f32_16x16x32_bf16 v[176:179], a[92:95], a[68:71], v[176:179]\n"
"ds_read_b64_tr_b16 a[132:133], v242 offset:1024\n"
"s_add_u32 m0, s80, 24576\n"
"v_mfma_f32_16x16x32_bf16 v[180:183], a[96:99], a[8:11], v[228:231]\n"
"v_exp_f32 v161, v161\n"
"v_mfma_f32_16x16x32_bf16 v[180:183], a[100:103], a[28:31], v[180:183]\n"
"ds_read_b64_tr_b16 a[134:135], v242 offset:5120\n"
"v_mfma_f32_16x16x32_bf16 v[180:183], a[104:107], a[48:51], v[180:183]\n"
"v_exp_f32 v162, v162\n"
"v_mfma_f32_16x16x32_bf16 v[180:183], a[108:111], a[68:71], v[180:183]\n"
"buffer_load_dwordx4 v243, s[44:47], s68 offen nt lds\n"
"v_mfma_f32_16x16x32_bf16 v[184:187], a[80:83], a[12:15], v[232:235]\n"
"ds_read_b64_tr_b16 a[136:137], v241 offset:1536\n"
"s_add_u32 m0, s80, 25472\n"
"v_mfma_f32_16x16x32_bf16 v[184:187], a[84:87], a[32:35], v[184:187]\n"
"v_exp_f32 v163, v163\n"
"v_mfma_f32_16x16x32_bf16 v[184:187], a[88:91], a[52:55], v[184:187]\n"
"ds_read_b64_tr_b16 a[138:139], v241 offset:5632\n"
"v_mfma_f32_16x16x32_bf16 v[184:187], a[92:95], a[72:75], v[184:187]\n"
"v_exp_f32 v164, v164\n"
"v_mfma_f32_16x16x32_bf16 v[188:191], a[96:99], a[12:15], v[232:235]\n"
"buffer_load_dwordx4 v243, s[44:47], s68 offen offset:128 nt lds\n"
"v_mfma_f32_16x16x32_bf16 v[188:191], a[100:103], a[32:35], v[188:191]\n"
"ds_read_b64_tr_b16 a[140:141], v242 offset:1536\n"
"v_mfma_f32_16x16x32_bf16 v[188:191], a[104:107], a[52:55], v[188:191]\n"
"v_exp_f32 v165, v165\n"
"v_mfma_f32_16x16x32_bf16 v[188:191], a[108:111], a[72:75], v[188:191]\n"
"ds_read_b64_tr_b16 a[142:143], v242 offset:5632\n"
"v_mfma_f32_16x16x32_bf16 v[192:195], a[80:83], a[16:19], v[236:239]\n"
"v_exp_f32 v166, v166\n"
"v_mfma_f32_16x16x32_bf16 v[192:195], a[84:87], a[36:39], v[192:195]\n"
"v_exp_f32 v167, v167\n"
"v_mfma_f32_16x16x32_bf16 v[192:195], a[88:91], a[56:59], v[192:195]\n"
"v_exp_f32 v168, v168\n"
"v_mfma_f32_16x16x32_bf16 v[192:195], a[92:95], a[76:79], v[192:195]\n"
"v_mfma_f32_16x16x32_bf16 v[196:199], a[96:99], a[16:19], v[236:239]\n"
"v_exp_f32 v169, v169\n"
"v_mfma_f32_16x16x32_bf16 v[196:199], a[100:103], a[36:39], v[196:199]\n"
"v_mfma_f32_16x16x32_bf16 v[196:199], a[104:107], a[56:59], v[196:199]\n"
"v_exp_f32 v170, v170\n"
"v_mfma_f32_16x16x32_bf16 v[196:199], a[108:111], a[76:79], v[196:199]\n"
"s_waitcnt vmcnt(20) lgkmcnt(0)\n"
"s_barrier\n"
"v_mfma_f32_16x16x32_bf16 v[0:3], a[112:115], v[200:203], v[0:3]\n"
"ds_read_b128 a[80:83], v240 offset:0\n"
"s_add_u32 m0, s80, 131072\n"
"v_mfma_f32_16x16x32_bf16 v[20:23], a[116:119], v[200:203], v[20:23]\n"
"v_exp_f32 v171, v171\n"
"v_mfma_f32_16x16x32_bf16 v[40:43], a[120:123], v[200:203], v[40:43]\n"
"buffer_load_dwordx4 v244, s[48:51], 0 offen nt lds\n"
"v_mfma_f32_16x16x32_bf16 v[60:63], a[124:127], v[200:203], v[60:63]\n"
"v_exp_f32 v172, v172\n"
"v_mfma_f32_16x16x32_bf16 v[80:83], a[128:131], v[200:203], v[80:83]\n"
"ds_read_b128 a[84:87], v240 offset:512\n"
"s_add_u32 m0, s80, 131968\n"
"v_mfma_f32_16x16x32_bf16 v[100:103], a[132:135], v[200:203], v[100:103]\n"
"v_exp_f32 v173, v173\n"
"v_mfma_f32_16x16x32_bf16 v[120:123], a[136:139], v[200:203], v[120:123]\n"
"v_exp_f32 v174, v174\n"
"v_mfma_f32_16x16x32_bf16 v[140:143], a[140:143], v[200:203], v[140:143]\n"
"v_exp_f32 v175, v175\n"
"v_mfma_f32_16x16x32_bf16 a[144:147], a[164:167], v[200:203], a[144:147]\n"
"buffer_load_dwordx4 v244, s[48:51], 0 offen offset:128 nt lds\n"
"v_mfma_f32_16x16x32_bf16 v[4:7], a[112:115], v[204:207], v[4:7]\n"
"ds_read_b128 a[88:91], v240 offset:1024\n"
"s_add_u32 m0, s80, 139264\n"
"v_mfma_f32_16x16x32_bf16 v[24:27], a[116:119], v[204:207], v[24:27]\n"
"v_cvt_pk_bf16_f32 v200, v160, v161\n"
"v_cvt_pk_bf16_f32 v201, v162, v163\n"
"v_mfma_f32_16x16x32_bf16 v[44:47], a[120:123], v[204:207], v[44:47]\n"
"v_exp_f32 v176, v176\n"
"v_mfma_f32_16x16x32_bf16 v[64:67], a[124:127], v[204:207], v[64:67]\n"
"v_exp_f32 v177, v177\n"
"v_mfma_f32_16x16x32_bf16 v[84:87], a[128:131], v[204:207], v[84:87]\n"
"ds_read_b128 a[92:95], v240 offset:1536\n"
"v_cvt_pk_bf16_f32 v202, v164, v165\n"
"v_mfma_f32_16x16x32_bf16 v[104:107], a[132:135], v[204:207], v[104:107]\n"
"buffer_load_dwordx4 v244, s[48:51], s68 offen nt lds\n"
"v_mfma_f32_16x16x32_bf16 v[124:127], a[136:139], v[204:207], v[124:127]\n"
"s_add_u32 m0, s80, 140160\n"
"v_cvt_pk_bf16_f32 v203, v166, v167\n"
"v_mfma_f32_16x16x32_bf16 v[144:147], a[140:143], v[204:207], v[144:147]\n"
"v_exp_f32 v178, v178\n"
"v_mfma_f32_16x16x32_bf16 a[148:151], a[164:167], v[204:207], a[148:151]\n"
"ds_read_b128 a[96:99], v240 offset:4096\n"
"v_add_u32 v243, s85, v243\n"
"v_mfma_f32_16x16x32_bf16 v[8:11], a[112:115], v[208:211], v[8:11]\n"
"v_exp_f32 v179, v179\n"
"v_mfma_f32_16x16x32_bf16 v[28:31], a[116:119], v[208:211], v[28:31]\n"
"v_cvt_pk_bf16_f32 v204, v168, v169\n"
"v_cvt_pk_bf16_f32 v205, v170, v171\n"
"v_mfma_f32_16x16x32_bf16 v[48:51], a[120:123], v[208:211], v[48:51]\n"
"buffer_load_dwordx4 v244, s[48:51], s68 offen offset:128 nt lds\n"
"v_mfma_f32_16x16x32_bf16 v[68:71], a[124:127], v[208:211], v[68:71]\n"
"ds_read_b128 a[100:103], v240 offset:4608\n"
"v_cvt_pk_bf16_f32 v206, v172, v173\n"
"v_mfma_f32_16x16x32_bf16 v[88:91], a[128:131], v[208:211], v[88:91]\n"
"v_exp_f32 v180, v180\n"
"v_mfma_f32_16x16x32_bf16 v[108:111], a[132:135], v[208:211], v[108:111]\n"
"v_cvt_pk_bf16_f32 v207, v174, v175\n"
"v_add_u32 v244, s85, v244\n"
"v_mfma_f32_16x16x32_bf16 v[128:131], a[136:139], v[208:211], v[128:131]\n"
"v_exp_f32 v181, v181\n"
"v_mfma_f32_16x16x32_bf16 v[148:151], a[140:143], v[208:211], v[148:151]\n"
"ds_read_b128 a[104:107], v240 offset:5120\n"
"v_add_u32 v241, 81920, v246\n"
"v_mfma_f32_16x16x32_bf16 a[152:155], a[164:167], v[208:211], a[152:155]\n"
"v_exp_f32 v182, v182\n"
"v_mfma_f32_16x16x32_bf16 v[12:15], a[112:115], v[212:215], v[12:15]\n"
"v_cvt_pk_bf16_f32 v208, v176, v177\n"
"v_cvt_pk_bf16_f32 v209, v178, v179\n"
"v_mfma_f32_16x16x32_bf16 v[32:35], a[116:119], v[212:215], v[32:35]\n"
"v_exp_f32 v183, v183\n"
"v_mfma_f32_16x16x32_bf16 v[52:55], a[120:123], v[212:215], v[52:55]\n"
"ds_read_b128 a[108:111], v240 offset:5632\n"
"v_cvt_pk_bf16_f32 v210, v180, v181\n"
"v_mfma_f32_16x16x32_bf16 v[72:75], a[124:127], v[212:215], v[72:75]\n"
"v_exp_f32 v184, v184\n"
"v_mfma_f32_16x16x32_bf16 v[92:95], a[128:131], v[212:215], v[92:95]\n"
"v_cvt_pk_bf16_f32 v211, v182, v183\n"
"v_xor_b32 v242, 32, v241\n"
"v_mfma_f32_16x16x32_bf16 v[112:115], a[132:135], v[212:215], v[112:115]\n"
"v_exp_f32 v185, v185\n"
"v_mfma_f32_16x16x32_bf16 v[132:135], a[136:139], v[212:215], v[132:135]\n"
"v_exp_f32 v186, v186\n"
"v_mfma_f32_16x16x32_bf16 v[152:155], a[140:143], v[212:215], v[152:155]\n"
"v_exp_f32 v187, v187\n"
"v_mfma_f32_16x16x32_bf16 a[156:159], a[164:167], v[212:215], a[156:159]\n"
"v_mfma_f32_16x16x32_bf16 v[16:19], a[112:115], v[216:219], v[16:19]\n"
"v_exp_f32 v188, v188\n"
"v_mfma_f32_16x16x32_bf16 v[36:39], a[116:119], v[216:219], v[36:39]\n"
"v_exp_f32 v189, v189\n"
"v_mfma_f32_16x16x32_bf16 v[56:59], a[120:123], v[216:219], v[56:59]\n"
"v_exp_f32 v190, v190\n"
"v_mfma_f32_16x16x32_bf16 v[76:79], a[124:127], v[216:219], v[76:79]\n"
"v_cvt_pk_bf16_f32 v212, v184, v185\n"
"v_cvt_pk_bf16_f32 v213, v186, v187\n"
"v_mfma_f32_16x16x32_bf16 v[96:99], a[128:131], v[216:219], v[96:99]\n"
"v_exp_f32 v191, v191\n"
"v_mfma_f32_16x16x32_bf16 v[116:119], a[132:135], v[216:219], v[116:119]\n"
"v_exp_f32 v192, v192\n"
"v_mfma_f32_16x16x32_bf16 v[136:139], a[136:139], v[216:219], v[136:139]\n"
"v_mfma_f32_16x16x32_bf16 v[156:159], a[140:143], v[216:219], v[156:159]\n"
"v_exp_f32 v193, v193\n"
"v_mfma_f32_16x16x32_bf16 a[160:163], a[164:167], v[216:219], a[160:163]\n"
"s_waitcnt lgkmcnt(0)\n"
"v_mfma_f32_16x16x32_bf16 v[160:163], a[80:83], a[0:3], v[220:223]\n"
"ds_read_b64_tr_b16 a[112:113], v241 offset:0\n"
"v_mfma_f32_16x16x32_bf16 v[160:163], a[84:87], a[20:23], v[160:163]\n"
"v_exp_f32 v194, v194\n"
"v_mfma_f32_16x16x32_bf16 v[160:163], a[88:91], a[40:43], v[160:163]\n"
"ds_read_b64_tr_b16 a[114:115], v241 offset:4096\n"
"v_cvt_pk_bf16_f32 v214, v188, v189\n"
"v_mfma_f32_16x16x32_bf16 v[160:163], a[92:95], a[60:63], v[160:163]\n"
"v_exp_f32 v195, v195\n"
"v_mfma_f32_16x16x32_bf16 v[164:167], a[96:99], a[0:3], v[220:223]\n"
"ds_read_b64_tr_b16 a[116:117], v242 offset:0\n"
"v_cvt_pk_bf16_f32 v215, v190, v191\n"
"v_mfma_f32_16x16x32_bf16 v[164:167], a[100:103], a[20:23], v[164:167]\n"
"v_exp_f32 v196, v196\n"
"v_mfma_f32_16x16x32_bf16 v[164:167], a[104:107], a[40:43], v[164:167]\n"
"ds_read_b64_tr_b16 a[118:119], v242 offset:4096\n"
"v_cvt_pk_bf16_f32 v216, v192, v193\n"
"v_mfma_f32_16x16x32_bf16 v[164:167], a[108:111], a[60:63], v[164:167]\n"
"v_exp_f32 v197, v197\n"
"v_mfma_f32_16x16x32_bf16 v[168:171], a[80:83], a[4:7], v[224:227]\n"
"ds_read_b64_tr_b16 a[120:121], v241 offset:512\n"
"v_cvt_pk_bf16_f32 v217, v194, v195\n"
"v_mfma_f32_16x16x32_bf16 v[168:171], a[84:87], a[24:27], v[168:171]\n"
"v_exp_f32 v198, v198\n"
"v_mfma_f32_16x16x32_bf16 v[168:171], a[88:91], a[44:47], v[168:171]\n"
"ds_read_b64_tr_b16 a[122:123], v241 offset:4608\n"
"v_cvt_pk_bf16_f32 v218, v196, v197\n"
"v_mfma_f32_16x16x32_bf16 v[168:171], a[92:95], a[64:67], v[168:171]\n"
"v_exp_f32 v199, v199\n"
"v_mfma_f32_16x16x32_bf16 v[172:175], a[96:99], a[4:7], v[224:227]\n"
"ds_read_b64_tr_b16 a[124:125], v242 offset:512\n"
"v_cvt_pk_bf16_f32 v219, v198, v199\n"
"v_mfma_f32_16x16x32_bf16 v[172:175], a[100:103], a[24:27], v[172:175]\n"
"v_mfma_f32_16x16x32_bf16 v[172:175], a[104:107], a[44:47], v[172:175]\n"
"ds_read_b64_tr_b16 a[126:127], v242 offset:4608\n"
"v_mfma_f32_16x16x32_bf16 v[172:175], a[108:111], a[64:67], v[172:175]\n"
"ds_read_b64_tr_b16 a[128:129], v241 offset:1024\n"
"v_add_u32 v240, 65536, v245\n"
"v_mfma_f32_16x16x32_bf16 v[176:179], a[80:83], a[8:11], v[228:231]\n"
"v_exp_f32 v160, v160\n"
"v_mfma_f32_16x16x32_bf16 v[176:179], a[84:87], a[28:31], v[176:179]\n"
"ds_read_b64_tr_b16 a[130:131], v241 offset:5120\n"
"v_mfma_f32_16x16x32_bf16 v[176:179], a[88:91], a[48:51], v[176:179]\n"
"v_mfma_f32_16x16x32_bf16 v[176:179], a[92:95], a[68:71], v[176:179]\n"
"ds_read_b64_tr_b16 a[132:133], v242 offset:1024\n"
"v_mfma_f32_16x16x32_bf16 v[180:183], a[96:99], a[8:11], v[228:231]\n"
"v_exp_f32 v161, v161\n"
"v_mfma_f32_16x16x32_bf16 v[180:183], a[100:103], a[28:31], v[180:183]\n"
"ds_read_b64_tr_b16 a[134:135], v242 offset:5120\n"
"v_mfma_f32_16x16x32_bf16 v[180:183], a[104:107], a[48:51], v[180:183]\n"
"v_exp_f32 v162, v162\n"
"v_mfma_f32_16x16x32_bf16 v[180:183], a[108:111], a[68:71], v[180:183]\n"
"v_mfma_f32_16x16x32_bf16 v[184:187], a[80:83], a[12:15], v[232:235]\n"
"ds_read_b64_tr_b16 a[136:137], v241 offset:1536\n"
"v_mfma_f32_16x16x32_bf16 v[184:187], a[84:87], a[32:35], v[184:187]\n"
"v_exp_f32 v163, v163\n"
"v_mfma_f32_16x16x32_bf16 v[184:187], a[88:91], a[52:55], v[184:187]\n"
"ds_read_b64_tr_b16 a[138:139], v241 offset:5632\n"
"v_mfma_f32_16x16x32_bf16 v[184:187], a[92:95], a[72:75], v[184:187]\n"
"v_exp_f32 v164, v164\n"
"v_mfma_f32_16x16x32_bf16 v[188:191], a[96:99], a[12:15], v[232:235]\n"
"v_mfma_f32_16x16x32_bf16 v[188:191], a[100:103], a[32:35], v[188:191]\n"
"ds_read_b64_tr_b16 a[140:141], v242 offset:1536\n"
"v_mfma_f32_16x16x32_bf16 v[188:191], a[104:107], a[52:55], v[188:191]\n"
"v_exp_f32 v165, v165\n"
"v_mfma_f32_16x16x32_bf16 v[188:191], a[108:111], a[72:75], v[188:191]\n"
"ds_read_b64_tr_b16 a[142:143], v242 offset:5632\n"
"v_mfma_f32_16x16x32_bf16 v[192:195], a[80:83], a[16:19], v[236:239]\n"
"v_exp_f32 v166, v166\n"
"v_mfma_f32_16x16x32_bf16 v[192:195], a[84:87], a[36:39], v[192:195]\n"
"v_exp_f32 v167, v167\n"
"v_mfma_f32_16x16x32_bf16 v[192:195], a[88:91], a[56:59], v[192:195]\n"
"v_exp_f32 v168, v168\n"
"v_mfma_f32_16x16x32_bf16 v[192:195], a[92:95], a[76:79], v[192:195]\n"
"v_mfma_f32_16x16x32_bf16 v[196:199], a[96:99], a[16:19], v[236:239]\n"
"v_exp_f32 v169, v169\n"
"v_mfma_f32_16x16x32_bf16 v[196:199], a[100:103], a[36:39], v[196:199]\n"
"v_mfma_f32_16x16x32_bf16 v[196:199], a[104:107], a[56:59], v[196:199]\n"
"v_exp_f32 v170, v170\n"
"v_mfma_f32_16x16x32_bf16 v[196:199], a[108:111], a[76:79], v[196:199]\n"
"s_waitcnt vmcnt(16) lgkmcnt(0)\n"
"s_barrier\n"
"v_mfma_f32_16x16x32_bf16 v[0:3], a[112:115], v[200:203], v[0:3]\n"
"ds_read_b128 a[80:83], v240 offset:0\n"
"s_add_u32 m0, s80, 147456\n"
"v_mfma_f32_16x16x32_bf16 v[20:23], a[116:119], v[200:203], v[20:23]\n"
"v_exp_f32 v171, v171\n"
"v_mfma_f32_16x16x32_bf16 v[40:43], a[120:123], v[200:203], v[40:43]\n"
"buffer_load_dwordx4 v244, s[48:51], 0 offen nt lds\n"
"v_mfma_f32_16x16x32_bf16 v[60:63], a[124:127], v[200:203], v[60:63]\n"
"v_exp_f32 v172, v172\n"
"v_mfma_f32_16x16x32_bf16 v[80:83], a[128:131], v[200:203], v[80:83]\n"
"ds_read_b128 a[84:87], v240 offset:512\n"
"s_add_u32 m0, s80, 148352\n"
"v_mfma_f32_16x16x32_bf16 v[100:103], a[132:135], v[200:203], v[100:103]\n"
"v_exp_f32 v173, v173\n"
"v_mfma_f32_16x16x32_bf16 v[120:123], a[136:139], v[200:203], v[120:123]\n"
"v_exp_f32 v174, v174\n"
"v_mfma_f32_16x16x32_bf16 v[140:143], a[140:143], v[200:203], v[140:143]\n"
"v_exp_f32 v175, v175\n"
"v_mfma_f32_16x16x32_bf16 a[144:147], a[164:167], v[200:203], a[144:147]\n"
"buffer_load_dwordx4 v244, s[48:51], 0 offen offset:128 nt lds\n"
"v_mfma_f32_16x16x32_bf16 v[4:7], a[112:115], v[204:207], v[4:7]\n"
"ds_read_b128 a[88:91], v240 offset:1024\n"
"s_add_u32 m0, s80, 155648\n"
"v_mfma_f32_16x16x32_bf16 v[24:27], a[116:119], v[204:207], v[24:27]\n"
"v_cvt_pk_bf16_f32 v200, v160, v161\n"
"v_cvt_pk_bf16_f32 v201, v162, v163\n"
"v_mfma_f32_16x16x32_bf16 v[44:47], a[120:123], v[204:207], v[44:47]\n"
"v_exp_f32 v176, v176\n"
"v_mfma_f32_16x16x32_bf16 v[64:67], a[124:127], v[204:207], v[64:67]\n"
"v_exp_f32 v177, v177\n"
"v_mfma_f32_16x16x32_bf16 v[84:87], a[128:131], v[204:207], v[84:87]\n"
"ds_read_b128 a[92:95], v240 offset:1536\n"
"v_cvt_pk_bf16_f32 v202, v164, v165\n"
"v_mfma_f32_16x16x32_bf16 v[104:107], a[132:135], v[204:207], v[104:107]\n"
"buffer_load_dwordx4 v244, s[48:51], s68 offen nt lds\n"
"v_mfma_f32_16x16x32_bf16 v[124:127], a[136:139], v[204:207], v[124:127]\n"
"s_add_u32 m0, s80, 156544\n"
"v_cvt_pk_bf16_f32 v203, v166, v167\n"
"v_mfma_f32_16x16x32_bf16 v[144:147], a[140:143], v[204:207], v[144:147]\n"
"v_exp_f32 v178, v178\n"
"v_mfma_f32_16x16x32_bf16 a[148:151], a[164:167], v[204:207], a[148:151]\n"
"ds_read_b128 a[96:99], v240 offset:4096\n"
"v_mfma_f32_16x16x32_bf16 v[8:11], a[112:115], v[208:211], v[8:11]\n"
"v_exp_f32 v179, v179\n"
"v_mfma_f32_16x16x32_bf16 v[28:31], a[116:119], v[208:211], v[28:31]\n"
"v_cvt_pk_bf16_f32 v204, v168, v169\n"
"v_cvt_pk_bf16_f32 v205, v170, v171\n"
"v_mfma_f32_16x16x32_bf16 v[48:51], a[120:123], v[208:211], v[48:51]\n"
"buffer_load_dwordx4 v244, s[48:51], s68 offen offset:128 nt lds\n"
"v_mfma_f32_16x16x32_bf16 v[68:71], a[124:127], v[208:211], v[68:71]\n"
"ds_read_b128 a[100:103], v240 offset:4608\n"
"v_cvt_pk_bf16_f32 v206, v172, v173\n"
"v_mfma_f32_16x16x32_bf16 v[88:91], a[128:131], v[208:211], v[88:91]\n"
"v_exp_f32 v180, v180\n"
"v_mfma_f32_16x16x32_bf16 v[108:111], a[132:135], v[208:211], v[108:111]\n"
"v_cvt_pk_bf16_f32 v207, v174, v175\n"
"v_add_u32 v244, s85, v244\n"
"v_mfma_f32_16x16x32_bf16 v[128:131], a[136:139], v[208:211], v[128:131]\n"
"v_exp_f32 v181, v181\n"
"v_mfma_f32_16x16x32_bf16 v[148:151], a[140:143], v[208:211], v[148:151]\n"
"ds_read_b128 a[104:107], v240 offset:5120\n"
"v_add_u32 v241, 98304, v246\n"
"v_mfma_f32_16x16x32_bf16 a[152:155], a[164:167], v[208:211], a[152:155]\n"
"v_exp_f32 v182, v182\n"
"v_mfma_f32_16x16x32_bf16 v[12:15], a[112:115], v[212:215], v[12:15]\n"
"v_cvt_pk_bf16_f32 v208, v176, v177\n"
"v_cvt_pk_bf16_f32 v209, v178, v179\n"
"v_mfma_f32_16x16x32_bf16 v[32:35], a[116:119], v[212:215], v[32:35]\n"
"v_exp_f32 v183, v183\n"
"v_mfma_f32_16x16x32_bf16 v[52:55], a[120:123], v[212:215], v[52:55]\n"
"ds_read_b128 a[108:111], v240 offset:5632\n"
"v_cvt_pk_bf16_f32 v210, v180, v181\n"
"v_mfma_f32_16x16x32_bf16 v[72:75], a[124:127], v[212:215], v[72:75]\n"
"v_exp_f32 v184, v184\n"
"v_mfma_f32_16x16x32_bf16 v[92:95], a[128:131], v[212:215], v[92:95]\n"
"v_cvt_pk_bf16_f32 v211, v182, v183\n"
"v_xor_b32 v242, 32, v241\n"
"v_mfma_f32_16x16x32_bf16 v[112:115], a[132:135], v[212:215], v[112:115]\n"
"v_exp_f32 v185, v185\n"
"v_mfma_f32_16x16x32_bf16 v[132:135], a[136:139], v[212:215], v[132:135]\n"
"v_exp_f32 v186, v186\n"
"v_mfma_f32_16x16x32_bf16 v[152:155], a[140:143], v[212:215], v[152:155]\n"
"v_exp_f32 v187, v187\n"
"v_mfma_f32_16x16x32_bf16 a[156:159], a[164:167], v[212:215], a[156:159]\n"
"v_mfma_f32_16x16x32_bf16 v[16:19], a[112:115], v[216:219], v[16:19]\n"
"v_exp_f32 v188, v188\n"
"v_mfma_f32_16x16x32_bf16 v[36:39], a[116:119], v[216:219], v[36:39]\n"
"v_exp_f32 v189, v189\n"
"v_mfma_f32_16x16x32_bf16 v[56:59], a[120:123], v[216:219], v[56:59]\n"
"v_exp_f32 v190, v190\n"
"v_mfma_f32_16x16x32_bf16 v[76:79], a[124:127], v[216:219], v[76:79]\n"
"v_cvt_pk_bf16_f32 v212, v184, v185\n"
"v_cvt_pk_bf16_f32 v213, v186, v187\n"
"v_mfma_f32_16x16x32_bf16 v[96:99], a[128:131], v[216:219], v[96:99]\n"
"v_exp_f32 v191, v191\n"
"v_mfma_f32_16x16x32_bf16 v[116:119], a[132:135], v[216:219], v[116:119]\n"
"v_exp_f32 v192, v192\n"
"v_mfma_f32_16x16x32_bf16 v[136:139], a[136:139], v[216:219], v[136:139]\n"
"v_mfma_f32_16x16x32_bf16 v[156:159], a[140:143], v[216:219], v[156:159]\n"
"v_exp_f32 v193, v193\n"
"v_mfma_f32_16x16x32_bf16 a[160:163], a[164:167], v[216:219], a[160:163]\n"
"s_waitcnt lgkmcnt(0)\n"
"v_mfma_f32_16x16x32_bf16 v[160:163], a[80:83], a[0:3], v[220:223]\n"
"ds_read_b64_tr_b16 a[112:113], v241 offset:0\n"
"s_add_u32 m0, s80, 32768\n"
"v_mfma_f32_16x16x32_bf16 v[160:163], a[84:87], a[20:23], v[160:163]\n"
"v_exp_f32 v194, v194\n"
"v_mfma_f32_16x16x32_bf16 v[160:163], a[88:91], a[40:43], v[160:163]\n"
"ds_read_b64_tr_b16 a[114:115], v241 offset:4096\n"
"v_cvt_pk_bf16_f32 v214, v188, v189\n"
"v_mfma_f32_16x16x32_bf16 v[160:163], a[92:95], a[60:63], v[160:163]\n"
"v_exp_f32 v195, v195\n"
"v_mfma_f32_16x16x32_bf16 v[164:167], a[96:99], a[0:3], v[220:223]\n"
"ds_read_b64_tr_b16 a[116:117], v242 offset:0\n"
"v_cvt_pk_bf16_f32 v215, v190, v191\n"
"v_mfma_f32_16x16x32_bf16 v[164:167], a[100:103], a[20:23], v[164:167]\n"
"v_exp_f32 v196, v196\n"
"v_mfma_f32_16x16x32_bf16 v[164:167], a[104:107], a[40:43], v[164:167]\n"
"ds_read_b64_tr_b16 a[118:119], v242 offset:4096\n"
"v_cvt_pk_bf16_f32 v216, v192, v193\n"
"v_mfma_f32_16x16x32_bf16 v[164:167], a[108:111], a[60:63], v[164:167]\n"
"v_exp_f32 v197, v197\n"
"v_mfma_f32_16x16x32_bf16 v[168:171], a[80:83], a[4:7], v[224:227]\n"
"ds_read_b64_tr_b16 a[120:121], v241 offset:512\n"
"v_cvt_pk_bf16_f32 v217, v194, v195\n"
"v_mfma_f32_16x16x32_bf16 v[168:171], a[84:87], a[24:27], v[168:171]\n"
"v_exp_f32 v198, v198\n"
"v_mfma_f32_16x16x32_bf16 v[168:171], a[88:91], a[44:47], v[168:171]\n"
"ds_read_b64_tr_b16 a[122:123], v241 offset:4608\n"
"v_cvt_pk_bf16_f32 v218, v196, v197\n"
"v_mfma_f32_16x16x32_bf16 v[168:171], a[92:95], a[64:67], v[168:171]\n"
"v_exp_f32 v199, v199\n"
"v_mfma_f32_16x16x32_bf16 v[172:175], a[96:99], a[4:7], v[224:227]\n"
"ds_read_b64_tr_b16 a[124:125], v242 offset:512\n"
"v_cvt_pk_bf16_f32 v219, v198, v199\n"
"v_mfma_f32_16x16x32_bf16 v[172:175], a[100:103], a[24:27], v[172:175]\n"
"buffer_load_dwordx4 v243, s[44:47], 0 offen nt lds\n"
"v_mfma_f32_16x16x32_bf16 v[172:175], a[104:107], a[44:47], v[172:175]\n"
"ds_read_b64_tr_b16 a[126:127], v242 offset:4608\n"
"s_add_u32 m0, s80, 33664\n"
"v_mfma_f32_16x16x32_bf16 v[172:175], a[108:111], a[64:67], v[172:175]\n"
"ds_read_b64_tr_b16 a[128:129], v241 offset:1024\n"
"v_add_u32 v240, 0, v245\n"
"v_mfma_f32_16x16x32_bf16 v[176:179], a[80:83], a[8:11], v[228:231]\n"
"v_exp_f32 v160, v160\n"
"v_mfma_f32_16x16x32_bf16 v[176:179], a[84:87], a[28:31], v[176:179]\n"
"ds_read_b64_tr_b16 a[130:131], v241 offset:5120\n"
"v_mfma_f32_16x16x32_bf16 v[176:179], a[88:91], a[48:51], v[176:179]\n"
"buffer_load_dwordx4 v243, s[44:47], 0 offen offset:128 nt lds\n"
"v_mfma_f32_16x16x32_bf16 v[176:179], a[92:95], a[68:71], v[176:179]\n"
"ds_read_b64_tr_b16 a[132:133], v242 offset:1024\n"
"s_add_u32 m0, s80, 40960\n"
"v_mfma_f32_16x16x32_bf16 v[180:183], a[96:99], a[8:11], v[228:231]\n"
"v_exp_f32 v161, v161\n"
"v_mfma_f32_16x16x32_bf16 v[180:183], a[100:103], a[28:31], v[180:183]\n"
"ds_read_b64_tr_b16 a[134:135], v242 offset:5120\n"
"v_mfma_f32_16x16x32_bf16 v[180:183], a[104:107], a[48:51], v[180:183]\n"
"v_exp_f32 v162, v162\n"
"v_mfma_f32_16x16x32_bf16 v[180:183], a[108:111], a[68:71], v[180:183]\n"
"buffer_load_dwordx4 v243, s[44:47], s68 offen nt lds\n"
"v_mfma_f32_16x16x32_bf16 v[184:187], a[80:83], a[12:15], v[232:235]\n"
"ds_read_b64_tr_b16 a[136:137], v241 offset:1536\n"
"s_add_u32 m0, s80, 41856\n"
"v_mfma_f32_16x16x32_bf16 v[184:187], a[84:87], a[32:35], v[184:187]\n"
"v_exp_f32 v163, v163\n"
"v_mfma_f32_16x16x32_bf16 v[184:187], a[88:91], a[52:55], v[184:187]\n"
"ds_read_b64_tr_b16 a[138:139], v241 offset:5632\n"
"v_mfma_f32_16x16x32_bf16 v[184:187], a[92:95], a[72:75], v[184:187]\n"
"v_exp_f32 v164, v164\n"
"v_mfma_f32_16x16x32_bf16 v[188:191], a[96:99], a[12:15], v[232:235]\n"
"buffer_load_dwordx4 v243, s[44:47], s68 offen offset:128 nt lds\n"
"v_mfma_f32_16x16x32_bf16 v[188:191], a[100:103], a[32:35], v[188:191]\n"
"ds_read_b64_tr_b16 a[140:141], v242 offset:1536\n"
"v_mfma_f32_16x16x32_bf16 v[188:191], a[104:107], a[52:55], v[188:191]\n"
"v_exp_f32 v165, v165\n"
"v_mfma_f32_16x16x32_bf16 v[188:191], a[108:111], a[72:75], v[188:191]\n"
"ds_read_b64_tr_b16 a[142:143], v242 offset:5632\n"
"v_mfma_f32_16x16x32_bf16 v[192:195], a[80:83], a[16:19], v[236:239]\n"
"v_exp_f32 v166, v166\n"
"v_mfma_f32_16x16x32_bf16 v[192:195], a[84:87], a[36:39], v[192:195]\n"
"v_exp_f32 v167, v167\n"
"v_mfma_f32_16x16x32_bf16 v[192:195], a[88:91], a[56:59], v[192:195]\n"
"v_exp_f32 v168, v168\n"
"v_mfma_f32_16x16x32_bf16 v[192:195], a[92:95], a[76:79], v[192:195]\n"
"v_mfma_f32_16x16x32_bf16 v[196:199], a[96:99], a[16:19], v[236:239]\n"
"v_exp_f32 v169, v169\n"
"v_mfma_f32_16x16x32_bf16 v[196:199], a[100:103], a[36:39], v[196:199]\n"
"v_mfma_f32_16x16x32_bf16 v[196:199], a[104:107], a[56:59], v[196:199]\n"
"v_exp_f32 v170, v170\n"
"v_mfma_f32_16x16x32_bf16 v[196:199], a[108:111], a[76:79], v[196:199]\n"
"s_waitcnt vmcnt(16) lgkmcnt(0)\n"
"s_barrier\n"
"v_mfma_f32_16x16x32_bf16 v[0:3], a[112:115], v[200:203], v[0:3]\n"
"ds_read_b128 a[80:83], v240 offset:0\n"
"s_add_u32 m0, s80, 81920\n"
"v_mfma_f32_16x16x32_bf16 v[20:23], a[116:119], v[200:203], v[20:23]\n"
"v_exp_f32 v171, v171\n"
"v_mfma_f32_16x16x32_bf16 v[40:43], a[120:123], v[200:203], v[40:43]\n"
"buffer_load_dwordx4 v244, s[48:51], 0 offen nt lds\n"
"v_mfma_f32_16x16x32_bf16 v[60:63], a[124:127], v[200:203], v[60:63]\n"
"v_exp_f32 v172, v172\n"
"v_mfma_f32_16x16x32_bf16 v[80:83], a[128:131], v[200:203], v[80:83]\n"
"ds_read_b128 a[84:87], v240 offset:512\n"
"s_add_u32 m0, s80, 82816\n"
"v_mfma_f32_16x16x32_bf16 v[100:103], a[132:135], v[200:203], v[100:103]\n"
"v_exp_f32 v173, v173\n"
"v_mfma_f32_16x16x32_bf16 v[120:123], a[136:139], v[200:203], v[120:123]\n"
"v_exp_f32 v174, v174\n"
"v_mfma_f32_16x16x32_bf16 v[140:143], a[140:143], v[200:203], v[140:143]\n"
"v_exp_f32 v175, v175\n"
"v_mfma_f32_16x16x32_bf16 a[144:147], a[164:167], v[200:203], a[144:147]\n"
"buffer_load_dwordx4 v244, s[48:51], 0 offen offset:128 nt lds\n"
"v_mfma_f32_16x16x32_bf16 v[4:7], a[112:115], v[204:207], v[4:7]\n"
"ds_read_b128 a[88:91], v240 offset:1024\n"
"s_add_u32 m0, s80, 90112\n"
"v_mfma_f32_16x16x32_bf16 v[24:27], a[116:119], v[204:207], v[24:27]\n"
"v_cvt_pk_bf16_f32 v200, v160, v161\n"
"v_cvt_pk_bf16_f32 v201, v162, v163\n"
"v_mfma_f32_16x16x32_bf16 v[44:47], a[120:123], v[204:207], v[44:47]\n"
"v_exp_f32 v176, v176\n"
"v_mfma_f32_16x16x32_bf16 v[64:67], a[124:127], v[204:207], v[64:67]\n"
"v_exp_f32 v177, v177\n"
"v_mfma_f32_16x16x32_bf16 v[84:87], a[128:131], v[204:207], v[84:87]\n"
"ds_read_b128 a[92:95], v240 offset:1536\n"
"v_cvt_pk_bf16_f32 v202, v164, v165\n"
"v_mfma_f32_16x16x32_bf16 v[104:107], a[132:135], v[204:207], v[104:107]\n"
"buffer_load_dwordx4 v244, s[48:51], s68 offen nt lds\n"
"v_mfma_f32_16x16x32_bf16 v[124:127], a[136:139], v[204:207], v[124:127]\n"
"s_add_u32 m0, s80, 91008\n"
"v_cvt_pk_bf16_f32 v203, v166, v167\n"
"v_mfma_f32_16x16x32_bf16 v[144:147], a[140:143], v[204:207], v[144:147]\n"
"v_exp_f32 v178, v178\n"
"v_mfma_f32_16x16x32_bf16 a[148:151], a[164:167], v[204:207], a[148:151]\n"
"ds_read_b128 a[96:99], v240 offset:4096\n"
"v_add_u32 v243, s85, v243\n"
"v_mfma_f32_16x16x32_bf16 v[8:11], a[112:115], v[208:211], v[8:11]\n"
"v_exp_f32 v179, v179\n"
"v_mfma_f32_16x16x32_bf16 v[28:31], a[116:119], v[208:211], v[28:31]\n"
"v_cvt_pk_bf16_f32 v204, v168, v169\n"
"v_cvt_pk_bf16_f32 v205, v170, v171\n"
"v_mfma_f32_16x16x32_bf16 v[48:51], a[120:123], v[208:211], v[48:51]\n"
"buffer_load_dwordx4 v244, s[48:51], s68 offen offset:128 nt lds\n"
"v_mfma_f32_16x16x32_bf16 v[68:71], a[124:127], v[208:211], v[68:71]\n"
"ds_read_b128 a[100:103], v240 offset:4608\n"
"v_cvt_pk_bf16_f32 v206, v172, v173\n"
"v_mfma_f32_16x16x32_bf16 v[88:91], a[128:131], v[208:211], v[88:91]\n"
"v_exp_f32 v180, v180\n"
"v_mfma_f32_16x16x32_bf16 v[108:111], a[132:135], v[208:211], v[108:111]\n"
"v_cvt_pk_bf16_f32 v207, v174, v175\n"
"v_add_u32 v244, s85, v244\n"
"v_mfma_f32_16x16x32_bf16 v[128:131], a[136:139], v[208:211], v[128:131]\n"
"v_exp_f32 v181, v181\n"
"v_mfma_f32_16x16x32_bf16 v[148:151], a[140:143], v[208:211], v[148:151]\n"
"ds_read_b128 a[104:107], v240 offset:5120\n"
"v_add_u32 v241, 114688, v246\n"
"v_mfma_f32_16x16x32_bf16 a[152:155], a[164:167], v[208:211], a[152:155]\n"
"v_exp_f32 v182, v182\n"
"v_mfma_f32_16x16x32_bf16 v[12:15], a[112:115], v[212:215], v[12:15]\n"
"v_cvt_pk_bf16_f32 v208, v176, v177\n"
"v_cvt_pk_bf16_f32 v209, v178, v179\n"
"v_mfma_f32_16x16x32_bf16 v[32:35], a[116:119], v[212:215], v[32:35]\n"
"v_exp_f32 v183, v183\n"
"v_mfma_f32_16x16x32_bf16 v[52:55], a[120:123], v[212:215], v[52:55]\n"
"ds_read_b128 a[108:111], v240 offset:5632\n"
"v_cvt_pk_bf16_f32 v210, v180, v181\n"
"v_mfma_f32_16x16x32_bf16 v[72:75], a[124:127], v[212:215], v[72:75]\n"
"v_exp_f32 v184, v184\n"
"v_mfma_f32_16x16x32_bf16 v[92:95], a[128:131], v[212:215], v[92:95]\n"
"v_cvt_pk_bf16_f32 v211, v182, v183\n"
"v_xor_b32 v242, 32, v241\n"
"v_mfma_f32_16x16x32_bf16 v[112:115], a[132:135], v[212:215], v[112:115]\n"
"v_exp_f32 v185, v185\n"
"v_mfma_f32_16x16x32_bf16 v[132:135], a[136:139], v[212:215], v[132:135]\n"
"v_exp_f32 v186, v186\n"
"v_mfma_f32_16x16x32_bf16 v[152:155], a[140:143], v[212:215], v[152:155]\n"
"v_exp_f32 v187, v187\n"
"v_mfma_f32_16x16x32_bf16 a[156:159], a[164:167], v[212:215], a[156:159]\n"
"v_mfma_f32_16x16x32_bf16 v[16:19], a[112:115], v[216:219], v[16:19]\n"
"v_exp_f32 v188, v188\n"
"v_mfma_f32_16x16x32_bf16 v[36:39], a[116:119], v[216:219], v[36:39]\n"
"v_exp_f32 v189, v189\n"
"v_mfma_f32_16x16x32_bf16 v[56:59], a[120:123], v[216:219], v[56:59]\n"
"v_exp_f32 v190, v190\n"
"v_mfma_f32_16x16x32_bf16 v[76:79], a[124:127], v[216:219], v[76:79]\n"
"v_cvt_pk_bf16_f32 v212, v184, v185\n"
"v_cvt_pk_bf16_f32 v213, v186, v187\n"
"v_mfma_f32_16x16x32_bf16 v[96:99], a[128:131], v[216:219], v[96:99]\n"
"v_exp_f32 v191, v191\n"
"v_mfma_f32_16x16x32_bf16 v[116:119], a[132:135], v[216:219], v[116:119]\n"
"v_exp_f32 v192, v192\n"
"v_mfma_f32_16x16x32_bf16 v[136:139], a[136:139], v[216:219], v[136:139]\n"
"v_mfma_f32_16x16x32_bf16 v[156:159], a[140:143], v[216:219], v[156:159]\n"
"v_exp_f32 v193, v193\n"
"v_mfma_f32_16x16x32_bf16 a[160:163], a[164:167], v[216:219], a[160:163]\n"
"s_waitcnt lgkmcnt(0)\n"
"v_mfma_f32_16x16x32_bf16 v[160:163], a[80:83], a[0:3], v[220:223]\n"
"ds_read_b64_tr_b16 a[112:113], v241 offset:0\n"
"s_add_u32 m0, s80, 49152\n"
"v_mfma_f32_16x16x32_bf16 v[160:163], a[84:87], a[20:23], v[160:163]\n"
"v_exp_f32 v194, v194\n"
"v_mfma_f32_16x16x32_bf16 v[160:163], a[88:91], a[40:43], v[160:163]\n"
"ds_read_b64_tr_b16 a[114:115], v241 offset:4096\n"
"v_cvt_pk_bf16_f32 v214, v188, v189\n"
"v_mfma_f32_16x16x32_bf16 v[160:163], a[92:95], a[60:63], v[160:163]\n"
"v_exp_f32 v195, v195\n"
"v_mfma_f32_16x16x32_bf16 v[164:167], a[96:99], a[0:3], v[220:223]\n"
"ds_read_b64_tr_b16 a[116:117], v242 offset:0\n"
"v_cvt_pk_bf16_f32 v215, v190, v191\n"
"v_mfma_f32_16x16x32_bf16 v[164:167], a[100:103], a[20:23], v[164:167]\n"
"v_exp_f32 v196, v196\n"
"v_mfma_f32_16x16x32_bf16 v[164:167], a[104:107], a[40:43], v[164:167]\n"
"ds_read_b64_tr_b16 a[118:119], v242 offset:4096\n"
"v_cvt_pk_bf16_f32 v216, v192, v193\n"
"v_mfma_f32_16x16x32_bf16 v[164:167], a[108:111], a[60:63], v[164:167]\n"
"v_exp_f32 v197, v197\n"
"v_mfma_f32_16x16x32_bf16 v[168:171], a[80:83], a[4:7], v[224:227]\n"
"ds_read_b64_tr_b16 a[120:121], v241 offset:512\n"
"v_cvt_pk_bf16_f32 v217, v194, v195\n"
"v_mfma_f32_16x16x32_bf16 v[168:171], a[84:87], a[24:27], v[168:171]\n"
"v_exp_f32 v198, v198\n"
"v_mfma_f32_16x16x32_bf16 v[168:171], a[88:91], a[44:47], v[168:171]\n"
"ds_read_b64_tr_b16 a[122:123], v241 offset:4608\n"
"v_cvt_pk_bf16_f32 v218, v196, v197\n"
"v_mfma_f32_16x16x32_bf16 v[168:171], a[92:95], a[64:67], v[168:171]\n"
"v_exp_f32 v199, v199\n"
"v_mfma_f32_16x16x32_bf16 v[172:175], a[96:99], a[4:7], v[224:227]\n"
"ds_read_b64_tr_b16 a[124:125], v242 offset:512\n"
"v_cvt_pk_bf16_f32 v219, v198, v199\n"
"v_mfma_f32_16x16x32_bf16 v[172:175], a[100:103], a[24:27], v[172:175]\n"
"buffer_load_dwordx4 v243, s[44:47], 0 offen nt lds\n"
"v_mfma_f32_16x16x32_bf16 v[172:175], a[104:107], a[44:47], v[172:175]\n"
"ds_read_b64_tr_b16 a[126:127], v242 offset:4608\n"
"s_add_u32 m0, s80, 50048\n"
"v_mfma_f32_16x16x32_bf16 v[172:175], a[108:111], a[64:67], v[172:175]\n"
"ds_read_b64_tr_b16 a[128:129], v241 offset:1024\n"
"v_add_u32 v240, 16384, v245\n"
"v_mfma_f32_16x16x32_bf16 v[176:179], a[80:83], a[8:11], v[228:231]\n"
"v_exp_f32 v160, v160\n"
"v_mfma_f32_16x16x32_bf16 v[176:179], a[84:87], a[28:31], v[176:179]\n"
"ds_read_b64_tr_b16 a[130:131], v241 offset:5120\n"
"v_mfma_f32_16x16x32_bf16 v[176:179], a[88:91], a[48:51], v[176:179]\n"
"buffer_load_dwordx4 v243, s[44:47], 0 offen offset:128 nt lds\n"
"v_mfma_f32_16x16x32_bf16 v[176:179], a[92:95], a[68:71], v[176:179]\n"
"ds_read_b64_tr_b16 a[132:133], v242 offset:1024\n"
"s_add_u32 m0, s80, 57344\n"
"v_mfma_f32_16x16x32_bf16 v[180:183], a[96:99], a[8:11], v[228:231]\n"
"v_exp_f32 v161, v161\n"
"v_mfma_f32_16x16x32_bf16 v[180:183], a[100:103], a[28:31], v[180:183]\n"
"ds_read_b64_tr_b16 a[134:135], v242 offset:5120\n"
"v_mfma_f32_16x16x32_bf16 v[180:183], a[104:107], a[48:51], v[180:183]\n"
"v_exp_f32 v162, v162\n"
"v_mfma_f32_16x16x32_bf16 v[180:183], a[108:111], a[68:71], v[180:183]\n"
"buffer_load_dwordx4 v243, s[44:47], s68 offen nt lds\n"
"v_mfma_f32_16x16x32_bf16 v[184:187], a[80:83], a[12:15], v[232:235]\n"
"ds_read_b64_tr_b16 a[136:137], v241 offset:1536\n"
"s_add_u32 m0, s80, 58240\n"
"v_mfma_f32_16x16x32_bf16 v[184:187], a[84:87], a[32:35], v[184:187]\n"
"v_exp_f32 v163, v163\n"
"v_mfma_f32_16x16x32_bf16 v[184:187], a[88:91], a[52:55], v[184:187]\n"
"ds_read_b64_tr_b16 a[138:139], v241 offset:5632\n"
"v_mfma_f32_16x16x32_bf16 v[184:187], a[92:95], a[72:75], v[184:187]\n"
"v_exp_f32 v164, v164\n"
"v_mfma_f32_16x16x32_bf16 v[188:191], a[96:99], a[12:15], v[232:235]\n"
"buffer_load_dwordx4 v243, s[44:47], s68 offen offset:128 nt lds\n"
"v_mfma_f32_16x16x32_bf16 v[188:191], a[100:103], a[32:35], v[188:191]\n"
"ds_read_b64_tr_b16 a[140:141], v242 offset:1536\n"
"v_mfma_f32_16x16x32_bf16 v[188:191], a[104:107], a[52:55], v[188:191]\n"
"v_exp_f32 v165, v165\n"
"v_mfma_f32_16x16x32_bf16 v[188:191], a[108:111], a[72:75], v[188:191]\n"
"ds_read_b64_tr_b16 a[142:143], v242 offset:5632\n"
"v_mfma_f32_16x16x32_bf16 v[192:195], a[80:83], a[16:19], v[236:239]\n"
"v_exp_f32 v166, v166\n"
"v_mfma_f32_16x16x32_bf16 v[192:195], a[84:87], a[36:39], v[192:195]\n"
"v_exp_f32 v167, v167\n"
"v_mfma_f32_16x16x32_bf16 v[192:195], a[88:91], a[56:59], v[192:195]\n"
"v_exp_f32 v168, v168\n"
"v_mfma_f32_16x16x32_bf16 v[192:195], a[92:95], a[76:79], v[192:195]\n"
"v_mfma_f32_16x16x32_bf16 v[196:199], a[96:99], a[16:19], v[236:239]\n"
"v_exp_f32 v169, v169\n"
"v_mfma_f32_16x16x32_bf16 v[196:199], a[100:103], a[36:39], v[196:199]\n"
"v_mfma_f32_16x16x32_bf16 v[196:199], a[104:107], a[56:59], v[196:199]\n"
"v_exp_f32 v170, v170\n"
"v_mfma_f32_16x16x32_bf16 v[196:199], a[108:111], a[76:79], v[196:199]\n"
"s_waitcnt vmcnt(16) lgkmcnt(0)\n"
"s_barrier\n"
"v_mfma_f32_16x16x32_bf16 v[0:3], a[112:115], v[200:203], v[0:3]\n"
"ds_read_b128 a[80:83], v240 offset:0\n"
"v_mfma_f32_16x16x32_bf16 v[20:23], a[116:119], v[200:203], v[20:23]\n"
"v_exp_f32 v171, v171\n"
"v_mfma_f32_16x16x32_bf16 v[40:43], a[120:123], v[200:203], v[40:43]\n"
"v_mfma_f32_16x16x32_bf16 v[60:63], a[124:127], v[200:203], v[60:63]\n"
"v_exp_f32 v172, v172\n"
"v_mfma_f32_16x16x32_bf16 v[80:83], a[128:131], v[200:203], v[80:83]\n"
"ds_read_b128 a[84:87], v240 offset:512\n"
"v_mfma_f32_16x16x32_bf16 v[100:103], a[132:135], v[200:203], v[100:103]\n"
"v_exp_f32 v173, v173\n"
"v_mfma_f32_16x16x32_bf16 v[120:123], a[136:139], v[200:203], v[120:123]\n"
"v_exp_f32 v174, v174\n"
"v_mfma_f32_16x16x32_bf16 v[140:143], a[140:143], v[200:203], v[140:143]\n"
"v_exp_f32 v175, v175\n"
"v_mfma_f32_16x16x32_bf16 a[144:147], a[164:167], v[200:203], a[144:147]\n"
"v_mfma_f32_16x16x32_bf16 v[4:7], a[112:115], v[204:207], v[4:7]\n"
"ds_read_b128 a[88:91], v240 offset:1024\n"
"v_mfma_f32_16x16x32_bf16 v[24:27], a[116:119], v[204:207], v[24:27]\n"
"v_cvt_pk_bf16_f32 v200, v160, v161\n"
"v_cvt_pk_bf16_f32 v201, v162, v163\n"
"v_mfma_f32_16x16x32_bf16 v[44:47], a[120:123], v[204:207], v[44:47]\n"
"v_exp_f32 v176, v176\n"
"v_mfma_f32_16x16x32_bf16 v[64:67], a[124:127], v[204:207], v[64:67]\n"
"v_exp_f32 v177, v177\n"
"v_mfma_f32_16x16x32_bf16 v[84:87], a[128:131], v[204:207], v[84:87]\n"
"ds_read_b128 a[92:95], v240 offset:1536\n"
"v_cvt_pk_bf16_f32 v202, v164, v165\n"
"v_mfma_f32_16x16x32_bf16 v[104:107], a[132:135], v[204:207], v[104:107]\n"
"v_mfma_f32_16x16x32_bf16 v[124:127], a[136:139], v[204:207], v[124:127]\n"
"v_cvt_pk_bf16_f32 v203, v166, v167\n"
"v_mfma_f32_16x16x32_bf16 v[144:147], a[140:143], v[204:207], v[144:147]\n"
"v_exp_f32 v178, v178\n"
"v_mfma_f32_16x16x32_bf16 a[148:151], a[164:167], v[204:207], a[148:151]\n"
"ds_read_b128 a[96:99], v240 offset:4096\n"
"v_add_u32 v243, s85, v243\n"
"v_mfma_f32_16x16x32_bf16 v[8:11], a[112:115], v[208:211], v[8:11]\n"
"v_exp_f32 v179, v179\n"
"v_mfma_f32_16x16x32_bf16 v[28:31], a[116:119], v[208:211], v[28:31]\n"
"v_cvt_pk_bf16_f32 v204, v168, v169\n"
"v_cvt_pk_bf16_f32 v205, v170, v171\n"
"v_mfma_f32_16x16x32_bf16 v[48:51], a[120:123], v[208:211], v[48:51]\n"
"v_mfma_f32_16x16x32_bf16 v[68:71], a[124:127], v[208:211], v[68:71]\n"
"ds_read_b128 a[100:103], v240 offset:4608\n"
"v_cvt_pk_bf16_f32 v206, v172, v173\n"
"v_mfma_f32_16x16x32_bf16 v[88:91], a[128:131], v[208:211], v[88:91]\n"
"v_exp_f32 v180, v180\n"
"v_mfma_f32_16x16x32_bf16 v[108:111], a[132:135], v[208:211], v[108:111]\n"
"v_cvt_pk_bf16_f32 v207, v174, v175\n"
"v_mfma_f32_16x16x32_bf16 v[128:131], a[136:139], v[208:211], v[128:131]\n"
"v_exp_f32 v181, v181\n"
"v_mfma_f32_16x16x32_bf16 v[148:151], a[140:143], v[208:211], v[148:151]\n"
"ds_read_b128 a[104:107], v240 offset:5120\n"
"v_add_u32 v241, 131072, v246\n"
"v_mfma_f32_16x16x32_bf16 a[152:155], a[164:167], v[208:211], a[152:155]\n"
"v_exp_f32 v182, v182\n"
"v_mfma_f32_16x16x32_bf16 v[12:15], a[112:115], v[212:215], v[12:15]\n"
"v_cvt_pk_bf16_f32 v208, v176, v177\n"
"v_cvt_pk_bf16_f32 v209, v178, v179\n"
"v_mfma_f32_16x16x32_bf16 v[32:35], a[116:119], v[212:215], v[32:35]\n"
"v_exp_f32 v183, v183\n"
"v_mfma_f32_16x16x32_bf16 v[52:55], a[120:123], v[212:215], v[52:55]\n"
"ds_read_b128 a[108:111], v240 offset:5632\n"
"v_cvt_pk_bf16_f32 v210, v180, v181\n"
"v_mfma_f32_16x16x32_bf16 v[72:75], a[124:127], v[212:215], v[72:75]\n"
"v_exp_f32 v184, v184\n"
"v_mfma_f32_16x16x32_bf16 v[92:95], a[128:131], v[212:215], v[92:95]\n"
"v_cvt_pk_bf16_f32 v211, v182, v183\n"
"v_xor_b32 v242, 32, v241\n"
"v_mfma_f32_16x16x32_bf16 v[112:115], a[132:135], v[212:215], v[112:115]\n"
"v_exp_f32 v185, v185\n"
"v_mfma_f32_16x16x32_bf16 v[132:135], a[136:139], v[212:215], v[132:135]\n"
"v_exp_f32 v186, v186\n"
"v_mfma_f32_16x16x32_bf16 v[152:155], a[140:143], v[212:215], v[152:155]\n"
"v_exp_f32 v187, v187\n"
"v_mfma_f32_16x16x32_bf16 a[156:159], a[164:167], v[212:215], a[156:159]\n"
"v_mfma_f32_16x16x32_bf16 v[16:19], a[112:115], v[216:219], v[16:19]\n"
"v_exp_f32 v188, v188\n"
"v_mfma_f32_16x16x32_bf16 v[36:39], a[116:119], v[216:219], v[36:39]\n"
"v_exp_f32 v189, v189\n"
"v_mfma_f32_16x16x32_bf16 v[56:59], a[120:123], v[216:219], v[56:59]\n"
"v_exp_f32 v190, v190\n"
"v_mfma_f32_16x16x32_bf16 v[76:79], a[124:127], v[216:219], v[76:79]\n"
"v_cvt_pk_bf16_f32 v212, v184, v185\n"
"v_cvt_pk_bf16_f32 v213, v186, v187\n"
"v_mfma_f32_16x16x32_bf16 v[96:99], a[128:131], v[216:219], v[96:99]\n"
"v_exp_f32 v191, v191\n"
"v_mfma_f32_16x16x32_bf16 v[116:119], a[132:135], v[216:219], v[116:119]\n"
"v_exp_f32 v192, v192\n"
"v_mfma_f32_16x16x32_bf16 v[136:139], a[136:139], v[216:219], v[136:139]\n"
"v_mfma_f32_16x16x32_bf16 v[156:159], a[140:143], v[216:219], v[156:159]\n"
"v_exp_f32 v193, v193\n"
"v_mfma_f32_16x16x32_bf16 a[160:163], a[164:167], v[216:219], a[160:163]\n"
"s_waitcnt lgkmcnt(0)\n"
"v_mfma_f32_16x16x32_bf16 v[160:163], a[80:83], a[0:3], v[220:223]\n"
"ds_read_b64_tr_b16 a[112:113], v241 offset:0\n"
"s_add_u32 m0, s80, 65536\n"
"v_mfma_f32_16x16x32_bf16 v[160:163], a[84:87], a[20:23], v[160:163]\n"
"v_exp_f32 v194, v194\n"
"v_mfma_f32_16x16x32_bf16 v[160:163], a[88:91], a[40:43], v[160:163]\n"
"ds_read_b64_tr_b16 a[114:115], v241 offset:4096\n"
"v_cvt_pk_bf16_f32 v214, v188, v189\n"
"v_mfma_f32_16x16x32_bf16 v[160:163], a[92:95], a[60:63], v[160:163]\n"
"v_exp_f32 v195, v195\n"
"v_mfma_f32_16x16x32_bf16 v[164:167], a[96:99], a[0:3], v[220:223]\n"
"ds_read_b64_tr_b16 a[116:117], v242 offset:0\n"
"v_cvt_pk_bf16_f32 v215, v190, v191\n"
"v_mfma_f32_16x16x32_bf16 v[164:167], a[100:103], a[20:23], v[164:167]\n"
"v_exp_f32 v196, v196\n"
"v_mfma_f32_16x16x32_bf16 v[164:167], a[104:107], a[40:43], v[164:167]\n"
"ds_read_b64_tr_b16 a[118:119], v242 offset:4096\n"
"v_cvt_pk_bf16_f32 v216, v192, v193\n"
"v_mfma_f32_16x16x32_bf16 v[164:167], a[108:111], a[60:63], v[164:167]\n"
"v_exp_f32 v197, v197\n"
"v_mfma_f32_16x16x32_bf16 v[168:171], a[80:83], a[4:7], v[224:227]\n"
"ds_read_b64_tr_b16 a[120:121], v241 offset:512\n"
"v_cvt_pk_bf16_f32 v217, v194, v195\n"
"v_mfma_f32_16x16x32_bf16 v[168:171], a[84:87], a[24:27], v[168:171]\n"
"v_exp_f32 v198, v198\n"
"v_mfma_f32_16x16x32_bf16 v[168:171], a[88:91], a[44:47], v[168:171]\n"
"ds_read_b64_tr_b16 a[122:123], v241 offset:4608\n"
"v_cvt_pk_bf16_f32 v218, v196, v197\n"
"v_mfma_f32_16x16x32_bf16 v[168:171], a[92:95], a[64:67], v[168:171]\n"
"v_exp_f32 v199, v199\n"
"v_mfma_f32_16x16x32_bf16 v[172:175], a[96:99], a[4:7], v[224:227]\n"
"ds_read_b64_tr_b16 a[124:125], v242 offset:512\n"
"v_cvt_pk_bf16_f32 v219, v198, v199\n"
"v_mfma_f32_16x16x32_bf16 v[172:175], a[100:103], a[24:27], v[172:175]\n"
"buffer_load_dwordx4 v243, s[44:47], 0 offen nt lds\n"
"v_mfma_f32_16x16x32_bf16 v[172:175], a[104:107], a[44:47], v[172:175]\n"
"ds_read_b64_tr_b16 a[126:127], v242 offset:4608\n"
"s_add_u32 m0, s80, 66432\n"
"v_mfma_f32_16x16x32_bf16 v[172:175], a[108:111], a[64:67], v[172:175]\n"
"ds_read_b64_tr_b16 a[128:129], v241 offset:1024\n"
"v_add_u32 v240, 32768, v245\n"
"v_mfma_f32_16x16x32_bf16 v[176:179], a[80:83], a[8:11], v[228:231]\n"
"v_exp_f32 v160, v160\n"
"v_mfma_f32_16x16x32_bf16 v[176:179], a[84:87], a[28:31], v[176:179]\n"
"ds_read_b64_tr_b16 a[130:131], v241 offset:5120\n"
"v_mfma_f32_16x16x32_bf16 v[176:179], a[88:91], a[48:51], v[176:179]\n"
"buffer_load_dwordx4 v243, s[44:47], 0 offen offset:128 nt lds\n"
"v_mfma_f32_16x16x32_bf16 v[176:179], a[92:95], a[68:71], v[176:179]\n"
"ds_read_b64_tr_b16 a[132:133], v242 offset:1024\n"
"s_add_u32 m0, s80, 73728\n"
"v_mfma_f32_16x16x32_bf16 v[180:183], a[96:99], a[8:11], v[228:231]\n"
"v_exp_f32 v161, v161\n"
"v_mfma_f32_16x16x32_bf16 v[180:183], a[100:103], a[28:31], v[180:183]\n"
"ds_read_b64_tr_b16 a[134:135], v242 offset:5120\n"
"v_mfma_f32_16x16x32_bf16 v[180:183], a[104:107], a[48:51], v[180:183]\n"
"v_exp_f32 v162, v162\n"
"v_mfma_f32_16x16x32_bf16 v[180:183], a[108:111], a[68:71], v[180:183]\n"
"buffer_load_dwordx4 v243, s[44:47], s68 offen nt lds\n"
"v_mfma_f32_16x16x32_bf16 v[184:187], a[80:83], a[12:15], v[232:235]\n"
"ds_read_b64_tr_b16 a[136:137], v241 offset:1536\n"
"s_add_u32 m0, s80, 74624\n"
"v_mfma_f32_16x16x32_bf16 v[184:187], a[84:87], a[32:35], v[184:187]\n"
"v_exp_f32 v163, v163\n"
"v_mfma_f32_16x16x32_bf16 v[184:187], a[88:91], a[52:55], v[184:187]\n"
"ds_read_b64_tr_b16 a[138:139], v241 offset:5632\n"
"v_mfma_f32_16x16x32_bf16 v[184:187], a[92:95], a[72:75], v[184:187]\n"
"v_exp_f32 v164, v164\n"
"v_mfma_f32_16x16x32_bf16 v[188:191], a[96:99], a[12:15], v[232:235]\n"
"buffer_load_dwordx4 v243, s[44:47], s68 offen offset:128 nt lds\n"
"v_mfma_f32_16x16x32_bf16 v[188:191], a[100:103], a[32:35], v[188:191]\n"
"ds_read_b64_tr_b16 a[140:141], v242 offset:1536\n"
"v_mfma_f32_16x16x32_bf16 v[188:191], a[104:107], a[52:55], v[188:191]\n"
"v_exp_f32 v165, v165\n"
"v_mfma_f32_16x16x32_bf16 v[188:191], a[108:111], a[72:75], v[188:191]\n"
"ds_read_b64_tr_b16 a[142:143], v242 offset:5632\n"
"v_mfma_f32_16x16x32_bf16 v[192:195], a[80:83], a[16:19], v[236:239]\n"
"v_exp_f32 v166, v166\n"
"v_mfma_f32_16x16x32_bf16 v[192:195], a[84:87], a[36:39], v[192:195]\n"
"v_exp_f32 v167, v167\n"
"v_mfma_f32_16x16x32_bf16 v[192:195], a[88:91], a[56:59], v[192:195]\n"
"v_exp_f32 v168, v168\n"
"v_mfma_f32_16x16x32_bf16 v[192:195], a[92:95], a[76:79], v[192:195]\n"
"v_mfma_f32_16x16x32_bf16 v[196:199], a[96:99], a[16:19], v[236:239]\n"
"v_exp_f32 v169, v169\n"
"v_mfma_f32_16x16x32_bf16 v[196:199], a[100:103], a[36:39], v[196:199]\n"
"v_mfma_f32_16x16x32_bf16 v[196:199], a[104:107], a[56:59], v[196:199]\n"
"v_exp_f32 v170, v170\n"
"v_mfma_f32_16x16x32_bf16 v[196:199], a[108:111], a[76:79], v[196:199]\n"
"s_waitcnt vmcnt(12) lgkmcnt(0)\n"
"s_barrier\n"
"v_mfma_f32_16x16x32_bf16 v[0:3], a[112:115], v[200:203], v[0:3]\n"
"ds_read_b128 a[80:83], v240 offset:0\n"
"s_add_u32 m0, s80, 98304\n"
"v_mfma_f32_16x16x32_bf16 v[20:23], a[116:119], v[200:203], v[20:23]\n"
"v_exp_f32 v171, v171\n"
"v_mfma_f32_16x16x32_bf16 v[40:43], a[120:123], v[200:203], v[40:43]\n"
"buffer_load_dwordx4 v244, s[48:51], 0 offen nt lds\n"
"v_mfma_f32_16x16x32_bf16 v[60:63], a[124:127], v[200:203], v[60:63]\n"
"v_exp_f32 v172, v172\n"
"v_mfma_f32_16x16x32_bf16 v[80:83], a[128:131], v[200:203], v[80:83]\n"
"ds_read_b128 a[84:87], v240 offset:512\n"
"s_add_u32 m0, s80, 99200\n"
"v_mfma_f32_16x16x32_bf16 v[100:103], a[132:135], v[200:203], v[100:103]\n"
"v_exp_f32 v173, v173\n"
"v_mfma_f32_16x16x32_bf16 v[120:123], a[136:139], v[200:203], v[120:123]\n"
"v_exp_f32 v174, v174\n"
"v_mfma_f32_16x16x32_bf16 v[140:143], a[140:143], v[200:203], v[140:143]\n"
"v_exp_f32 v175, v175\n"
"v_mfma_f32_16x16x32_bf16 a[144:147], a[164:167], v[200:203], a[144:147]\n"
"buffer_load_dwordx4 v244, s[48:51], 0 offen offset:128 nt lds\n"
"v_mfma_f32_16x16x32_bf16 v[4:7], a[112:115], v[204:207], v[4:7]\n"
"ds_read_b128 a[88:91], v240 offset:1024\n"
"s_add_u32 m0, s80, 106496\n"
"v_mfma_f32_16x16x32_bf16 v[24:27], a[116:119], v[204:207], v[24:27]\n"
"v_cvt_pk_bf16_f32 v200, v160, v161\n"
"v_cvt_pk_bf16_f32 v201, v162, v163\n"
"v_mfma_f32_16x16x32_bf16 v[44:47], a[120:123], v[204:207], v[44:47]\n"
"v_exp_f32 v176, v176\n"
"v_mfma_f32_16x16x32_bf16 v[64:67], a[124:127], v[204:207], v[64:67]\n"
"v_exp_f32 v177, v177\n"
"v_mfma_f32_16x16x32_bf16 v[84:87], a[128:131], v[204:207], v[84:87]\n"
"ds_read_b128 a[92:95], v240 offset:1536\n"
"v_cvt_pk_bf16_f32 v202, v164, v165\n"
"v_mfma_f32_16x16x32_bf16 v[104:107], a[132:135], v[204:207], v[104:107]\n"
"buffer_load_dwordx4 v244, s[48:51], s68 offen nt lds\n"
"v_mfma_f32_16x16x32_bf16 v[124:127], a[136:139], v[204:207], v[124:127]\n"
"s_add_u32 m0, s80, 107392\n"
"v_cvt_pk_bf16_f32 v203, v166, v167\n"
"v_mfma_f32_16x16x32_bf16 v[144:147], a[140:143], v[204:207], v[144:147]\n"
"v_exp_f32 v178, v178\n"
"v_mfma_f32_16x16x32_bf16 a[148:151], a[164:167], v[204:207], a[148:151]\n"
"ds_read_b128 a[96:99], v240 offset:4096\n"
"v_add_u32 v243, s85, v243\n"
"v_mfma_f32_16x16x32_bf16 v[8:11], a[112:115], v[208:211], v[8:11]\n"
"v_exp_f32 v179, v179\n"
"v_mfma_f32_16x16x32_bf16 v[28:31], a[116:119], v[208:211], v[28:31]\n"
"v_cvt_pk_bf16_f32 v204, v168, v169\n"
"v_cvt_pk_bf16_f32 v205, v170, v171\n"
"v_mfma_f32_16x16x32_bf16 v[48:51], a[120:123], v[208:211], v[48:51]\n"
"buffer_load_dwordx4 v244, s[48:51], s68 offen offset:128 nt lds\n"
"v_mfma_f32_16x16x32_bf16 v[68:71], a[124:127], v[208:211], v[68:71]\n"
"ds_read_b128 a[100:103], v240 offset:4608\n"
"v_cvt_pk_bf16_f32 v206, v172, v173\n"
"v_mfma_f32_16x16x32_bf16 v[88:91], a[128:131], v[208:211], v[88:91]\n"
"v_exp_f32 v180, v180\n"
"v_mfma_f32_16x16x32_bf16 v[108:111], a[132:135], v[208:211], v[108:111]\n"
"v_cvt_pk_bf16_f32 v207, v174, v175\n"
"v_add_u32 v244, s85, v244\n"
"v_mfma_f32_16x16x32_bf16 v[128:131], a[136:139], v[208:211], v[128:131]\n"
"v_exp_f32 v181, v181\n"
"v_mfma_f32_16x16x32_bf16 v[148:151], a[140:143], v[208:211], v[148:151]\n"
"ds_read_b128 a[104:107], v240 offset:5120\n"
"v_add_u32 v241, 147456, v246\n"
"v_mfma_f32_16x16x32_bf16 a[152:155], a[164:167], v[208:211], a[152:155]\n"
"v_exp_f32 v182, v182\n"
"v_mfma_f32_16x16x32_bf16 v[12:15], a[112:115], v[212:215], v[12:15]\n"
"v_cvt_pk_bf16_f32 v208, v176, v177\n"
"v_cvt_pk_bf16_f32 v209, v178, v179\n"
"v_mfma_f32_16x16x32_bf16 v[32:35], a[116:119], v[212:215], v[32:35]\n"
"v_exp_f32 v183, v183\n"
"v_mfma_f32_16x16x32_bf16 v[52:55], a[120:123], v[212:215], v[52:55]\n"
"ds_read_b128 a[108:111], v240 offset:5632\n"
"v_cvt_pk_bf16_f32 v210, v180, v181\n"
"v_mfma_f32_16x16x32_bf16 v[72:75], a[124:127], v[212:215], v[72:75]\n"
"v_exp_f32 v184, v184\n"
"v_mfma_f32_16x16x32_bf16 v[92:95], a[128:131], v[212:215], v[92:95]\n"
"v_cvt_pk_bf16_f32 v211, v182, v183\n"
"v_xor_b32 v242, 32, v241\n"
"v_mfma_f32_16x16x32_bf16 v[112:115], a[132:135], v[212:215], v[112:115]\n"
"v_exp_f32 v185, v185\n"
"v_mfma_f32_16x16x32_bf16 v[132:135], a[136:139], v[212:215], v[132:135]\n"
"v_exp_f32 v186, v186\n"
"v_mfma_f32_16x16x32_bf16 v[152:155], a[140:143], v[212:215], v[152:155]\n"
"v_exp_f32 v187, v187\n"
"v_mfma_f32_16x16x32_bf16 a[156:159], a[164:167], v[212:215], a[156:159]\n"
"v_mfma_f32_16x16x32_bf16 v[16:19], a[112:115], v[216:219], v[16:19]\n"
"v_exp_f32 v188, v188\n"
"v_mfma_f32_16x16x32_bf16 v[36:39], a[116:119], v[216:219], v[36:39]\n"
"v_exp_f32 v189, v189\n"
"v_mfma_f32_16x16x32_bf16 v[56:59], a[120:123], v[216:219], v[56:59]\n"
"v_exp_f32 v190, v190\n"
"v_mfma_f32_16x16x32_bf16 v[76:79], a[124:127], v[216:219], v[76:79]\n"
"v_cvt_pk_bf16_f32 v212, v184, v185\n"
"v_cvt_pk_bf16_f32 v213, v186, v187\n"
"v_mfma_f32_16x16x32_bf16 v[96:99], a[128:131], v[216:219], v[96:99]\n"
"v_exp_f32 v191, v191\n"
"v_mfma_f32_16x16x32_bf16 v[116:119], a[132:135], v[216:219], v[116:119]\n"
"v_exp_f32 v192, v192\n"
"v_mfma_f32_16x16x32_bf16 v[136:139], a[136:139], v[216:219], v[136:139]\n"
"v_mfma_f32_16x16x32_bf16 v[156:159], a[140:143], v[216:219], v[156:159]\n"
"v_exp_f32 v193, v193\n"
"v_mfma_f32_16x16x32_bf16 a[160:163], a[164:167], v[216:219], a[160:163]\n"
"s_waitcnt lgkmcnt(0)\n"
"v_mfma_f32_16x16x32_bf16 v[160:163], a[80:83], a[0:3], v[220:223]\n"
"ds_read_b64_tr_b16 a[112:113], v241 offset:0\n"
"s_add_u32 m0, s80, 0\n"
"v_mfma_f32_16x16x32_bf16 v[160:163], a[84:87], a[20:23], v[160:163]\n"
"v_exp_f32 v194, v194\n"
"v_mfma_f32_16x16x32_bf16 v[160:163], a[88:91], a[40:43], v[160:163]\n"
"ds_read_b64_tr_b16 a[114:115], v241 offset:4096\n"
"v_cvt_pk_bf16_f32 v214, v188, v189\n"
"v_mfma_f32_16x16x32_bf16 v[160:163], a[92:95], a[60:63], v[160:163]\n"
"v_exp_f32 v195, v195\n"
"v_mfma_f32_16x16x32_bf16 v[164:167], a[96:99], a[0:3], v[220:223]\n"
"ds_read_b64_tr_b16 a[116:117], v242 offset:0\n"
"v_cvt_pk_bf16_f32 v215, v190, v191\n"
"v_mfma_f32_16x16x32_bf16 v[164:167], a[100:103], a[20:23], v[164:167]\n"
"v_exp_f32 v196, v196\n"
"v_mfma_f32_16x16x32_bf16 v[164:167], a[104:107], a[40:43], v[164:167]\n"
"ds_read_b64_tr_b16 a[118:119], v242 offset:4096\n"
"v_cvt_pk_bf16_f32 v216, v192, v193\n"
"v_mfma_f32_16x16x32_bf16 v[164:167], a[108:111], a[60:63], v[164:167]\n"
"v_exp_f32 v197, v197\n"
"v_mfma_f32_16x16x32_bf16 v[168:171], a[80:83], a[4:7], v[224:227]\n"
"ds_read_b64_tr_b16 a[120:121], v241 offset:512\n"
"v_cvt_pk_bf16_f32 v217, v194, v195\n"
"v_mfma_f32_16x16x32_bf16 v[168:171], a[84:87], a[24:27], v[168:171]\n"
"v_exp_f32 v198, v198\n"
"v_mfma_f32_16x16x32_bf16 v[168:171], a[88:91], a[44:47], v[168:171]\n"
"ds_read_b64_tr_b16 a[122:123], v241 offset:4608\n"
"v_cvt_pk_bf16_f32 v218, v196, v197\n"
"v_mfma_f32_16x16x32_bf16 v[168:171], a[92:95], a[64:67], v[168:171]\n"
"v_exp_f32 v199, v199\n"
"v_mfma_f32_16x16x32_bf16 v[172:175], a[96:99], a[4:7], v[224:227]\n"
"ds_read_b64_tr_b16 a[124:125], v242 offset:512\n"
"v_cvt_pk_bf16_f32 v219, v198, v199\n"
"v_mfma_f32_16x16x32_bf16 v[172:175], a[100:103], a[24:27], v[172:175]\n"
"buffer_load_dwordx4 v243, s[44:47], 0 offen nt lds\n"
"v_mfma_f32_16x16x32_bf16 v[172:175], a[104:107], a[44:47], v[172:175]\n"
"ds_read_b64_tr_b16 a[126:127], v242 offset:4608\n"
"s_add_u32 m0, s80, 896\n"
"v_mfma_f32_16x16x32_bf16 v[172:175], a[108:111], a[64:67], v[172:175]\n"
"ds_read_b64_tr_b16 a[128:129], v241 offset:1024\n"
"v_add_u32 v240, 49152, v245\n"
"v_mfma_f32_16x16x32_bf16 v[176:179], a[80:83], a[8:11], v[228:231]\n"
"v_exp_f32 v160, v160\n"
"v_mfma_f32_16x16x32_bf16 v[176:179], a[84:87], a[28:31], v[176:179]\n"
"ds_read_b64_tr_b16 a[130:131], v241 offset:5120\n"
"v_mfma_f32_16x16x32_bf16 v[176:179], a[88:91], a[48:51], v[176:179]\n"
"buffer_load_dwordx4 v243, s[44:47], 0 offen offset:128 nt lds\n"
"v_mfma_f32_16x16x32_bf16 v[176:179], a[92:95], a[68:71], v[176:179]\n"
"ds_read_b64_tr_b16 a[132:133], v242 offset:1024\n"
"s_add_u32 m0, s80, 8192\n"
"v_mfma_f32_16x16x32_bf16 v[180:183], a[96:99], a[8:11], v[228:231]\n"
"v_exp_f32 v161, v161\n"
"v_mfma_f32_16x16x32_bf16 v[180:183], a[100:103], a[28:31], v[180:183]\n"
"ds_read_b64_tr_b16 a[134:135], v242 offset:5120\n"
"v_mfma_f32_16x16x32_bf16 v[180:183], a[104:107], a[48:51], v[180:183]\n"
"v_exp_f32 v162, v162\n"
"v_mfma_f32_16x16x32_bf16 v[180:183], a[108:111], a[68:71], v[180:183]\n"
"buffer_load_dwordx4 v243, s[44:47], s68 offen nt lds\n"
"v_mfma_f32_16x16x32_bf16 v[184:187], a[80:83], a[12:15], v[232:235]\n"
"ds_read_b64_tr_b16 a[136:137], v241 offset:1536\n"
"s_add_u32 m0, s80, 9088\n"
"v_mfma_f32_16x16x32_bf16 v[184:187], a[84:87], a[32:35], v[184:187]\n"
"v_exp_f32 v163, v163\n"
"v_mfma_f32_16x16x32_bf16 v[184:187], a[88:91], a[52:55], v[184:187]\n"
"ds_read_b64_tr_b16 a[138:139], v241 offset:5632\n"
"v_mfma_f32_16x16x32_bf16 v[184:187], a[92:95], a[72:75], v[184:187]\n"
"v_exp_f32 v164, v164\n"
"v_mfma_f32_16x16x32_bf16 v[188:191], a[96:99], a[12:15], v[232:235]\n"
"buffer_load_dwordx4 v243, s[44:47], s68 offen offset:128 nt lds\n"
"v_mfma_f32_16x16x32_bf16 v[188:191], a[100:103], a[32:35], v[188:191]\n"
"ds_read_b64_tr_b16 a[140:141], v242 offset:1536\n"
"v_mfma_f32_16x16x32_bf16 v[188:191], a[104:107], a[52:55], v[188:191]\n"
"v_exp_f32 v165, v165\n"
"v_mfma_f32_16x16x32_bf16 v[188:191], a[108:111], a[72:75], v[188:191]\n"
"ds_read_b64_tr_b16 a[142:143], v242 offset:5632\n"
"v_mfma_f32_16x16x32_bf16 v[192:195], a[80:83], a[16:19], v[236:239]\n"
"v_exp_f32 v166, v166\n"
"v_mfma_f32_16x16x32_bf16 v[192:195], a[84:87], a[36:39], v[192:195]\n"
"v_exp_f32 v167, v167\n"
"v_mfma_f32_16x16x32_bf16 v[192:195], a[88:91], a[56:59], v[192:195]\n"
"v_exp_f32 v168, v168\n"
"v_mfma_f32_16x16x32_bf16 v[192:195], a[92:95], a[76:79], v[192:195]\n"
"v_mfma_f32_16x16x32_bf16 v[196:199], a[96:99], a[16:19], v[236:239]\n"
"v_exp_f32 v169, v169\n"
"v_mfma_f32_16x16x32_bf16 v[196:199], a[100:103], a[36:39], v[196:199]\n"
"v_mfma_f32_16x16x32_bf16 v[196:199], a[104:107], a[56:59], v[196:199]\n"
"v_exp_f32 v170, v170\n"
"v_mfma_f32_16x16x32_bf16 v[196:199], a[108:111], a[76:79], v[196:199]\n"
"s_waitcnt vmcnt(12) lgkmcnt(0)\n"
"s_barrier\n"
"v_mfma_f32_16x16x32_bf16 v[0:3], a[112:115], v[200:203], v[0:3]\n"
"ds_read_b128 a[80:83], v240 offset:0\n"
"s_add_u32 m0, s80, 114688\n"
"v_mfma_f32_16x16x32_bf16 v[20:23], a[116:119], v[200:203], v[20:23]\n"
"v_exp_f32 v171, v171\n"
"v_mfma_f32_16x16x32_bf16 v[40:43], a[120:123], v[200:203], v[40:43]\n"
"buffer_load_dwordx4 v244, s[48:51], 0 offen nt lds\n"
"v_mfma_f32_16x16x32_bf16 v[60:63], a[124:127], v[200:203], v[60:63]\n"
"v_exp_f32 v172, v172\n"
"v_mfma_f32_16x16x32_bf16 v[80:83], a[128:131], v[200:203], v[80:83]\n"
"ds_read_b128 a[84:87], v240 offset:512\n"
"s_add_u32 m0, s80, 115584\n"
"v_mfma_f32_16x16x32_bf16 v[100:103], a[132:135], v[200:203], v[100:103]\n"
"v_exp_f32 v173, v173\n"
"v_mfma_f32_16x16x32_bf16 v[120:123], a[136:139], v[200:203], v[120:123]\n"
"v_exp_f32 v174, v174\n"
"v_mfma_f32_16x16x32_bf16 v[140:143], a[140:143], v[200:203], v[140:143]\n"
"v_exp_f32 v175, v175\n"
"v_mfma_f32_16x16x32_bf16 a[144:147], a[164:167], v[200:203], a[144:147]\n"
"buffer_load_dwordx4 v244, s[48:51], 0 offen offset:128 nt lds\n"
"v_mfma_f32_16x16x32_bf16 v[4:7], a[112:115], v[204:207], v[4:7]\n"
"ds_read_b128 a[88:91], v240 offset:1024\n"
"s_add_u32 m0, s80, 122880\n"
"v_mfma_f32_16x16x32_bf16 v[24:27], a[116:119], v[204:207], v[24:27]\n"
"v_cvt_pk_bf16_f32 v200, v160, v161\n"
"v_cvt_pk_bf16_f32 v201, v162, v163\n"
"v_mfma_f32_16x16x32_bf16 v[44:47], a[120:123], v[204:207], v[44:47]\n"
"v_exp_f32 v176, v176\n"
"v_mfma_f32_16x16x32_bf16 v[64:67], a[124:127], v[204:207], v[64:67]\n"
"v_exp_f32 v177, v177\n"
"v_mfma_f32_16x16x32_bf16 v[84:87], a[128:131], v[204:207], v[84:87]\n"
"ds_read_b128 a[92:95], v240 offset:1536\n"
"v_cvt_pk_bf16_f32 v202, v164, v165\n"
"v_mfma_f32_16x16x32_bf16 v[104:107], a[132:135], v[204:207], v[104:107]\n"
"buffer_load_dwordx4 v244, s[48:51], s68 offen nt lds\n"
"v_mfma_f32_16x16x32_bf16 v[124:127], a[136:139], v[204:207], v[124:127]\n"
"s_add_u32 m0, s80, 123776\n"
"v_cvt_pk_bf16_f32 v203, v166, v167\n"
"v_mfma_f32_16x16x32_bf16 v[144:147], a[140:143], v[204:207], v[144:147]\n"
"v_exp_f32 v178, v178\n"
"v_mfma_f32_16x16x32_bf16 a[148:151], a[164:167], v[204:207], a[148:151]\n"
"ds_read_b128 a[96:99], v240 offset:4096\n"
"v_add_u32 v243, s85, v243\n"
"v_mfma_f32_16x16x32_bf16 v[8:11], a[112:115], v[208:211], v[8:11]\n"
"v_exp_f32 v179, v179\n"
"v_mfma_f32_16x16x32_bf16 v[28:31], a[116:119], v[208:211], v[28:31]\n"
"v_cvt_pk_bf16_f32 v204, v168, v169\n"
"v_cvt_pk_bf16_f32 v205, v170, v171\n"
"v_mfma_f32_16x16x32_bf16 v[48:51], a[120:123], v[208:211], v[48:51]\n"
"buffer_load_dwordx4 v244, s[48:51], s68 offen offset:128 nt lds\n"
"v_mfma_f32_16x16x32_bf16 v[68:71], a[124:127], v[208:211], v[68:71]\n"
"ds_read_b128 a[100:103], v240 offset:4608\n"
"v_cvt_pk_bf16_f32 v206, v172, v173\n"
"v_mfma_f32_16x16x32_bf16 v[88:91], a[128:131], v[208:211], v[88:91]\n"
"v_exp_f32 v180, v180\n"
"v_mfma_f32_16x16x32_bf16 v[108:111], a[132:135], v[208:211], v[108:111]\n"
"v_cvt_pk_bf16_f32 v207, v174, v175\n"
"v_add_u32 v244, s85, v244\n"
"v_mfma_f32_16x16x32_bf16 v[128:131], a[136:139], v[208:211], v[128:131]\n"
"v_exp_f32 v181, v181\n"
"v_mfma_f32_16x16x32_bf16 v[148:151], a[140:143], v[208:211], v[148:151]\n"
"ds_read_b128 a[104:107], v240 offset:5120\n"
"v_add_u32 v241, 81920, v246\n"
"v_mfma_f32_16x16x32_bf16 a[152:155], a[164:167], v[208:211], a[152:155]\n"
"v_exp_f32 v182, v182\n"
"v_mfma_f32_16x16x32_bf16 v[12:15], a[112:115], v[212:215], v[12:15]\n"
"v_cvt_pk_bf16_f32 v208, v176, v177\n"
"v_cvt_pk_bf16_f32 v209, v178, v179\n"
"v_mfma_f32_16x16x32_bf16 v[32:35], a[116:119], v[212:215], v[32:35]\n"
"v_exp_f32 v183, v183\n"
"v_mfma_f32_16x16x32_bf16 v[52:55], a[120:123], v[212:215], v[52:55]\n"
"ds_read_b128 a[108:111], v240 offset:5632\n"
"v_cvt_pk_bf16_f32 v210, v180, v181\n"
"v_mfma_f32_16x16x32_bf16 v[72:75], a[124:127], v[212:215], v[72:75]\n"
"v_exp_f32 v184, v184\n"
"v_mfma_f32_16x16x32_bf16 v[92:95], a[128:131], v[212:215], v[92:95]\n"
"v_cvt_pk_bf16_f32 v211, v182, v183\n"
"v_xor_b32 v242, 32, v241\n"
"v_mfma_f32_16x16x32_bf16 v[112:115], a[132:135], v[212:215], v[112:115]\n"
"v_exp_f32 v185, v185\n"
"v_mfma_f32_16x16x32_bf16 v[132:135], a[136:139], v[212:215], v[132:135]\n"
"v_exp_f32 v186, v186\n"
"v_mfma_f32_16x16x32_bf16 v[152:155], a[140:143], v[212:215], v[152:155]\n"
"v_exp_f32 v187, v187\n"
"v_mfma_f32_16x16x32_bf16 a[156:159], a[164:167], v[212:215], a[156:159]\n"
"v_mfma_f32_16x16x32_bf16 v[16:19], a[112:115], v[216:219], v[16:19]\n"
"v_exp_f32 v188, v188\n"
"v_mfma_f32_16x16x32_bf16 v[36:39], a[116:119], v[216:219], v[36:39]\n"
"v_exp_f32 v189, v189\n"
"v_mfma_f32_16x16x32_bf16 v[56:59], a[120:123], v[216:219], v[56:59]\n"
"v_exp_f32 v190, v190\n"
"v_mfma_f32_16x16x32_bf16 v[76:79], a[124:127], v[216:219], v[76:79]\n"
"v_cvt_pk_bf16_f32 v212, v184, v185\n"
"v_cvt_pk_bf16_f32 v213, v186, v187\n"
"v_mfma_f32_16x16x32_bf16 v[96:99], a[128:131], v[216:219], v[96:99]\n"
"v_exp_f32 v191, v191\n"
"v_mfma_f32_16x16x32_bf16 v[116:119], a[132:135], v[216:219], v[116:119]\n"
"v_exp_f32 v192, v192\n"
"v_mfma_f32_16x16x32_bf16 v[136:139], a[136:139], v[216:219], v[136:139]\n"
"v_mfma_f32_16x16x32_bf16 v[156:159], a[140:143], v[216:219], v[156:159]\n"
"v_exp_f32 v193, v193\n"
"v_mfma_f32_16x16x32_bf16 a[160:163], a[164:167], v[216:219], a[160:163]\n"
"s_waitcnt lgkmcnt(0)\n"
"v_mfma_f32_16x16x32_bf16 v[160:163], a[80:83], a[0:3], v[220:223]\n"
"ds_read_b64_tr_b16 a[112:113], v241 offset:0\n"
"v_mfma_f32_16x16x32_bf16 v[160:163], a[84:87], a[20:23], v[160:163]\n"
"v_exp_f32 v194, v194\n"
"v_mfma_f32_16x16x32_bf16 v[160:163], a[88:91], a[40:43], v[160:163]\n"
"ds_read_b64_tr_b16 a[114:115], v241 offset:4096\n"
"v_cvt_pk_bf16_f32 v214, v188, v189\n"
"v_mfma_f32_16x16x32_bf16 v[160:163], a[92:95], a[60:63], v[160:163]\n"
"v_exp_f32 v195, v195\n"
"v_mfma_f32_16x16x32_bf16 v[164:167], a[96:99], a[0:3], v[220:223]\n"
"ds_read_b64_tr_b16 a[116:117], v242 offset:0\n"
"v_cvt_pk_bf16_f32 v215, v190, v191\n"
"v_mfma_f32_16x16x32_bf16 v[164:167], a[100:103], a[20:23], v[164:167]\n"
"v_exp_f32 v196, v196\n"
"v_mfma_f32_16x16x32_bf16 v[164:167], a[104:107], a[40:43], v[164:167]\n"
"ds_read_b64_tr_b16 a[118:119], v242 offset:4096\n"
"v_cvt_pk_bf16_f32 v216, v192, v193\n"
"v_mfma_f32_16x16x32_bf16 v[164:167], a[108:111], a[60:63], v[164:167]\n"
"v_exp_f32 v197, v197\n"
"v_mfma_f32_16x16x32_bf16 v[168:171], a[80:83], a[4:7], v[224:227]\n"
"ds_read_b64_tr_b16 a[120:121], v241 offset:512\n"
"v_cvt_pk_bf16_f32 v217, v194, v195\n"
"v_mfma_f32_16x16x32_bf16 v[168:171], a[84:87], a[24:27], v[168:171]\n"
"v_exp_f32 v198, v198\n"
"v_mfma_f32_16x16x32_bf16 v[168:171], a[88:91], a[44:47], v[168:171]\n"
"ds_read_b64_tr_b16 a[122:123], v241 offset:4608\n"
"v_cvt_pk_bf16_f32 v218, v196, v197\n"
"v_mfma_f32_16x16x32_bf16 v[168:171], a[92:95], a[64:67], v[168:171]\n"
"v_exp_f32 v199, v199\n"
"v_mfma_f32_16x16x32_bf16 v[172:175], a[96:99], a[4:7], v[224:227]\n"
"ds_read_b64_tr_b16 a[124:125], v242 offset:512\n"
"v_cvt_pk_bf16_f32 v219, v198, v199\n"
"v_mfma_f32_16x16x32_bf16 v[172:175], a[100:103], a[24:27], v[172:175]\n"
"v_mfma_f32_16x16x32_bf16 v[172:175], a[104:107], a[44:47], v[172:175]\n"
"ds_read_b64_tr_b16 a[126:127], v242 offset:4608\n"
"v_mfma_f32_16x16x32_bf16 v[172:175], a[108:111], a[64:67], v[172:175]\n"
"ds_read_b64_tr_b16 a[128:129], v241 offset:1024\n"
"v_add_u32 v240, 65536, v245\n"
"v_mfma_f32_16x16x32_bf16 v[176:179], a[80:83], a[8:11], v[228:231]\n"
"v_exp_f32 v160, v160\n"
"v_mfma_f32_16x16x32_bf16 v[176:179], a[84:87], a[28:31], v[176:179]\n"
"ds_read_b64_tr_b16 a[130:131], v241 offset:5120\n"
"v_mfma_f32_16x16x32_bf16 v[176:179], a[88:91], a[48:51], v[176:179]\n"
"v_mfma_f32_16x16x32_bf16 v[176:179], a[92:95], a[68:71], v[176:179]\n"
"ds_read_b64_tr_b16 a[132:133], v242 offset:1024\n"
"v_mfma_f32_16x16x32_bf16 v[180:183], a[96:99], a[8:11], v[228:231]\n"
"v_exp_f32 v161, v161\n"
"v_mfma_f32_16x16x32_bf16 v[180:183], a[100:103], a[28:31], v[180:183]\n"
"ds_read_b64_tr_b16 a[134:135], v242 offset:5120\n"
"v_mfma_f32_16x16x32_bf16 v[180:183], a[104:107], a[48:51], v[180:183]\n"
"v_exp_f32 v162, v162\n"
"v_mfma_f32_16x16x32_bf16 v[180:183], a[108:111], a[68:71], v[180:183]\n"
"v_mfma_f32_16x16x32_bf16 v[184:187], a[80:83], a[12:15], v[232:235]\n"
"ds_read_b64_tr_b16 a[136:137], v241 offset:1536\n"
"v_mfma_f32_16x16x32_bf16 v[184:187], a[84:87], a[32:35], v[184:187]\n"
"v_exp_f32 v163, v163\n"
"v_mfma_f32_16x16x32_bf16 v[184:187], a[88:91], a[52:55], v[184:187]\n"
"ds_read_b64_tr_b16 a[138:139], v241 offset:5632\n"
"v_mfma_f32_16x16x32_bf16 v[184:187], a[92:95], a[72:75], v[184:187]\n"
"v_exp_f32 v164, v164\n"
"v_mfma_f32_16x16x32_bf16 v[188:191], a[96:99], a[12:15], v[232:235]\n"
"v_mfma_f32_16x16x32_bf16 v[188:191], a[100:103], a[32:35], v[188:191]\n"
"ds_read_b64_tr_b16 a[140:141], v242 offset:1536\n"
"v_mfma_f32_16x16x32_bf16 v[188:191], a[104:107], a[52:55], v[188:191]\n"
"v_exp_f32 v165, v165\n"
"v_mfma_f32_16x16x32_bf16 v[188:191], a[108:111], a[72:75], v[188:191]\n"
"ds_read_b64_tr_b16 a[142:143], v242 offset:5632\n"
"v_mfma_f32_16x16x32_bf16 v[192:195], a[80:83], a[16:19], v[236:239]\n"
"v_exp_f32 v166, v166\n"
"v_mfma_f32_16x16x32_bf16 v[192:195], a[84:87], a[36:39], v[192:195]\n"
"v_exp_f32 v167, v167\n"
"v_mfma_f32_16x16x32_bf16 v[192:195], a[88:91], a[56:59], v[192:195]\n"
"v_exp_f32 v168, v168\n"
"v_mfma_f32_16x16x32_bf16 v[192:195], a[92:95], a[76:79], v[192:195]\n"
"v_mfma_f32_16x16x32_bf16 v[196:199], a[96:99], a[16:19], v[236:239]\n"
"v_exp_f32 v169, v169\n"
"v_mfma_f32_16x16x32_bf16 v[196:199], a[100:103], a[36:39], v[196:199]\n"
"v_mfma_f32_16x16x32_bf16 v[196:199], a[104:107], a[56:59], v[196:199]\n"
"v_exp_f32 v170, v170\n"
"v_mfma_f32_16x16x32_bf16 v[196:199], a[108:111], a[76:79], v[196:199]\n"
"s_waitcnt vmcnt(8) lgkmcnt(0)\n"
"s_barrier\n"
"v_mfma_f32_16x16x32_bf16 v[0:3], a[112:115], v[200:203], v[0:3]\n"
"ds_read_b128 a[80:83], v240 offset:0\n"
"s_add_u32 m0, s80, 131072\n"
"v_mfma_f32_16x16x32_bf16 v[20:23], a[116:119], v[200:203], v[20:23]\n"
"v_exp_f32 v171, v171\n"
"v_mfma_f32_16x16x32_bf16 v[40:43], a[120:123], v[200:203], v[40:43]\n"
"buffer_load_dwordx4 v244, s[48:51], 0 offen nt lds\n"
"v_mfma_f32_16x16x32_bf16 v[60:63], a[124:127], v[200:203], v[60:63]\n"
"v_exp_f32 v172, v172\n"
"v_mfma_f32_16x16x32_bf16 v[80:83], a[128:131], v[200:203], v[80:83]\n"
"ds_read_b128 a[84:87], v240 offset:512\n"
"s_add_u32 m0, s80, 131968\n"
"v_mfma_f32_16x16x32_bf16 v[100:103], a[132:135], v[200:203], v[100:103]\n"
"v_exp_f32 v173, v173\n"
"v_mfma_f32_16x16x32_bf16 v[120:123], a[136:139], v[200:203], v[120:123]\n"
"v_exp_f32 v174, v174\n"
"v_mfma_f32_16x16x32_bf16 v[140:143], a[140:143], v[200:203], v[140:143]\n"
"v_exp_f32 v175, v175\n"
"v_mfma_f32_16x16x32_bf16 a[144:147], a[164:167], v[200:203], a[144:147]\n"
"buffer_load_dwordx4 v244, s[48:51], 0 offen offset:128 nt lds\n"
"v_mfma_f32_16x16x32_bf16 v[4:7], a[112:115], v[204:207], v[4:7]\n"
"ds_read_b128 a[88:91], v240 offset:1024\n"
"s_add_u32 m0, s80, 139264\n"
"v_mfma_f32_16x16x32_bf16 v[24:27], a[116:119], v[204:207], v[24:27]\n"
"v_cvt_pk_bf16_f32 v200, v160, v161\n"
"v_cvt_pk_bf16_f32 v201, v162, v163\n"
"v_mfma_f32_16x16x32_bf16 v[44:47], a[120:123], v[204:207], v[44:47]\n"
"v_exp_f32 v176, v176\n"
"v_mfma_f32_16x16x32_bf16 v[64:67], a[124:127], v[204:207], v[64:67]\n"
"v_exp_f32 v177, v177\n"
"v_mfma_f32_16x16x32_bf16 v[84:87], a[128:131], v[204:207], v[84:87]\n"
"ds_read_b128 a[92:95], v240 offset:1536\n"
"v_cvt_pk_bf16_f32 v202, v164, v165\n"
"v_mfma_f32_16x16x32_bf16 v[104:107], a[132:135], v[204:207], v[104:107]\n"
"buffer_load_dwordx4 v244, s[48:51], s68 offen nt lds\n"
"v_mfma_f32_16x16x32_bf16 v[124:127], a[136:139], v[204:207], v[124:127]\n"
"s_add_u32 m0, s80, 140160\n"
"v_cvt_pk_bf16_f32 v203, v166, v167\n"
"v_mfma_f32_16x16x32_bf16 v[144:147], a[140:143], v[204:207], v[144:147]\n"
"v_exp_f32 v178, v178\n"
"v_mfma_f32_16x16x32_bf16 a[148:151], a[164:167], v[204:207], a[148:151]\n"
"ds_read_b128 a[96:99], v240 offset:4096\n"
"v_mfma_f32_16x16x32_bf16 v[8:11], a[112:115], v[208:211], v[8:11]\n"
"v_exp_f32 v179, v179\n"
"v_mfma_f32_16x16x32_bf16 v[28:31], a[116:119], v[208:211], v[28:31]\n"
"v_cvt_pk_bf16_f32 v204, v168, v169\n"
"v_cvt_pk_bf16_f32 v205, v170, v171\n"
"v_mfma_f32_16x16x32_bf16 v[48:51], a[120:123], v[208:211], v[48:51]\n"
"buffer_load_dwordx4 v244, s[48:51], s68 offen offset:128 nt lds\n"
"v_mfma_f32_16x16x32_bf16 v[68:71], a[124:127], v[208:211], v[68:71]\n"
"ds_read_b128 a[100:103], v240 offset:4608\n"
"v_cvt_pk_bf16_f32 v206, v172, v173\n"
"v_mfma_f32_16x16x32_bf16 v[88:91], a[128:131], v[208:211], v[88:91]\n"
"v_exp_f32 v180, v180\n"
"v_mfma_f32_16x16x32_bf16 v[108:111], a[132:135], v[208:211], v[108:111]\n"
"v_cvt_pk_bf16_f32 v207, v174, v175\n"
"v_add_u32 v244, s85, v244\n"
"v_mfma_f32_16x16x32_bf16 v[128:131], a[136:139], v[208:211], v[128:131]\n"
"v_exp_f32 v181, v181\n"
"v_mfma_f32_16x16x32_bf16 v[148:151], a[140:143], v[208:211], v[148:151]\n"
"ds_read_b128 a[104:107], v240 offset:5120\n"
"v_add_u32 v241, 98304, v246\n"
"v_mfma_f32_16x16x32_bf16 a[152:155], a[164:167], v[208:211], a[152:155]\n"
"v_exp_f32 v182, v182\n"
"v_mfma_f32_16x16x32_bf16 v[12:15], a[112:115], v[212:215], v[12:15]\n"
"v_cvt_pk_bf16_f32 v208, v176, v177\n"
"v_cvt_pk_bf16_f32 v209, v178, v179\n"
"v_mfma_f32_16x16x32_bf16 v[32:35], a[116:119], v[212:215], v[32:35]\n"
"v_exp_f32 v183, v183\n"
"v_mfma_f32_16x16x32_bf16 v[52:55], a[120:123], v[212:215], v[52:55]\n"
"ds_read_b128 a[108:111], v240 offset:5632\n"
"v_cvt_pk_bf16_f32 v210, v180, v181\n"
"v_mfma_f32_16x16x32_bf16 v[72:75], a[124:127], v[212:215], v[72:75]\n"
"v_exp_f32 v184, v184\n"
"v_mfma_f32_16x16x32_bf16 v[92:95], a[128:131], v[212:215], v[92:95]\n"
"v_cvt_pk_bf16_f32 v211, v182, v183\n"
"v_xor_b32 v242, 32, v241\n"
"v_mfma_f32_16x16x32_bf16 v[112:115], a[132:135], v[212:215], v[112:115]\n"
"v_exp_f32 v185, v185\n"
"v_mfma_f32_16x16x32_bf16 v[132:135], a[136:139], v[212:215], v[132:135]\n"
"v_exp_f32 v186, v186\n"
"v_mfma_f32_16x16x32_bf16 v[152:155], a[140:143], v[212:215], v[152:155]\n"
"v_exp_f32 v187, v187\n"
"v_mfma_f32_16x16x32_bf16 a[156:159], a[164:167], v[212:215], a[156:159]\n"
"v_mfma_f32_16x16x32_bf16 v[16:19], a[112:115], v[216:219], v[16:19]\n"
"v_exp_f32 v188, v188\n"
"v_mfma_f32_16x16x32_bf16 v[36:39], a[116:119], v[216:219], v[36:39]\n"
"v_exp_f32 v189, v189\n"
"v_mfma_f32_16x16x32_bf16 v[56:59], a[120:123], v[216:219], v[56:59]\n"
"v_exp_f32 v190, v190\n"
"v_mfma_f32_16x16x32_bf16 v[76:79], a[124:127], v[216:219], v[76:79]\n"
"v_cvt_pk_bf16_f32 v212, v184, v185\n"
"v_cvt_pk_bf16_f32 v213, v186, v187\n"
"v_mfma_f32_16x16x32_bf16 v[96:99], a[128:131], v[216:219], v[96:99]\n"
"v_exp_f32 v191, v191\n"
"v_mfma_f32_16x16x32_bf16 v[116:119], a[132:135], v[216:219], v[116:119]\n"
"v_exp_f32 v192, v192\n"
"v_mfma_f32_16x16x32_bf16 v[136:139], a[136:139], v[216:219], v[136:139]\n"
"v_mfma_f32_16x16x32_bf16 v[156:159], a[140:143], v[216:219], v[156:159]\n"
"v_exp_f32 v193, v193\n"
"v_mfma_f32_16x16x32_bf16 a[160:163], a[164:167], v[216:219], a[160:163]\n"
"s_waitcnt lgkmcnt(0)\n"
"v_mfma_f32_16x16x32_bf16 v[160:163], a[80:83], a[0:3], v[220:223]\n"
"ds_read_b64_tr_b16 a[112:113], v241 offset:0\n"
"v_mfma_f32_16x16x32_bf16 v[160:163], a[84:87], a[20:23], v[160:163]\n"
"v_exp_f32 v194, v194\n"
"v_mfma_f32_16x16x32_bf16 v[160:163], a[88:91], a[40:43], v[160:163]\n"
"ds_read_b64_tr_b16 a[114:115], v241 offset:4096\n"
"v_cvt_pk_bf16_f32 v214, v188, v189\n"
"v_mfma_f32_16x16x32_bf16 v[160:163], a[92:95], a[60:63], v[160:163]\n"
"v_exp_f32 v195, v195\n"
"v_mfma_f32_16x16x32_bf16 v[164:167], a[96:99], a[0:3], v[220:223]\n"
"ds_read_b64_tr_b16 a[116:117], v242 offset:0\n"
"v_cvt_pk_bf16_f32 v215, v190, v191\n"
"v_mfma_f32_16x16x32_bf16 v[164:167], a[100:103], a[20:23], v[164:167]\n"
"v_exp_f32 v196, v196\n"
"v_mfma_f32_16x16x32_bf16 v[164:167], a[104:107], a[40:43], v[164:167]\n"
"ds_read_b64_tr_b16 a[118:119], v242 offset:4096\n"
"v_cvt_pk_bf16_f32 v216, v192, v193\n"
"v_mfma_f32_16x16x32_bf16 v[164:167], a[108:111], a[60:63], v[164:167]\n"
"v_exp_f32 v197, v197\n"
"v_mfma_f32_16x16x32_bf16 v[168:171], a[80:83], a[4:7], v[224:227]\n"
"ds_read_b64_tr_b16 a[120:121], v241 offset:512\n"
"v_cvt_pk_bf16_f32 v217, v194, v195\n"
"v_mfma_f32_16x16x32_bf16 v[168:171], a[84:87], a[24:27], v[168:171]\n"
"v_exp_f32 v198, v198\n"
"v_mfma_f32_16x16x32_bf16 v[168:171], a[88:91], a[44:47], v[168:171]\n"
"ds_read_b64_tr_b16 a[122:123], v241 offset:4608\n"
"v_cvt_pk_bf16_f32 v218, v196, v197\n"
"v_mfma_f32_16x16x32_bf16 v[168:171], a[92:95], a[64:67], v[168:171]\n"
"v_exp_f32 v199, v199\n"
"v_mfma_f32_16x16x32_bf16 v[172:175], a[96:99], a[4:7], v[224:227]\n"
"ds_read_b64_tr_b16 a[124:125], v242 offset:512\n"
"v_cvt_pk_bf16_f32 v219, v198, v199\n"
"v_mfma_f32_16x16x32_bf16 v[172:175], a[100:103], a[24:27], v[172:175]\n"
"v_mfma_f32_16x16x32_bf16 v[172:175], a[104:107], a[44:47], v[172:175]\n"
"ds_read_b64_tr_b16 a[126:127], v242 offset:4608\n"
"v_mfma_f32_16x16x32_bf16 v[172:175], a[108:111], a[64:67], v[172:175]\n"
"ds_read_b64_tr_b16 a[128:129], v241 offset:1024\n"
"v_add_u32 v240, 0, v245\n"
"v_mfma_f32_16x16x32_bf16 v[176:179], a[80:83], a[8:11], v[228:231]\n"
"v_exp_f32 v160, v160\n"
"v_mfma_f32_16x16x32_bf16 v[176:179], a[84:87], a[28:31], v[176:179]\n"
"ds_read_b64_tr_b16 a[130:131], v241 offset:5120\n"
"v_mfma_f32_16x16x32_bf16 v[176:179], a[88:91], a[48:51], v[176:179]\n"
"v_mfma_f32_16x16x32_bf16 v[176:179], a[92:95], a[68:71], v[176:179]\n"
"ds_read_b64_tr_b16 a[132:133], v242 offset:1024\n"
"v_mfma_f32_16x16x32_bf16 v[180:183], a[96:99], a[8:11], v[228:231]\n"
"v_exp_f32 v161, v161\n"
"v_mfma_f32_16x16x32_bf16 v[180:183], a[100:103], a[28:31], v[180:183]\n"
"ds_read_b64_tr_b16 a[134:135], v242 offset:5120\n"
"v_mfma_f32_16x16x32_bf16 v[180:183], a[104:107], a[48:51], v[180:183]\n"
"v_exp_f32 v162, v162\n"
"v_mfma_f32_16x16x32_bf16 v[180:183], a[108:111], a[68:71], v[180:183]\n"
"v_mfma_f32_16x16x32_bf16 v[184:187], a[80:83], a[12:15], v[232:235]\n"
"ds_read_b64_tr_b16 a[136:137], v241 offset:1536\n"
"v_mfma_f32_16x16x32_bf16 v[184:187], a[84:87], a[32:35], v[184:187]\n"
"v_exp_f32 v163, v163\n"
"v_mfma_f32_16x16x32_bf16 v[184:187], a[88:91], a[52:55], v[184:187]\n"
"ds_read_b64_tr_b16 a[138:139], v241 offset:5632\n"
"v_mfma_f32_16x16x32_bf16 v[184:187], a[92:95], a[72:75], v[184:187]\n"
"v_exp_f32 v164, v164\n"
"v_mfma_f32_16x16x32_bf16 v[188:191], a[96:99], a[12:15], v[232:235]\n"
"v_mfma_f32_16x16x32_bf16 v[188:191], a[100:103], a[32:35], v[188:191]\n"
"ds_read_b64_tr_b16 a[140:141], v242 offset:1536\n"
"v_mfma_f32_16x16x32_bf16 v[188:191], a[104:107], a[52:55], v[188:191]\n"
"v_exp_f32 v165, v165\n"
"v_mfma_f32_16x16x32_bf16 v[188:191], a[108:111], a[72:75], v[188:191]\n"
"ds_read_b64_tr_b16 a[142:143], v242 offset:5632\n"
"v_mfma_f32_16x16x32_bf16 v[192:195], a[80:83], a[16:19], v[236:239]\n"
"v_exp_f32 v166, v166\n"
"v_mfma_f32_16x16x32_bf16 v[192:195], a[84:87], a[36:39], v[192:195]\n"
"v_exp_f32 v167, v167\n"
"v_mfma_f32_16x16x32_bf16 v[192:195], a[88:91], a[56:59], v[192:195]\n"
"v_exp_f32 v168, v168\n"
"v_mfma_f32_16x16x32_bf16 v[192:195], a[92:95], a[76:79], v[192:195]\n"
"v_mfma_f32_16x16x32_bf16 v[196:199], a[96:99], a[16:19], v[236:239]\n"
"v_exp_f32 v169, v169\n"
"v_mfma_f32_16x16x32_bf16 v[196:199], a[100:103], a[36:39], v[196:199]\n"
"s_cmp_lg_u32 s74, 0\n"
"s_cbranch_scc1 L_tc%=\n"
"buffer_load_dword a175, v252, s[40:43], 0 offen\n"
"buffer_load_dword a175, v253, s[64:67], 0 offen\n"
"L_tc%=:\n"
"v_mfma_f32_16x16x32_bf16 v[196:199], a[104:107], a[56:59], v[196:199]\n"
"v_exp_f32 v170, v170\n"
"v_mfma_f32_16x16x32_bf16 v[196:199], a[108:111], a[76:79], v[196:199]\n"
"s_waitcnt vmcnt(4) lgkmcnt(0)\n"
"s_barrier\n"
"v_mfma_f32_16x16x32_bf16 v[0:3], a[112:115], v[200:203], v[0:3]\n"
"ds_read_b128 a[80:83], v240 offset:0\n"
"v_mfma_f32_16x16x32_bf16 v[20:23], a[116:119], v[200:203], v[20:23]\n"
"v_exp_f32 v171, v171\n"
"v_mfma_f32_16x16x32_bf16 v[40:43], a[120:123], v[200:203], v[40:43]\n"
"v_mfma_f32_16x16x32_bf16 v[60:63], a[124:127], v[200:203], v[60:63]\n"
"v_exp_f32 v172, v172\n"
"v_mfma_f32_16x16x32_bf16 v[80:83], a[128:131], v[200:203], v[80:83]\n"
"ds_read_b128 a[84:87], v240 offset:512\n"
"v_mfma_f32_16x16x32_bf16 v[100:103], a[132:135], v[200:203], v[100:103]\n"
"v_exp_f32 v173, v173\n"
"v_mfma_f32_16x16x32_bf16 v[120:123], a[136:139], v[200:203], v[120:123]\n"
"v_exp_f32 v174, v174\n"
"v_mfma_f32_16x16x32_bf16 v[140:143], a[140:143], v[200:203], v[140:143]\n"
"v_exp_f32 v175, v175\n"
"v_mfma_f32_16x16x32_bf16 a[144:147], a[164:167], v[200:203], a[144:147]\n"
"v_mfma_f32_16x16x32_bf16 v[4:7], a[112:115], v[204:207], v[4:7]\n"
"ds_read_b128 a[88:91], v240 offset:1024\n"
"v_mfma_f32_16x16x32_bf16 v[24:27], a[116:119], v[204:207], v[24:27]\n"
"v_cvt_pk_bf16_f32 v200, v160, v161\n"
"v_cvt_pk_bf16_f32 v201, v162, v163\n"
"v_mfma_f32_16x16x32_bf16 v[44:47], a[120:123], v[204:207], v[44:47]\n"
"v_exp_f32 v176, v176\n"
"v_mfma_f32_16x16x32_bf16 v[64:67], a[124:127], v[204:207], v[64:67]\n"
"v_exp_f32 v177, v177\n"
"v_mfma_f32_16x16x32_bf16 v[84:87], a[128:131], v[204:207], v[84:87]\n"
"ds_read_b128 a[92:95], v240 offset:1536\n"
"v_cvt_pk_bf16_f32 v202, v164, v165\n"
"v_mfma_f32_16x16x32_bf16 v[104:107], a[132:135], v[204:207], v[104:107]\n"
"v_mfma_f32_16x16x32_bf16 v[124:127], a[136:139], v[204:207], v[124:127]\n"
"v_cvt_pk_bf16_f32 v203, v166, v167\n"
"v_mfma_f32_16x16x32_bf16 v[144:147], a[140:143], v[204:207], v[144:147]\n"
"v_exp_f32 v178, v178\n"
"v_mfma_f32_16x16x32_bf16 a[148:151], a[164:167], v[204:207], a[148:151]\n"
"ds_read_b128 a[96:99], v240 offset:4096\n"
"v_mfma_f32_16x16x32_bf16 v[8:11], a[112:115], v[208:211], v[8:11]\n"
"v_exp_f32 v179, v179\n"
"v_mfma_f32_16x16x32_bf16 v[28:31], a[116:119], v[208:211], v[28:31]\n"
"v_cvt_pk_bf16_f32 v204, v168, v169\n"
"v_cvt_pk_bf16_f32 v205, v170, v171\n"
"v_mfma_f32_16x16x32_bf16 v[48:51], a[120:123], v[208:211], v[48:51]\n"
"v_mfma_f32_16x16x32_bf16 v[68:71], a[124:127], v[208:211], v[68:71]\n"
"ds_read_b128 a[100:103], v240 offset:4608\n"
"v_cvt_pk_bf16_f32 v206, v172, v173\n"
"v_mfma_f32_16x16x32_bf16 v[88:91], a[128:131], v[208:211], v[88:91]\n"
"v_exp_f32 v180, v180\n"
"v_mfma_f32_16x16x32_bf16 v[108:111], a[132:135], v[208:211], v[108:111]\n"
"v_cvt_pk_bf16_f32 v207, v174, v175\n"
"v_mfma_f32_16x16x32_bf16 v[128:131], a[136:139], v[208:211], v[128:131]\n"
"v_exp_f32 v181, v181\n"
"v_mfma_f32_16x16x32_bf16 v[148:151], a[140:143], v[208:211], v[148:151]\n"
"ds_read_b128 a[104:107], v240 offset:5120\n"
"v_add_u32 v241, 114688, v246\n"
"v_mfma_f32_16x16x32_bf16 a[152:155], a[164:167], v[208:211], a[152:155]\n"
"v_exp_f32 v182, v182\n"
"v_mfma_f32_16x16x32_bf16 v[12:15], a[112:115], v[212:215], v[12:15]\n"
"v_cvt_pk_bf16_f32 v208, v176, v177\n"
"v_cvt_pk_bf16_f32 v209, v178, v179\n"
"v_mfma_f32_16x16x32_bf16 v[32:35], a[116:119], v[212:215], v[32:35]\n"
"v_exp_f32 v183, v183\n"
"v_mfma_f32_16x16x32_bf16 v[52:55], a[120:123], v[212:215], v[52:55]\n"
"ds_read_b128 a[108:111], v240 offset:5632\n"
"v_cvt_pk_bf16_f32 v210, v180, v181\n"
"v_mfma_f32_16x16x32_bf16 v[72:75], a[124:127], v[212:215], v[72:75]\n"
"v_exp_f32 v184, v184\n"
"v_mfma_f32_16x16x32_bf16 v[92:95], a[128:131], v[212:215], v[92:95]\n"
"v_cvt_pk_bf16_f32 v211, v182, v183\n"
"v_xor_b32 v242, 32, v241\n"
"v_mfma_f32_16x16x32_bf16 v[112:115], a[132:135], v[212:215], v[112:115]\n"
"v_exp_f32 v185, v185\n"
"v_mfma_f32_16x16x32_bf16 v[132:135], a[136:139], v[212:215], v[132:135]\n"
"v_exp_f32 v186, v186\n"
"v_mfma_f32_16x16x32_bf16 v[152:155], a[140:143], v[212:215], v[152:155]\n"
"v_exp_f32 v187, v187\n"
"v_mfma_f32_16x16x32_bf16 a[156:159], a[164:167], v[212:215], a[156:159]\n"
"v_mfma_f32_16x16x32_bf16 v[16:19], a[112:115], v[216:219], v[16:19]\n"
"v_exp_f32 v188, v188\n"
"v_mfma_f32_16x16x32_bf16 v[36:39], a[116:119], v[216:219], v[36:39]\n"
"v_exp_f32 v189, v189\n"
"v_mfma_f32_16x16x32_bf16 v[56:59], a[120:123], v[216:219], v[56:59]\n"
"v_exp_f32 v190, v190\n"
"v_mfma_f32_16x16x32_bf16 v[76:79], a[124:127], v[216:219], v[76:79]\n"
"v_cvt_pk_bf16_f32 v212, v184, v185\n"
"v_cvt_pk_bf16_f32 v213, v186, v187\n"
"v_mfma_f32_16x16x32_bf16 v[96:99], a[128:131], v[216:219], v[96:99]\n"
"v_exp_f32 v191, v191\n"
"v_mfma_f32_16x16x32_bf16 v[116:119], a[132:135], v[216:219], v[116:119]\n"
"v_exp_f32 v192, v192\n"
"v_mfma_f32_16x16x32_bf16 v[136:139], a[136:139], v[216:219], v[136:139]\n"
"v_mfma_f32_16x16x32_bf16 v[156:159], a[140:143], v[216:219], v[156:159]\n"
"v_exp_f32 v193, v193\n"
"v_mfma_f32_16x16x32_bf16 a[160:163], a[164:167], v[216:219], a[160:163]\n"
"s_waitcnt lgkmcnt(0)\n"
"s_cmp_eq_u32 s74, 1\n"
"s_cbranch_scc1 L_p4%=\n"
"s_barrier\n"
"v_accvgpr_read_b32 v247, a172\n"
"v_accvgpr_read_b32 v248, a173\n"
"s_xor_b32 s78, s87, 1\n"
"s_lshl_b32 s78, s78, 9\n"
"v_add_u32 v247, s78, v247\n"
"v_accvgpr_read_b32 v243, a170\n"
"v_accvgpr_read_b32 v244, a171\n"
"s_xor_b32 s78, s84, 262144\n"
"v_add_u32 v243, s78, v243\n"
"v_add_u32 v244, s78, v244\n"
"s_add_u32 s19, s80, 32768\n"
"s_add_u32 s20, s80, 49152\n"
"s_add_u32 s21, s80, 86016\n"
"s_add_u32 s76, s81, 65536\n"
"s_add_u32 s77, s81, 102400\n"
"s_mov_b32 s17, 0\n"
"s_mov_b32 s16, 0\n"
"s_bfe_u32 s17, %[info], 0x4000e\n"
"s_lshl_b32 s16, s17, 19\n"
"s_lshl_b32 s17, s17, 7\n"
"v_add_u32 v248, s17, v248\n"
"s_add_u32 s79, s80, 147456\n"
"v_add_u32 v245, 32768, v249\n"
"v_add_u32 v246, 65536, v250\n"
"v_mfma_f32_16x16x32_bf16 v[160:163], a[80:83], a[0:3], v[220:223]\n"
"ds_read_b64_tr_b16 a[112:113], v241 offset:0\n"
"s_add_u32 m0, s19, 0\n"
"v_mfma_f32_16x16x32_bf16 v[160:163], a[84:87], a[20:23], v[160:163]\n"
"v_exp_f32 v194, v194\n"
"v_mfma_f32_16x16x32_bf16 v[160:163], a[88:91], a[40:43], v[160:163]\n"
"ds_read_b64_tr_b16 a[114:115], v241 offset:4096\n"
"v_cvt_pk_bf16_f32 v214, v188, v189\n"
"v_mfma_f32_16x16x32_bf16 v[160:163], a[92:95], a[60:63], v[160:163]\n"
"v_exp_f32 v195, v195\n"
"v_mfma_f32_16x16x32_bf16 v[164:167], a[96:99], a[0:3], v[220:223]\n"
"ds_read_b64_tr_b16 a[116:117], v242 offset:0\n"
"v_cvt_pk_bf16_f32 v215, v190, v191\n"
"v_mfma_f32_16x16x32_bf16 v[164:167], a[100:103], a[20:23], v[164:167]\n"
"v_exp_f32 v196, v196\n"
"v_mfma_f32_16x16x32_bf16 v[164:167], a[104:107], a[40:43], v[164:167]\n"
"ds_read_b64_tr_b16 a[118:119], v242 offset:4096\n"
"v_cvt_pk_bf16_f32 v216, v192, v193\n"
"v_mfma_f32_16x16x32_bf16 v[164:167], a[108:111], a[60:63], v[164:167]\n"
"v_exp_f32 v197, v197\n"
"v_mfma_f32_16x16x32_bf16 v[168:171], a[80:83], a[4:7], v[224:227]\n"
"ds_read_b64_tr_b16 a[120:121], v241 offset:512\n"
"v_cvt_pk_bf16_f32 v217, v194, v195\n"
"v_mfma_f32_16x16x32_bf16 v[168:171], a[84:87], a[24:27], v[168:171]\n"
"v_exp_f32 v198, v198\n"
"v_mfma_f32_16x16x32_bf16 v[168:171], a[88:91], a[44:47], v[168:171]\n"
"ds_read_b64_tr_b16 a[122:123], v241 offset:4608\n"
"v_cvt_pk_bf16_f32 v218, v196, v197\n"
"v_mfma_f32_16x16x32_bf16 v[168:171], a[92:95], a[64:67], v[168:171]\n"
"v_exp_f32 v199, v199\n"
"v_mfma_f32_16x16x32_bf16 v[172:175], a[96:99], a[4:7], v[224:227]\n"
"ds_read_b64_tr_b16 a[124:125], v242 offset:512\n"
"v_cvt_pk_bf16_f32 v219, v198, v199\n"
"v_mfma_f32_16x16x32_bf16 v[172:175], a[100:103], a[24:27], v[172:175]\n"
"buffer_load_dwordx4 v247, s[40:43], s16 offen lds\n"
"v_mfma_f32_16x16x32_bf16 v[172:175], a[104:107], a[44:47], v[172:175]\n"
"ds_read_b64_tr_b16 a[126:127], v242 offset:4608\n"
"s_add_u32 m0, s19, 896\n"
"v_mfma_f32_16x16x32_bf16 v[172:175], a[108:111], a[64:67], v[172:175]\n"
"ds_read_b64_tr_b16 a[128:129], v241 offset:1024\n"
"v_mfma_f32_16x16x32_bf16 v[176:179], a[80:83], a[8:11], v[228:231]\n"
"v_exp_f32 v160, v160\n"
"v_mfma_f32_16x16x32_bf16 v[176:179], a[84:87], a[28:31], v[176:179]\n"
"ds_read_b64_tr_b16 a[130:131], v241 offset:5120\n"
"v_mfma_f32_16x16x32_bf16 v[176:179], a[88:91], a[48:51], v[176:179]\n"
"buffer_load_dwordx4 v247, s[40:43], s16 offen offset:128 lds\n"
"v_mfma_f32_16x16x32_bf16 v[176:179], a[92:95], a[68:71], v[176:179]\n"
"ds_read_b64_tr_b16 a[132:133], v242 offset:1024\n"
"s_add_u32 m0, s19, 7936\n"
"v_mfma_f32_16x16x32_bf16 v[180:183], a[96:99], a[8:11], v[228:231]\n"
"v_exp_f32 v161, v161\n"
"v_mfma_f32_16x16x32_bf16 v[180:183], a[100:103], a[28:31], v[180:183]\n"
"ds_read_b64_tr_b16 a[134:135], v242 offset:5120\n"
"v_mfma_f32_16x16x32_bf16 v[180:183], a[104:107], a[48:51], v[180:183]\n"
"v_exp_f32 v162, v162\n"
"v_mfma_f32_16x16x32_bf16 v[180:183], a[108:111], a[68:71], v[180:183]\n"
"buffer_load_dwordx4 v247, s[40:43], s16 offen offset:256 lds\n"
"v_mfma_f32_16x16x32_bf16 v[184:187], a[80:83], a[12:15], v[232:235]\n"
"ds_read_b64_tr_b16 a[136:137], v241 offset:1536\n"
"s_add_u32 m0, s19, 8832\n"
"v_mfma_f32_16x16x32_bf16 v[184:187], a[84:87], a[32:35], v[184:187]\n"
"v_exp_f32 v163, v163\n"
"v_mfma_f32_16x16x32_bf16 v[184:187], a[88:91], a[52:55], v[184:187]\n"
"ds_read_b64_tr_b16 a[138:139], v241 offset:5632\n"
"v_mfma_f32_16x16x32_bf16 v[184:187], a[92:95], a[72:75], v[184:187]\n"
"v_exp_f32 v164, v164\n"
"v_mfma_f32_16x16x32_bf16 v[188:191], a[96:99], a[12:15], v[232:235]\n"
"buffer_load_dwordx4 v247, s[40:43], s16 offen offset:384 lds\n"
"v_mfma_f32_16x16x32_bf16 v[188:191], a[100:103], a[32:35], v[188:191]\n"
"ds_read_b64_tr_b16 a[140:141], v242 offset:1536\n"
"s_add_u32 s16, s16, 262144\n"
"v_mfma_f32_16x16x32_bf16 v[188:191], a[104:107], a[52:55], v[188:191]\n"
"v_exp_f32 v165, v165\n"
"v_mfma_f32_16x16x32_bf16 v[188:191], a[108:111], a[72:75], v[188:191]\n"
"ds_read_b64_tr_b16 a[142:143], v242 offset:5632\n"
"s_cmp_eq_u32 s16, 5242880\n"
"s_cselect_b32 s16, 0, s16\n"
"v_mfma_f32_16x16x32_bf16 v[192:195], a[80:83], a[16:19], v[236:239]\n"
"v_exp_f32 v166, v166\n"
"s_add_u32 m0, s76, 0\n"
"v_mfma_f32_16x16x32_bf16 v[192:195], a[84:87], a[36:39], v[192:195]\n"
"v_exp_f32 v167, v167\n"
"v_mfma_f32_16x16x32_bf16 v[192:195], a[88:91], a[56:59], v[192:195]\n"
"v_exp_f32 v168, v168\n"
"v_mfma_f32_16x16x32_bf16 v[192:195], a[92:95], a[76:79], v[192:195]\n"
"buffer_load_dwordx4 v248, s[36:39], 0 offen lds\n"
"v_mfma_f32_16x16x32_bf16 v[196:199], a[96:99], a[16:19], v[236:239]\n"
"v_exp_f32 v169, v169\n"
"s_add_u32 m0, s76, 4096\n"
"v_mfma_f32_16x16x32_bf16 v[196:199], a[100:103], a[36:39], v[196:199]\n"
"buffer_load_dwordx4 v248, s[36:39], s69 offen lds\n"
"v_mfma_f32_16x16x32_bf16 v[196:199], a[104:107], a[56:59], v[196:199]\n"
"v_exp_f32 v170, v170\n"
"v_mfma_f32_16x16x32_bf16 v[196:199], a[108:111], a[76:79], v[196:199]\n"
"s_waitcnt vmcnt(8) lgkmcnt(0)\n"
"s_barrier\n"
"v_mfma_f32_16x16x32_bf16 v[0:3], a[112:115], v[200:203], v[0:3]\n"
"ds_read_b64_tr_b16 a[176:177], v241 offset:16384\n"
"s_add_u32 m0, s76, 8192\n"
"v_mfma_f32_16x16x32_bf16 v[20:23], a[116:119], v[200:203], v[20:23]\n"
"v_exp_f32 v171, v171\n"
"v_mfma_f32_16x16x32_bf16 v[40:43], a[120:123], v[200:203], v[40:43]\n"
"buffer_load_dwordx4 v248, s[36:39], s70 offen lds\n"
"v_mfma_f32_16x16x32_bf16 v[60:63], a[124:127], v[200:203], v[60:63]\n"
"v_exp_f32 v172, v172\n"
"v_mfma_f32_16x16x32_bf16 v[80:83], a[128:131], v[200:203], v[80:83]\n"
"ds_read_b64_tr_b16 a[178:179], v241 offset:20480\n"
"s_add_u32 m0, s76, 12288\n"
"v_mfma_f32_16x16x32_bf16 v[100:103], a[132:135], v[200:203], v[100:103]\n"
"v_exp_f32 v173, v173\n"
"v_mfma_f32_16x16x32_bf16 v[120:123], a[136:139], v[200:203], v[120:123]\n"
"v_exp_f32 v174, v174\n"
"v_mfma_f32_16x16x32_bf16 v[140:143], a[140:143], v[200:203], v[140:143]\n"
"v_exp_f32 v175, v175\n"
"v_mfma_f32_16x16x32_bf16 a[144:147], a[164:167], v[200:203], a[144:147]\n"
"buffer_load_dwordx4 v248, s[36:39], s71 offen lds\n"
"v_mfma_f32_16x16x32_bf16 v[4:7], a[112:115], v[204:207], v[4:7]\n"
"ds_read_b64_tr_b16 a[180:181], v242 offset:16384\n"
"s_add_u32 m0, s76, 16384\n"
"v_mfma_f32_16x16x32_bf16 v[24:27], a[116:119], v[204:207], v[24:27]\n"
"ds_read_b64_tr_b16 a[182:183], v242 offset:20480\n"
"v_cvt_pk_bf16_f32 v200, v160, v161\n"
"v_cvt_pk_bf16_f32 v201, v162, v163\n"
"v_mfma_f32_16x16x32_bf16 v[44:47], a[120:123], v[204:207], v[44:47]\n"
"v_exp_f32 v176, v176\n"
"v_mfma_f32_16x16x32_bf16 v[64:67], a[124:127], v[204:207], v[64:67]\n"
"v_exp_f32 v177, v177\n"
"v_mfma_f32_16x16x32_bf16 v[84:87], a[128:131], v[204:207], v[84:87]\n"
"ds_read_b64_tr_b16 a[184:185], v241 offset:16896\n"
"v_cvt_pk_bf16_f32 v202, v164, v165\n"
"v_mfma_f32_16x16x32_bf16 v[104:107], a[132:135], v[204:207], v[104:107]\n"
"buffer_load_dwordx4 v248, s[36:39], s72 offen lds\n"
"v_mfma_f32_16x16x32_bf16 v[124:127], a[136:139], v[204:207], v[124:127]\n"
"ds_read_b64_tr_b16 a[186:187], v241 offset:20992\n"
"v_cvt_pk_bf16_f32 v203, v166, v167\n"
"v_mfma_f32_16x16x32_bf16 v[144:147], a[140:143], v[204:207], v[144:147]\n"
"v_exp_f32 v178, v178\n"
"v_mfma_f32_16x16x32_bf16 a[148:151], a[164:167], v[204:207], a[148:151]\n"
"ds_read_b64_tr_b16 a[188:189], v242 offset:16896\n"
"s_add_u32 s17, s17, 128\n"
"v_mfma_f32_16x16x32_bf16 v[8:11], a[112:115], v[208:211], v[8:11]\n"
"v_exp_f32 v179, v179\n"
"v_mfma_f32_16x16x32_bf16 v[28:31], a[116:119], v[208:211], v[28:31]\n"
"ds_read_b64_tr_b16 a[190:191], v242 offset:20992\n"
"v_cvt_pk_bf16_f32 v204, v168, v169\n"
"v_cvt_pk_bf16_f32 v205, v170, v171\n"
"v_mfma_f32_16x16x32_bf16 v[48:51], a[120:123], v[208:211], v[48:51]\n"
"s_cmp_eq_u32 s17, 1280\n"
"s_cselect_b32 s18, -1152, s24\n"
"s_cselect_b32 s17, 0, s17\n"
"v_mfma_f32_16x16x32_bf16 v[68:71], a[124:127], v[208:211], v[68:71]\n"
"ds_read_b64_tr_b16 a[192:193], v241 offset:17408\n"
"v_cvt_pk_bf16_f32 v206, v172, v173\n"
"v_mfma_f32_16x16x32_bf16 v[88:91], a[128:131], v[208:211], v[88:91]\n"
"v_exp_f32 v180, v180\n"
"v_mfma_f32_16x16x32_bf16 v[108:111], a[132:135], v[208:211], v[108:111]\n"
"ds_read_b64_tr_b16 a[194:195], v241 offset:21504\n"
"v_cvt_pk_bf16_f32 v207, v174, v175\n"
"v_mfma_f32_16x16x32_bf16 v[128:131], a[136:139], v[208:211], v[128:131]\n"
"v_exp_f32 v181, v181\n"
"v_mfma_f32_16x16x32_bf16 v[148:151], a[140:143], v[208:211], v[148:151]\n"
"ds_read_b64_tr_b16 a[196:197], v242 offset:17408\n"
"v_add_u32 v248, s18, v248\n"
"v_mfma_f32_16x16x32_bf16 a[152:155], a[164:167], v[208:211], a[152:155]\n"
"v_exp_f32 v182, v182\n"
"v_mfma_f32_16x16x32_bf16 v[12:15], a[112:115], v[212:215], v[12:15]\n"
"ds_read_b64_tr_b16 a[198:199], v242 offset:21504\n"
"v_cvt_pk_bf16_f32 v208, v176, v177\n"
"v_cvt_pk_bf16_f32 v209, v178, v179\n"
"v_mfma_f32_16x16x32_bf16 v[32:35], a[116:119], v[212:215], v[32:35]\n"
"v_exp_f32 v183, v183\n"
"v_mfma_f32_16x16x32_bf16 v[52:55], a[120:123], v[212:215], v[52:55]\n"
"ds_read_b64_tr_b16 a[200:201], v241 offset:17920\n"
"v_cvt_pk_bf16_f32 v210, v180, v181\n"
"v_mfma_f32_16x16x32_bf16 v[72:75], a[124:127], v[212:215], v[72:75]\n"
"v_exp_f32 v184, v184\n"
"v_mfma_f32_16x16x32_bf16 v[92:95], a[128:131], v[212:215], v[92:95]\n"
"ds_read_b64_tr_b16 a[202:203], v241 offset:22016\n"
"v_cvt_pk_bf16_f32 v211, v182, v183\n"
"v_mfma_f32_16x16x32_bf16 v[112:115], a[132:135], v[212:215], v[112:115]\n"
"v_exp_f32 v185, v185\n"
"v_mfma_f32_16x16x32_bf16 v[132:135], a[136:139], v[212:215], v[132:135]\n"
"v_exp_f32 v186, v186\n"
"v_mfma_f32_16x16x32_bf16 v[152:155], a[140:143], v[212:215], v[152:155]\n"
"v_exp_f32 v187, v187\n"
"v_mfma_f32_16x16x32_bf16 a[156:159], a[164:167], v[212:215], a[156:159]\n"
"ds_read_b64_tr_b16 a[204:205], v242 offset:17920\n"
"s_add_u32 m0, s20, 0\n"
"v_mfma_f32_16x16x32_bf16 v[16:19], a[112:115], v[216:219], v[16:19]\n"
"v_exp_f32 v188, v188\n"
"v_mfma_f32_16x16x32_bf16 v[36:39], a[116:119], v[216:219], v[36:39]\n"
"v_exp_f32 v189, v189\n"
"v_mfma_f32_16x16x32_bf16 v[56:59], a[120:123], v[216:219], v[56:59]\n"
"v_exp_f32 v190, v190\n"
"v_mfma_f32_16x16x32_bf16 v[76:79], a[124:127], v[216:219], v[76:79]\n"
"ds_read_b64_tr_b16 a[206:207], v242 offset:22016\n"
"v_cvt_pk_bf16_f32 v212, v184, v185\n"
"v_cvt_pk_bf16_f32 v213, v186, v187\n"
"v_mfma_f32_16x16x32_bf16 v[96:99], a[128:131], v[216:219], v[96:99]\n"
"v_exp_f32 v191, v191\n"
"v_mfma_f32_16x16x32_bf16 v[116:119], a[132:135], v[216:219], v[116:119]\n"
"v_exp_f32 v192, v192\n"
"v_mfma_f32_16x16x32_bf16 v[136:139], a[136:139], v[216:219], v[136:139]\n"
"buffer_load_dwordx4 v247, s[40:43], s16 offen lds\n"
"v_mfma_f32_16x16x32_bf16 v[156:159], a[140:143], v[216:219], v[156:159]\n"
"v_exp_f32 v193, v193\n"
"v_mfma_f32_16x16x32_bf16 a[160:163], a[164:167], v[216:219], a[160:163]\n"
"s_waitcnt lgkmcnt(0)\n"
"s_barrier\n"
"v_mfma_f32_16x16x32_bf16 v[0:3], a[176:179], v[200:203], v[0:3]\n"
"v_exp_f32 v194, v194\n"
"v_mfma_f32_16x16x32_bf16 v[20:23], a[180:183], v[200:203], v[20:23]\n"
"v_cvt_pk_bf16_f32 v214, v188, v189\n"
"v_cvt_pk_bf16_f32 v215, v190, v191\n"
"s_add_u32 m0, s20, 896\n"
"v_mfma_f32_16x16x32_bf16 v[40:43], a[184:187], v[200:203], v[40:43]\n"
"v_exp_f32 v195, v195\n"
"v_mfma_f32_16x16x32_bf16 v[60:63], a[188:191], v[200:203], v[60:63]\n"
"v_cvt_pk_bf16_f32 v216, v192, v193\n"
"v_cvt_pk_bf16_f32 v217, v194, v195\n"
"v_mfma_f32_16x16x32_bf16 v[80:83], a[192:195], v[200:203], v[80:83]\n"
"v_exp_f32 v196, v196\n"
"v_mfma_f32_16x16x32_bf16 v[100:103], a[196:199], v[200:203], v[100:103]\n"
"buffer_load_dwordx4 v247, s[40:43], s16 offen offset:128 lds\n"
"v_mfma_f32_16x16x32_bf16 v[120:123], a[200:203], v[200:203], v[120:123]\n"
"v_exp_f32 v197, v197\n"
"v_mfma_f32_16x16x32_bf16 v[140:143], a[204:207], v[200:203], v[140:143]\n"
"v_cvt_pk_bf16_f32 v218, v196, v197\n"
"s_add_u32 m0, s20, 7936\n"
"v_mfma_f32_16x16x32_bf16 a[144:147], a[164:167], v[200:203], a[144:147]\n"
"v_exp_f32 v198, v198\n"
"v_mfma_f32_16x16x32_bf16 v[4:7], a[176:179], v[204:207], v[4:7]\n"
"buffer_load_dwordx4 v247, s[40:43], s16 offen offset:256 lds\n"
"v_mfma_f32_16x16x32_bf16 v[24:27], a[180:183], v[204:207], v[24:27]\n"
"v_exp_f32 v199, v199\n"
"v_mfma_f32_16x16x32_bf16 v[44:47], a[184:187], v[204:207], v[44:47]\n"
"v_cvt_pk_bf16_f32 v219, v198, v199\n"
"s_add_u32 m0, s20, 8832\n"
"v_mfma_f32_16x16x32_bf16 v[64:67], a[188:191], v[204:207], v[64:67]\n"
"buffer_load_dwordx4 v247, s[40:43], s16 offen offset:384 lds\n"
"v_mfma_f32_16x16x32_bf16 v[84:87], a[192:195], v[204:207], v[84:87]\n"
"s_add_u32 s16, s16, 262144\n"
"v_mfma_f32_16x16x32_bf16 v[104:107], a[196:199], v[204:207], v[104:107]\n"
"s_cmp_eq_u32 s16, 5242880\n"
"s_cselect_b32 s16, 0, s16\n"
"v_accvgpr_read_b32 v232, a144\n"
"v_mfma_f32_16x16x32_bf16 v[124:127], a[200:203], v[204:207], v[124:127]\n"
"s_add_u32 m0, s21, 0\n"
"s_nop 0\n"
"v_mfma_f32_16x16x32_bf16 v[144:147], a[204:207], v[204:207], v[144:147]\n"
"v_rcp_f32 v232, v232\n"
"v_mfma_f32_16x16x32_bf16 a[148:151], a[164:167], v[204:207], a[148:151]\n"
"buffer_load_dwordx4 v247, s[40:43], s16 offen lds\n"
"v_mfma_f32_16x16x32_bf16 v[8:11], a[176:179], v[208:211], v[8:11]\n"
"s_add_u32 m0, s21, 896\n"
"s_nop 0\n"
"v_mfma_f32_16x16x32_bf16 v[28:31], a[180:183], v[208:211], v[28:31]\n"
"v_mul_f32 v0, v232, v0\n"
"v_mul_f32 v1, v232, v1\n"
"v_mfma_f32_16x16x32_bf16 v[48:51], a[184:187], v[208:211], v[48:51]\n"
"buffer_load_dwordx4 v247, s[40:43], s16 offen offset:128 lds\n"
"v_mfma_f32_16x16x32_bf16 v[68:71], a[188:191], v[208:211], v[68:71]\n"
"s_add_u32 m0, s21, 7936\n"
"v_mfma_f32_16x16x32_bf16 v[88:91], a[192:195], v[208:211], v[88:91]\n"
"s_waitcnt vmcnt(6)\n"
"s_barrier\n"
"v_mfma_f32_16x16x32_bf16 v[108:111], a[196:199], v[208:211], v[108:111]\n"
"buffer_load_dwordx4 v247, s[40:43], s16 offen offset:256 lds\n"
"v_mfma_f32_16x16x32_bf16 v[128:131], a[200:203], v[208:211], v[128:131]\n"
"ds_read_b128 v[160:163], v246 offset:0\n"
"s_add_u32 m0, s21, 8832\n"
"v_mfma_f32_16x16x32_bf16 v[148:151], a[204:207], v[208:211], v[148:151]\n"
"ds_read_b128 v[164:167], v246 offset:2048\n"
"v_mfma_f32_16x16x32_bf16 a[152:155], a[164:167], v[208:211], a[152:155]\n"
"buffer_load_dwordx4 v247, s[40:43], s16 offen offset:384 lds\n"
"v_mfma_f32_16x16x32_bf16 v[12:15], a[176:179], v[212:215], v[12:15]\n"
"ds_read_b128 v[168:171], v246 offset:4096\n"
"s_add_u32 s16, s16, 262144\n"
"v_mfma_f32_16x16x32_bf16 v[32:35], a[180:183], v[212:215], v[32:35]\n"
"ds_read_b128 v[172:175], v246 offset:6144\n"
"s_cmp_eq_u32 s16, 5242880\n"
"s_cselect_b32 s16, 0, s16\n"
"v_mfma_f32_16x16x32_bf16 v[52:55], a[184:187], v[212:215], v[52:55]\n"
"ds_read_b128 v[176:179], v246 offset:8192\n"
"s_add_u32 m0, s77, 0\n"
"v_mfma_f32_16x16x32_bf16 v[72:75], a[188:191], v[212:215], v[72:75]\n"
"ds_read_b64_tr_b16 a[80:81], v245 offset:0\n"
"v_mfma_f32_16x16x32_bf16 v[92:95], a[192:195], v[212:215], v[92:95]\n"
"buffer_load_dwordx4 v248, s[36:39], 0 offen lds\n"
"v_mfma_f32_16x16x32_bf16 v[112:115], a[196:199], v[212:215], v[112:115]\n"
"ds_read_b64_tr_b16 a[82:83], v245 offset:256\n"
"v_mfma_f32_16x16x32_bf16 v[132:135], a[200:203], v[212:215], v[132:135]\n"
"ds_read_b64_tr_b16 a[84:85], v245 offset:8\n"
"v_mfma_f32_16x16x32_bf16 v[152:155], a[204:207], v[212:215], v[152:155]\n"
"ds_read_b64_tr_b16 a[86:87], v245 offset:264\n"
"v_mfma_f32_16x16x32_bf16 a[156:159], a[164:167], v[212:215], a[156:159]\n"
"ds_read_b64_tr_b16 a[88:89], v245 offset:512\n"
"v_mfma_f32_16x16x32_bf16 v[16:19], a[176:179], v[216:219], v[16:19]\n"
"ds_read_b64_tr_b16 a[90:91], v245 offset:768\n"
"v_mfma_f32_16x16x32_bf16 v[36:39], a[180:183], v[216:219], v[36:39]\n"
"ds_read_b64_tr_b16 a[92:93], v245 offset:520\n"
"v_mfma_f32_16x16x32_bf16 v[56:59], a[184:187], v[216:219], v[56:59]\n"
"ds_read_b64_tr_b16 a[94:95], v245 offset:776\n"
"v_mfma_f32_16x16x32_bf16 v[76:79], a[188:191], v[216:219], v[76:79]\n"
"ds_read_b64_tr_b16 a[96:97], v245 offset:1024\n"
"v_mfma_f32_16x16x32_bf16 v[96:99], a[192:195], v[216:219], v[96:99]\n"
"ds_read_b64_tr_b16 a[98:99], v245 offset:1280\n"
"v_mfma_f32_16x16x32_bf16 v[116:119], a[196:199], v[216:219], v[116:119]\n"
"ds_read_b64_tr_b16 a[100:101], v245 offset:1032\n"
"v_mfma_f32_16x16x32_bf16 v[136:139], a[200:203], v[216:219], v[136:139]\n"
"ds_read_b64_tr_b16 a[102:103], v245 offset:1288\n"
"v_mfma_f32_16x16x32_bf16 v[156:159], a[204:207], v[216:219], v[156:159]\n"
"ds_read_b64_tr_b16 a[104:105], v245 offset:1536\n"
"v_mfma_f32_16x16x32_bf16 a[160:163], a[164:167], v[216:219], a[160:163]\n"
"ds_read_b64_tr_b16 a[106:107], v245 offset:1792\n"
"s_nop 7\n"
"v_accvgpr_read_b32 v220, a144\n"
"v_accvgpr_read_b32 v221, a148\n"
"v_accvgpr_read_b32 v222, a152\n"
"v_accvgpr_read_b32 v223, a156\n"
"v_accvgpr_read_b32 v224, a160\n"
"v_add_f32 v225, v220, v221\n"
"v_add_f32 v226, v222, v223\n"
"v_add_f32 v225, v225, v224\n"
"v_add_f32 v225, v225, v226\n"
"v_cmp_nge_f32 vcc, s91, v225\n"
"s_nop 3\n"
"s_cbranch_vccnz L_slow0%=\n"
"L_sback0%=:\n"
"v_pk_mul_f32 v[2:3], v[2:3], v[232:233] op_sel:[0,0] op_sel_hi:[1,0]\n"
"v_pk_mul_f32 v[20:21], v[20:21], v[232:233] op_sel:[0,0] op_sel_hi:[1,0]\n"
"v_pk_mul_f32 v[22:23], v[22:23], v[232:233] op_sel:[0,0] op_sel_hi:[1,0]\n"
"v_cvt_pk_bf16_f32 v0, v0, v1\n"
"ds_read_b64_tr_b16 a[110:111], v245 offset:1800\n"
"v_cvt_pk_bf16_f32 v1, v2, v3\n"
"v_cvt_pk_bf16_f32 v2, v20, v21\n"
"v_cvt_pk_bf16_f32 v3, v22, v23\n"
"v_pk_mul_f32 v[40:41], v[40:41], v[232:233] op_sel:[0,0] op_sel_hi:[1,0]\n"
"v_accvgpr_write_b32 a176, v0\n"
"v_accvgpr_write_b32 a177, v1\n"
"v_accvgpr_write_b32 a178, v2\n"
"v_accvgpr_write_b32 a179, v3\n"
"v_pk_mul_f32 v[42:43], v[42:43], v[232:233] op_sel:[0,0] op_sel_hi:[1,0]\n"
"v_pk_mul_f32 v[60:61], v[60:61], v[232:233] op_sel:[0,0] op_sel_hi:[1,0]\n"
"v_pk_mul_f32 v[62:63], v[62:63], v[232:233] op_sel:[0,0] op_sel_hi:[1,0]\n"
"v_cvt_pk_bf16_f32 v40, v40, v41\n"
"ds_read_b64_tr_b16 a[108:109], v245 offset:1544\n"
"v_cvt_pk_bf16_f32 v41, v42, v43\n"
"v_cvt_pk_bf16_f32 v42, v60, v61\n"
"v_cvt_pk_bf16_f32 v43, v62, v63\n"
"v_pk_mul_f32 v[80:81], v[80:81], v[232:233] op_sel:[0,0] op_sel_hi:[1,0]\n"
"v_accvgpr_write_b32 a196, v40\n"
"v_accvgpr_write_b32 a197, v41\n"
"v_accvgpr_write_b32 a198, v42\n"
"v_accvgpr_write_b32 a199, v43\n"
"v_pk_mul_f32 v[82:83], v[82:83], v[232:233] op_sel:[0,0] op_sel_hi:[1,0]\n"
"v_pk_mul_f32 v[100:101], v[100:101], v[232:233] op_sel:[0,0] op_sel_hi:[1,0]\n"
"v_pk_mul_f32 v[102:103], v[102:103], v[232:233] op_sel:[0,0] op_sel_hi:[1,0]\n"
"v_cvt_pk_bf16_f32 v80, v80, v81\n"
"s_add_u32 m0, s77, 4096\n"
"s_nop 0\n"
"buffer_load_dwordx4 v248, s[36:39], s69 offen lds\n"
"v_cvt_pk_bf16_f32 v81, v82, v83\n"
"v_cvt_pk_bf16_f32 v82, v100, v101\n"
"v_cvt_pk_bf16_f32 v83, v102, v103\n"
"v_pk_mul_f32 v[120:121], v[120:121], v[232:233] op_sel:[0,0] op_sel_hi:[1,0]\n"
"v_accvgpr_write_b32 a216, v80\n"
"v_accvgpr_write_b32 a217, v81\n"
"v_accvgpr_write_b32 a218, v82\n"
"v_accvgpr_write_b32 a219, v83\n"
"v_pk_mul_f32 v[122:123], v[122:123], v[232:233] op_sel:[0,0] op_sel_hi:[1,0]\n"
"v_pk_mul_f32 v[140:141], v[140:141], v[232:233] op_sel:[0,0] op_sel_hi:[1,0]\n"
"v_pk_mul_f32 v[142:143], v[142:143], v[232:233] op_sel:[0,0] op_sel_hi:[1,0]\n"
"v_cvt_pk_bf16_f32 v120, v120, v121\n"
"s_add_u32 m0, s77, 8192\n"
"s_nop 0\n"
"buffer_load_dwordx4 v248, s[36:39], s70 offen lds\n"
"v_cvt_pk_bf16_f32 v121, v122, v123\n"
"v_cvt_pk_bf16_f32 v122, v140, v141\n"
"v_cvt_pk_bf16_f32 v123, v142, v143\n"
"v_accvgpr_read_b32 v233, a148\n"
"v_accvgpr_write_b32 a236, v120\n"
"v_accvgpr_write_b32 a237, v121\n"
"v_accvgpr_write_b32 a238, v122\n"
"v_accvgpr_write_b32 a239, v123\n"
"s_nop 0\n"
"v_rcp_f32 v233, v233\n"
"s_nop 0\n"
"v_pk_mul_f32 v[4:5], v[4:5], v[232:233] op_sel:[0,1] op_sel_hi:[1,1]\n"
"s_add_u32 m0, s77, 12288\n"
"s_nop 0\n"
"buffer_load_dwordx4 v248, s[36:39], s71 offen lds\n"
"v_pk_mul_f32 v[6:7], v[6:7], v[232:233] op_sel:[0,1] op_sel_hi:[1,1]\n"
"v_pk_mul_f32 v[24:25], v[24:25], v[232:233] op_sel:[0,1] op_sel_hi:[1,1]\n"
"v_pk_mul_f32 v[26:27], v[26:27], v[232:233] op_sel:[0,1] op_sel_hi:[1,1]\n"
"v_cvt_pk_bf16_f32 v4, v4, v5\n"
"v_cvt_pk_bf16_f32 v5, v6, v7\n"
"v_cvt_pk_bf16_f32 v6, v24, v25\n"
"v_cvt_pk_bf16_f32 v7, v26, v27\n"
"v_pk_mul_f32 v[44:45], v[44:45], v[232:233] op_sel:[0,1] op_sel_hi:[1,1]\n"
"s_add_u32 m0, s77, 16384\n"
"s_nop 0\n"
"buffer_load_dwordx4 v248, s[36:39], s72 offen lds\n"
"s_add_u32 s17, s17, 128\n"
"s_cmp_eq_u32 s17, 1280\n"
"s_cselect_b32 s18, -1152, s24\n"
"s_cselect_b32 s17, 0, s17\n"
"v_add_u32 v248, s18, v248\n"
"v_accvgpr_write_b32 a180, v4\n"
"v_accvgpr_write_b32 a181, v5\n"
"v_accvgpr_write_b32 a182, v6\n"
"v_accvgpr_write_b32 a183, v7\n"
"v_pk_mul_f32 v[46:47], v[46:47], v[232:233] op_sel:[0,1] op_sel_hi:[1,1]\n"
"v_pk_mul_f32 v[64:65], v[64:65], v[232:233] op_sel:[0,1] op_sel_hi:[1,1]\n"
"v_pk_mul_f32 v[66:67], v[66:67], v[232:233] op_sel:[0,1] op_sel_hi:[1,1]\n"
"v_cvt_pk_bf16_f32 v44, v44, v45\n"
"v_cvt_pk_bf16_f32 v45, v46, v47\n"
"v_cvt_pk_bf16_f32 v46, v64, v65\n"
"v_cvt_pk_bf16_f32 v47, v66, v67\n"
"v_pk_mul_f32 v[84:85], v[84:85], v[232:233] op_sel:[0,1] op_sel_hi:[1,1]\n"
"s_add_u32 m0, s79, 0\n"
"s_nop 0\n"
"buffer_load_dwordx4 v247, s[40:43], s16 offen lds\n"
"v_accvgpr_write_b32 a200, v44\n"
"v_accvgpr_write_b32 a201, v45\n"
"v_accvgpr_write_b32 a202, v46\n"
"v_accvgpr_write_b32 a203, v47\n"
"v_pk_mul_f32 v[86:87], v[86:87], v[232:233] op_sel:[0,1] op_sel_hi:[1,1]\n"
"v_pk_mul_f32 v[104:105], v[104:105], v[232:233] op_sel:[0,1] op_sel_hi:[1,1]\n"
"v_pk_mul_f32 v[106:107], v[106:107], v[232:233] op_sel:[0,1] op_sel_hi:[1,1]\n"
"v_cvt_pk_bf16_f32 v84, v84, v85\n"
"v_cvt_pk_bf16_f32 v85, v86, v87\n"
"v_cvt_pk_bf16_f32 v86, v104, v105\n"
"v_cvt_pk_bf16_f32 v87, v106, v107\n"
"v_pk_mul_f32 v[124:125], v[124:125], v[232:233] op_sel:[0,1] op_sel_hi:[1,1]\n"
"s_add_u32 m0, s79, 896\n"
"s_nop 0\n"
"buffer_load_dwordx4 v247, s[40:43], s16 offen offset:128 lds\n"
"v_accvgpr_write_b32 a220, v84\n"
"v_accvgpr_write_b32 a221, v85\n"
"v_accvgpr_write_b32 a222, v86\n"
"v_accvgpr_write_b32 a223, v87\n"
"v_pk_mul_f32 v[126:127], v[126:127], v[232:233] op_sel:[0,1] op_sel_hi:[1,1]\n"
"v_pk_mul_f32 v[144:145], v[144:145], v[232:233] op_sel:[0,1] op_sel_hi:[1,1]\n"
"v_pk_mul_f32 v[146:147], v[146:147], v[232:233] op_sel:[0,1] op_sel_hi:[1,1]\n"
"v_cvt_pk_bf16_f32 v124, v124, v125\n"
"v_cvt_pk_bf16_f32 v125, v126, v127\n"
"v_cvt_pk_bf16_f32 v126, v144, v145\n"
"v_cvt_pk_bf16_f32 v127, v146, v147\n"
"v_accvgpr_read_b32 v234, a152\n"
"s_add_u32 m0, s79, 7936\n"
"s_nop 0\n"
"buffer_load_dwordx4 v247, s[40:43], s16 offen offset:256 lds\n"
"v_accvgpr_write_b32 a240, v124\n"
"v_accvgpr_write_b32 a241, v125\n"
"v_accvgpr_write_b32 a242, v126\n"
"v_accvgpr_write_b32 a243, v127\n"
"s_nop 0\n"
"v_rcp_f32 v234, v234\n"
"s_nop 0\n"
"v_pk_mul_f32 v[8:9], v[8:9], v[234:235] op_sel:[0,0] op_sel_hi:[1,0]\n"
"v_pk_mul_f32 v[10:11], v[10:11], v[234:235] op_sel:[0,0] op_sel_hi:[1,0]\n"
"v_pk_mul_f32 v[28:29], v[28:29], v[234:235] op_sel:[0,0] op_sel_hi:[1,0]\n"
"v_pk_mul_f32 v[30:31], v[30:31], v[234:235] op_sel:[0,0] op_sel_hi:[1,0]\n"
"v_cvt_pk_bf16_f32 v8, v8, v9\n"
"s_add_u32 m0, s79, 8832\n"
"s_nop 0\n"
"buffer_load_dwordx4 v247, s[40:43], s16 offen offset:384 lds\n"
"s_add_u32 s16, s16, 262144\n"
"s_cmp_eq_u32 s16, 5242880\n"
"s_cselect_b32 s16, 0, s16\n"
"v_cvt_pk_bf16_f32 v9, v10, v11\n"
"v_cvt_pk_bf16_f32 v10, v28, v29\n"
"v_cvt_pk_bf16_f32 v11, v30, v31\n"
"v_pk_mul_f32 v[48:49], v[48:49], v[234:235] op_sel:[0,0] op_sel_hi:[1,0]\n"
"v_accvgpr_write_b32 a184, v8\n"
"v_accvgpr_write_b32 a185, v9\n"
"v_accvgpr_write_b32 a186, v10\n"
"v_accvgpr_write_b32 a187, v11\n"
"v_pk_mul_f32 v[50:51], v[50:51], v[234:235] op_sel:[0,0] op_sel_hi:[1,0]\n"
"v_pk_mul_f32 v[68:69], v[68:69], v[234:235] op_sel:[0,0] op_sel_hi:[1,0]\n"
"v_pk_mul_f32 v[70:71], v[70:71], v[234:235] op_sel:[0,0] op_sel_hi:[1,0]\n"
"v_cvt_pk_bf16_f32 v48, v48, v49\n"
"v_cvt_pk_bf16_f32 v49, v50, v51\n"
"v_cvt_pk_bf16_f32 v50, v68, v69\n"
"v_cvt_pk_bf16_f32 v51, v70, v71\n"
"v_pk_mul_f32 v[88:89], v[88:89], v[234:235] op_sel:[0,0] op_sel_hi:[1,0]\n"
"v_accvgpr_write_b32 a204, v48\n"
"v_accvgpr_write_b32 a205, v49\n"
"v_accvgpr_write_b32 a206, v50\n"
"v_accvgpr_write_b32 a207, v51\n"
"v_pk_mul_f32 v[90:91], v[90:91], v[234:235] op_sel:[0,0] op_sel_hi:[1,0]\n"
"v_pk_mul_f32 v[108:109], v[108:109], v[234:235] op_sel:[0,0] op_sel_hi:[1,0]\n"
"v_pk_mul_f32 v[110:111], v[110:111], v[234:235] op_sel:[0,0] op_sel_hi:[1,0]\n"
"v_cvt_pk_bf16_f32 v88, v88, v89\n"
"v_cvt_pk_bf16_f32 v89, v90, v91\n"
"v_cvt_pk_bf16_f32 v90, v108, v109\n"
"v_cvt_pk_bf16_f32 v91, v110, v111\n"
"v_pk_mul_f32 v[128:129], v[128:129], v[234:235] op_sel:[0,0] op_sel_hi:[1,0]\n"
"v_accvgpr_write_b32 a224, v88\n"
"v_accvgpr_write_b32 a225, v89\n"
"v_accvgpr_write_b32 a226, v90\n"
"v_accvgpr_write_b32 a227, v91\n"
"v_pk_mul_f32 v[130:131], v[130:131], v[234:235] op_sel:[0,0] op_sel_hi:[1,0]\n"
"v_pk_mul_f32 v[148:149], v[148:149], v[234:235] op_sel:[0,0] op_sel_hi:[1,0]\n"
"v_pk_mul_f32 v[150:151], v[150:151], v[234:235] op_sel:[0,0] op_sel_hi:[1,0]\n"
"v_cvt_pk_bf16_f32 v128, v128, v129\n"
"v_cvt_pk_bf16_f32 v129, v130, v131\n"
"v_cvt_pk_bf16_f32 v130, v148, v149\n"
"v_cvt_pk_bf16_f32 v131, v150, v151\n"
"v_accvgpr_read_b32 v235, a156\n"
"v_accvgpr_write_b32 a244, v128\n"
"v_accvgpr_write_b32 a245, v129\n"
"v_accvgpr_write_b32 a246, v130\n"
"v_accvgpr_write_b32 a247, v131\n"
"s_nop 0\n"
"v_rcp_f32 v235, v235\n"
"s_nop 0\n"
"v_pk_mul_f32 v[12:13], v[12:13], v[234:235] op_sel:[0,1] op_sel_hi:[1,1]\n"
"v_pk_mul_f32 v[14:15], v[14:15], v[234:235] op_sel:[0,1] op_sel_hi:[1,1]\n"
"v_pk_mul_f32 v[32:33], v[32:33], v[234:235] op_sel:[0,1] op_sel_hi:[1,1]\n"
"v_pk_mul_f32 v[34:35], v[34:35], v[234:235] op_sel:[0,1] op_sel_hi:[1,1]\n"
"v_cvt_pk_bf16_f32 v12, v12, v13\n"
"v_cvt_pk_bf16_f32 v13, v14, v15\n"
"v_cvt_pk_bf16_f32 v14, v32, v33\n"
"v_cvt_pk_bf16_f32 v15, v34, v35\n"
"v_pk_mul_f32 v[52:53], v[52:53], v[234:235] op_sel:[0,1] op_sel_hi:[1,1]\n"
"v_accvgpr_write_b32 a188, v12\n"
"v_accvgpr_write_b32 a189, v13\n"
"v_accvgpr_write_b32 a190, v14\n"
"v_accvgpr_write_b32 a191, v15\n"
"v_pk_mul_f32 v[54:55], v[54:55], v[234:235] op_sel:[0,1] op_sel_hi:[1,1]\n"
"v_pk_mul_f32 v[72:73], v[72:73], v[234:235] op_sel:[0,1] op_sel_hi:[1,1]\n"
"v_pk_mul_f32 v[74:75], v[74:75], v[234:235] op_sel:[0,1] op_sel_hi:[1,1]\n"
"v_cvt_pk_bf16_f32 v52, v52, v53\n"
"v_cvt_pk_bf16_f32 v53, v54, v55\n"
"v_cvt_pk_bf16_f32 v54, v72, v73\n"
"v_cvt_pk_bf16_f32 v55, v74, v75\n"
"v_pk_mul_f32 v[92:93], v[92:93], v[234:235] op_sel:[0,1] op_sel_hi:[1,1]\n"
"v_accvgpr_write_b32 a208, v52\n"
"v_accvgpr_write_b32 a209, v53\n"
"v_accvgpr_write_b32 a210, v54\n"
"v_accvgpr_write_b32 a211, v55\n"
"v_pk_mul_f32 v[94:95], v[94:95], v[234:235] op_sel:[0,1] op_sel_hi:[1,1]\n"
"v_pk_mul_f32 v[112:113], v[112:113], v[234:235] op_sel:[0,1] op_sel_hi:[1,1]\n"
"v_pk_mul_f32 v[114:115], v[114:115], v[234:235] op_sel:[0,1] op_sel_hi:[1,1]\n"
"v_cvt_pk_bf16_f32 v92, v92, v93\n"
"v_cvt_pk_bf16_f32 v93, v94, v95\n"
"v_cvt_pk_bf16_f32 v94, v112, v113\n"
"v_cvt_pk_bf16_f32 v95, v114, v115\n"
"v_pk_mul_f32 v[132:133], v[132:133], v[234:235] op_sel:[0,1] op_sel_hi:[1,1]\n"
"v_accvgpr_write_b32 a228, v92\n"
"v_accvgpr_write_b32 a229, v93\n"
"v_accvgpr_write_b32 a230, v94\n"
"v_accvgpr_write_b32 a231, v95\n"
"v_pk_mul_f32 v[134:135], v[134:135], v[234:235] op_sel:[0,1] op_sel_hi:[1,1]\n"
"v_pk_mul_f32 v[152:153], v[152:153], v[234:235] op_sel:[0,1] op_sel_hi:[1,1]\n"
"v_pk_mul_f32 v[154:155], v[154:155], v[234:235] op_sel:[0,1] op_sel_hi:[1,1]\n"
"v_cvt_pk_bf16_f32 v132, v132, v133\n"
"v_cvt_pk_bf16_f32 v133, v134, v135\n"
"v_cvt_pk_bf16_f32 v134, v152, v153\n"
"v_cvt_pk_bf16_f32 v135, v154, v155\n"
"v_accvgpr_read_b32 v236, a160\n"
"v_accvgpr_write_b32 a248, v132\n"
"v_accvgpr_write_b32 a249, v133\n"
"v_accvgpr_write_b32 a250, v134\n"
"v_accvgpr_write_b32 a251, v135\n"
"s_nop 0\n"
"v_rcp_f32 v236, v236\n"
"s_nop 0\n"
"v_pk_mul_f32 v[16:17], v[16:17], v[236:237] op_sel:[0,0] op_sel_hi:[1,0]\n"
"v_pk_mul_f32 v[18:19], v[18:19], v[236:237] op_sel:[0,0] op_sel_hi:[1,0]\n"
"v_pk_mul_f32 v[36:37], v[36:37], v[236:237] op_sel:[0,0] op_sel_hi:[1,0]\n"
"v_pk_mul_f32 v[38:39], v[38:39], v[236:237] op_sel:[0,0] op_sel_hi:[1,0]\n"
"v_cvt_pk_bf16_f32 v16, v16, v17\n"
"v_cvt_pk_bf16_f32 v17, v18, v19\n"
"v_cvt_pk_bf16_f32 v18, v36, v37\n"
"v_cvt_pk_bf16_f32 v19, v38, v39\n"
"v_pk_mul_f32 v[56:57], v[56:57], v[236:237] op_sel:[0,0] op_sel_hi:[1,0]\n"
"v_accvgpr_write_b32 a192, v16\n"
"v_accvgpr_write_b32 a193, v17\n"
"v_accvgpr_write_b32 a194, v18\n"
"v_accvgpr_write_b32 a195, v19\n"
"v_pk_mul_f32 v[58:59], v[58:59], v[236:237] op_sel:[0,0] op_sel_hi:[1,0]\n"
"v_pk_mul_f32 v[76:77], v[76:77], v[236:237] op_sel:[0,0] op_sel_hi:[1,0]\n"
"v_pk_mul_f32 v[78:79], v[78:79], v[236:237] op_sel:[0,0] op_sel_hi:[1,0]\n"
"v_cvt_pk_bf16_f32 v56, v56, v57\n"
"v_cvt_pk_bf16_f32 v57, v58, v59\n"
"v_cvt_pk_bf16_f32 v58, v76, v77\n"
"v_cvt_pk_bf16_f32 v59, v78, v79\n"
"v_pk_mul_f32 v[96:97], v[96:97], v[236:237] op_sel:[0,0] op_sel_hi:[1,0]\n"
"v_accvgpr_write_b32 a212, v56\n"
"v_accvgpr_write_b32 a213, v57\n"
"v_accvgpr_write_b32 a214, v58\n"
"v_accvgpr_write_b32 a215, v59\n"
"v_pk_mul_f32 v[98:99], v[98:99], v[236:237] op_sel:[0,0] op_sel_hi:[1,0]\n"
"v_pk_mul_f32 v[116:117], v[116:117], v[236:237] op_sel:[0,0] op_sel_hi:[1,0]\n"
"v_pk_mul_f32 v[118:119], v[118:119], v[236:237] op_sel:[0,0] op_sel_hi:[1,0]\n"
"v_cvt_pk_bf16_f32 v96, v96, v97\n"
"v_cvt_pk_bf16_f32 v97, v98, v99\n"
"v_cvt_pk_bf16_f32 v98, v116, v117\n"
"v_cvt_pk_bf16_f32 v99, v118, v119\n"
"v_pk_mul_f32 v[136:137], v[136:137], v[236:237] op_sel:[0,0] op_sel_hi:[1,0]\n"
"v_accvgpr_write_b32 a232, v96\n"
"v_accvgpr_write_b32 a233, v97\n"
"v_accvgpr_write_b32 a234, v98\n"
"v_accvgpr_write_b32 a235, v99\n"
"v_pk_mul_f32 v[138:139], v[138:139], v[236:237] op_sel:[0,0] op_sel_hi:[1,0]\n"
"v_pk_mul_f32 v[156:157], v[156:157], v[236:237] op_sel:[0,0] op_sel_hi:[1,0]\n"
"v_pk_mul_f32 v[158:159], v[158:159], v[236:237] op_sel:[0,0] op_sel_hi:[1,0]\n"
"v_cvt_pk_bf16_f32 v136, v136, v137\n"
"v_cvt_pk_bf16_f32 v137, v138, v139\n"
"v_cvt_pk_bf16_f32 v138, v156, v157\n"
"v_cvt_pk_bf16_f32 v139, v158, v159\n"
"v_accvgpr_write_b32 a252, v136\n"
"v_accvgpr_write_b32 a253, v137\n"
"v_accvgpr_write_b32 a254, v138\n"
"v_accvgpr_write_b32 a255, v139\n"
"s_mov_b32 s74, 1\n"
"s_xor_b32 s84, s84, 262144\n"
"s_xor_b32 s87, s87, 1\n"
"s_mov_b32 s86, 99\n"
"s_branch L_q%=\n"
"L_p4%=:\n"
"s_barrier\n"
"s_xor_b32 s83, s87, 1\n"
"s_mul_i32 s78, s83, 327680\n"
"s_mul_i32 s16, s83, -655360\n"
"s_add_u32 s16, s16, 327680\n"
"s_sub_u32 s19, 128, s16\n"
"s_sub_u32 s20, -1152, s16\n"
"s_bfe_u32 s18, %[info], 0x3000b\n"
"s_lshl_b32 s83, s18, 7\n"
"s_add_u32 s78, s78, s83\n"
"v_accvgpr_read_b32 v244, a174\n"
"s_nop 0\n"
"s_add_u32 s83, s78, 163840\n"
"v_add_u32 v243, s78, v244\n"
"v_add_u32 v244, s83, v244\n"
"s_bfe_u32 s25, %[info], 0x30003\n"
"s_mul_i32 s25, s25, 204800\n"
"s_mul_i32 s78, s73, 51200\n"
"s_add_u32 s25, s25, s78\n"
"s_mov_b32 s21, s18\n"
"s_lshl_b32 s78, s73, 14\n"
"s_add_u32 s78, s78, 65536\n"
"s_cmp_eq_u32 s73, 3\n"
"s_cselect_b32 s78, 147456, s78\n"
"v_add_u32 v252, s78, v251\n"
"s_lshl_b32 s78, s73, 12\n"
"s_add_u32 s78, s78, 114688\n"
"v_add_u32 v247, s78, v251\n"
"s_xor_b32 s83, s73, 2\n"
"s_lshl_b32 s78, s83, 14\n"
"s_add_u32 s78, s78, 65536\n"
"s_cmp_eq_u32 s83, 3\n"
"s_cselect_b32 s78, 147456, s78\n"
"v_add_u32 v253, s78, v251\n"
"s_lshl_b32 s78, s83, 12\n"
"s_add_u32 s78, s78, 114688\n"
"v_add_u32 v248, s78, v251\n"
"v_accvgpr_read_b32 v254, a169\n"
"s_nop 0\n"
"v_and_b32 v255, 511, v254\n"
"v_lshrrev_b32 v254, 11, v254\n"
"v_and_b32 v249, 1, v254\n"
"v_lshl_add_u32 v249, v249, 10, v255\n"
"v_lshrrev_b32 v254, 2, v254\n"
"v_mul_u32_u24 v254, 16896, v254\n"
"v_add_u32 v249, v249, v254\n"
"s_cmp_eq_u32 s73, 0\n"
"s_mul_i32 s64, s73, 20480\n"
"s_add_u32 s64, s64, 77824\n"
"s_cmp_eq_u32 s73, 0\n"
"s_cselect_b32 s64, 0, s64\n"
"s_xor_b32 s83, s73, 2\n"
"s_mul_i32 s65, s83, 20480\n"
"s_add_u32 s65, s65, 77824\n"
"s_cmp_eq_u32 s83, 0\n"
"s_cselect_b32 s65, 0, s65\n"
"v_mfma_f32_16x16x32_bf16 v[160:163], a[80:83], a[0:3], v[220:223]\n"
"ds_read_b64_tr_b16 a[112:113], v241 offset:0\n"
"s_add_u32 m0, s81, 0\n"
"v_mfma_f32_16x16x32_bf16 v[160:163], a[84:87], a[20:23], v[160:163]\n"
"v_exp_f32 v194, v194\n"
"v_mfma_f32_16x16x32_bf16 v[160:163], a[88:91], a[40:43], v[160:163]\n"
"ds_read_b64_tr_b16 a[114:115], v241 offset:4096\n"
"v_cvt_pk_bf16_f32 v214, v188, v189\n"
"v_mfma_f32_16x16x32_bf16 v[160:163], a[92:95], a[60:63], v[160:163]\n"
"ds_write_b128 v252, a[176:179] offset:0\n"
"v_exp_f32 v195, v195\n"
"v_mfma_f32_16x16x32_bf16 v[164:167], a[96:99], a[0:3], v[220:223]\n"
"ds_read_b64_tr_b16 a[116:117], v242 offset:0\n"
"v_cvt_pk_bf16_f32 v215, v190, v191\n"
"v_mfma_f32_16x16x32_bf16 v[164:167], a[100:103], a[20:23], v[164:167]\n"
"v_exp_f32 v196, v196\n"
"v_mfma_f32_16x16x32_bf16 v[164:167], a[104:107], a[40:43], v[164:167]\n"
"ds_read_b64_tr_b16 a[118:119], v242 offset:4096\n"
"v_cvt_pk_bf16_f32 v216, v192, v193\n"
"v_mfma_f32_16x16x32_bf16 v[164:167], a[108:111], a[60:63], v[164:167]\n"
"v_exp_f32 v197, v197\n"
"v_mfma_f32_16x16x32_bf16 v[168:171], a[80:83], a[4:7], v[224:227]\n"
"ds_read_b64_tr_b16 a[120:121], v241 offset:512\n"
"v_cvt_pk_bf16_f32 v217, v194, v195\n"
"v_mfma_f32_16x16x32_bf16 v[168:171], a[84:87], a[24:27], v[168:171]\n"
"ds_write_b128 v252, a[180:183] offset:1024\n"
"v_exp_f32 v198, v198\n"
"v_mfma_f32_16x16x32_bf16 v[168:171], a[88:91], a[44:47], v[168:171]\n"
"ds_read_b64_tr_b16 a[122:123], v241 offset:4608\n"
"v_cvt_pk_bf16_f32 v218, v196, v197\n"
"v_mfma_f32_16x16x32_bf16 v[168:171], a[92:95], a[64:67], v[168:171]\n"
"v_exp_f32 v199, v199\n"
"v_mfma_f32_16x16x32_bf16 v[172:175], a[96:99], a[4:7], v[224:227]\n"
"ds_read_b64_tr_b16 a[124:125], v242 offset:512\n"
"v_cvt_pk_bf16_f32 v219, v198, v199\n"
"v_mfma_f32_16x16x32_bf16 v[172:175], a[100:103], a[24:27], v[172:175]\n"
"buffer_load_dwordx4 v243, s[52:55], 0 offen lds\n"
"v_mfma_f32_16x16x32_bf16 v[172:175], a[104:107], a[44:47], v[172:175]\n"
"ds_read_b64_tr_b16 a[126:127], v242 offset:4608\n"
"s_add_u32 m0, s81, 4096\n"
"v_mfma_f32_16x16x32_bf16 v[172:175], a[108:111], a[64:67], v[172:175]\n"
"ds_read_b64_tr_b16 a[128:129], v241 offset:1024\n"
"v_mfma_f32_16x16x32_bf16 v[176:179], a[80:83], a[8:11], v[228:231]\n"
"ds_write_b128 v252, a[184:187] offset:2048\n"
"v_exp_f32 v160, v160\n"
"v_mfma_f32_16x16x32_bf16 v[176:179], a[84:87], a[28:31], v[176:179]\n"
"ds_read_b64_tr_b16 a[130:131], v241 offset:5120\n"
"v_mfma_f32_16x16x32_bf16 v[176:179], a[88:91], a[48:51], v[176:179]\n"
"buffer_load_dwordx4 v243, s[52:55], s69 offen lds\n"
"v_mfma_f32_16x16x32_bf16 v[176:179], a[92:95], a[68:71], v[176:179]\n"
"ds_read_b64_tr_b16 a[132:133], v242 offset:1024\n"
"s_add_u32 m0, s81, 8192\n"
"v_mfma_f32_16x16x32_bf16 v[180:183], a[96:99], a[8:11], v[228:231]\n"
"ds_write_b128 v252, a[188:191] offset:3072\n"
"v_exp_f32 v161, v161\n"
"v_mfma_f32_16x16x32_bf16 v[180:183], a[100:103], a[28:31], v[180:183]\n"
"ds_read_b64_tr_b16 a[134:135], v242 offset:5120\n"
"v_mfma_f32_16x16x32_bf16 v[180:183], a[104:107], a[48:51], v[180:183]\n"
"v_exp_f32 v162, v162\n"
"v_mfma_f32_16x16x32_bf16 v[180:183], a[108:111], a[68:71], v[180:183]\n"
"buffer_load_dwordx4 v243, s[52:55], s70 offen lds\n"
"v_mfma_f32_16x16x32_bf16 v[184:187], a[80:83], a[12:15], v[232:235]\n"
"ds_read_b64_tr_b16 a[136:137], v241 offset:1536\n"
"s_add_u32 m0, s81, 12288\n"
"v_mfma_f32_16x16x32_bf16 v[184:187], a[84:87], a[32:35], v[184:187]\n"
"ds_write_b128 v252, a[192:195] offset:4096\n"
"v_exp_f32 v163, v163\n"
"v_mfma_f32_16x16x32_bf16 v[184:187], a[88:91], a[52:55], v[184:187]\n"
"ds_read_b64_tr_b16 a[138:139], v241 offset:5632\n"
"v_mfma_f32_16x16x32_bf16 v[184:187], a[92:95], a[72:75], v[184:187]\n"
"v_exp_f32 v164, v164\n"
"v_mfma_f32_16x16x32_bf16 v[188:191], a[96:99], a[12:15], v[232:235]\n"
"buffer_load_dwordx4 v243, s[52:55], s71 offen lds\n"
"v_mfma_f32_16x16x32_bf16 v[188:191], a[100:103], a[32:35], v[188:191]\n"
"ds_read_b64_tr_b16 a[140:141], v242 offset:1536\n"
"s_add_u32 m0, s81, 16384\n"
"v_mfma_f32_16x16x32_bf16 v[188:191], a[104:107], a[52:55], v[188:191]\n"
"ds_write_b128 v252, a[196:199] offset:5120\n"
"v_exp_f32 v165, v165\n"
"v_mfma_f32_16x16x32_bf16 v[188:191], a[108:111], a[72:75], v[188:191]\n"
"ds_read_b64_tr_b16 a[142:143], v242 offset:5632\n"
"v_mfma_f32_16x16x32_bf16 v[192:195], a[80:83], a[16:19], v[236:239]\n"
"v_exp_f32 v166, v166\n"
"v_mfma_f32_16x16x32_bf16 v[192:195], a[84:87], a[36:39], v[192:195]\n"
"ds_write_b128 v252, a[200:203] offset:6144\n"
"v_exp_f32 v167, v167\n"
"v_mfma_f32_16x16x32_bf16 v[192:195], a[88:91], a[56:59], v[192:195]\n"
"v_exp_f32 v168, v168\n"
"v_mfma_f32_16x16x32_bf16 v[192:195], a[92:95], a[76:79], v[192:195]\n"
"buffer_load_dwordx4 v244, s[52:55], 0 offen lds\n"
"v_mfma_f32_16x16x32_bf16 v[196:199], a[96:99], a[16:19], v[236:239]\n"
"v_exp_f32 v169, v169\n"
"s_add_u32 m0, s81, 20480\n"
"v_mfma_f32_16x16x32_bf16 v[196:199], a[100:103], a[36:39], v[196:199]\n"
"buffer_load_dwordx4 v244, s[52:55], s69 offen lds\n"
"v_mfma_f32_16x16x32_bf16 v[196:199], a[104:107], a[56:59], v[196:199]\n"
"ds_write_b128 v252, a[204:207] offset:7168\n"
"v_exp_f32 v170, v170\n"
"v_mfma_f32_16x16x32_bf16 v[196:199], a[108:111], a[76:79], v[196:199]\n"
"s_waitcnt vmcnt(6) lgkmcnt(0)\n"
"s_barrier\n"
"v_mfma_f32_16x16x32_bf16 v[0:3], a[112:115], v[200:203], v[0:3]\n"
"ds_read_b64_tr_b16 a[80:81], v241 offset:16384\n"
"s_add_u32 m0, s81, 24576\n"
"v_mfma_f32_16x16x32_bf16 v[20:23], a[116:119], v[200:203], v[20:23]\n"
"v_exp_f32 v171, v171\n"
"v_mfma_f32_16x16x32_bf16 v[40:43], a[120:123], v[200:203], v[40:43]\n"
"buffer_load_dwordx4 v244, s[52:55], s70 offen lds\n"
"v_mfma_f32_16x16x32_bf16 v[60:63], a[124:127], v[200:203], v[60:63]\n"
"ds_write_b128 v252, a[208:211] offset:8192\n"
"v_exp_f32 v172, v172\n"
"v_mfma_f32_16x16x32_bf16 v[80:83], a[128:131], v[200:203], v[80:83]\n"
"ds_read_b64_tr_b16 a[82:83], v241 offset:20480\n"
"s_add_u32 m0, s81, 28672\n"
"v_mfma_f32_16x16x32_bf16 v[100:103], a[132:135], v[200:203], v[100:103]\n"
"v_exp_f32 v173, v173\n"
"v_mfma_f32_16x16x32_bf16 v[120:123], a[136:139], v[200:203], v[120:123]\n"
"v_exp_f32 v174, v174\n"
"v_mfma_f32_16x16x32_bf16 v[140:143], a[140:143], v[200:203], v[140:143]\n"
"v_exp_f32 v175, v175\n"
"v_mfma_f32_16x16x32_bf16 a[144:147], a[164:167], v[200:203], a[144:147]\n"
"buffer_load_dwordx4 v244, s[52:55], s71 offen lds\n"
"v_mfma_f32_16x16x32_bf16 v[4:7], a[112:115], v[204:207], v[4:7]\n"
"ds_read_b64_tr_b16 a[84:85], v242 offset:16384\n"
"v_add_u32 v243, s16, v243\n"
"v_mfma_f32_16x16x32_bf16 v[24:27], a[116:119], v[204:207], v[24:27]\n"
"ds_read_b64_tr_b16 a[86:87], v242 offset:20480\n"
"v_cvt_pk_bf16_f32 v200, v160, v161\n"
"v_cvt_pk_bf16_f32 v201, v162, v163\n"
"v_mfma_f32_16x16x32_bf16 v[44:47], a[120:123], v[204:207], v[44:47]\n"
"ds_write_b128 v252, a[212:215] offset:9216\n"
"v_exp_f32 v176, v176\n"
"v_mfma_f32_16x16x32_bf16 v[64:67], a[124:127], v[204:207], v[64:67]\n"
"v_exp_f32 v177, v177\n"
"v_mfma_f32_16x16x32_bf16 v[84:87], a[128:131], v[204:207], v[84:87]\n"
"ds_read_b64_tr_b16 a[88:89], v241 offset:16896\n"
"v_cvt_pk_bf16_f32 v202, v164, v165\n"
"v_mfma_f32_16x16x32_bf16 v[104:107], a[132:135], v[204:207], v[104:107]\n"
"ds_write_b128 v252, a[216:219] offset:10240\n"
"v_add_u32 v244, s16, v244\n"
"v_mfma_f32_16x16x32_bf16 v[124:127], a[136:139], v[204:207], v[124:127]\n"
"ds_read_b64_tr_b16 a[90:91], v241 offset:20992\n"
"v_cvt_pk_bf16_f32 v203, v166, v167\n"
"v_mfma_f32_16x16x32_bf16 v[144:147], a[140:143], v[204:207], v[144:147]\n"
"v_exp_f32 v178, v178\n"
"v_mfma_f32_16x16x32_bf16 a[148:151], a[164:167], v[204:207], a[148:151]\n"
"ds_read_b64_tr_b16 a[92:93], v242 offset:16896\n"
"s_add_u32 m0, s81, 32768\n"
"v_mfma_f32_16x16x32_bf16 v[8:11], a[112:115], v[208:211], v[8:11]\n"
"ds_write_b128 v252, a[220:223] offset:11264\n"
"v_exp_f32 v179, v179\n"
"v_mfma_f32_16x16x32_bf16 v[28:31], a[116:119], v[208:211], v[28:31]\n"
"ds_read_b64_tr_b16 a[94:95], v242 offset:20992\n"
"v_cvt_pk_bf16_f32 v204, v168, v169\n"
"v_cvt_pk_bf16_f32 v205, v170, v171\n"
"v_mfma_f32_16x16x32_bf16 v[48:51], a[120:123], v[208:211], v[48:51]\n"
"buffer_load_dwordx4 v243, s[52:55], 0 offen lds\n"
"v_mfma_f32_16x16x32_bf16 v[68:71], a[124:127], v[208:211], v[68:71]\n"
"ds_read_b64_tr_b16 a[96:97], v241 offset:17408\n"
"v_cvt_pk_bf16_f32 v206, v172, v173\n"
"v_mfma_f32_16x16x32_bf16 v[88:91], a[128:131], v[208:211], v[88:91]\n"
"v_exp_f32 v180, v180\n"
"v_mfma_f32_16x16x32_bf16 v[108:111], a[132:135], v[208:211], v[108:111]\n"
"ds_read_b64_tr_b16 a[98:99], v241 offset:21504\n"
"v_cvt_pk_bf16_f32 v207, v174, v175\n"
"v_mfma_f32_16x16x32_bf16 v[128:131], a[136:139], v[208:211], v[128:131]\n"
"ds_write_b128 v252, a[224:227] offset:12288\n"
"v_exp_f32 v181, v181\n"
"v_mfma_f32_16x16x32_bf16 v[148:151], a[140:143], v[208:211], v[148:151]\n"
"ds_read_b64_tr_b16 a[100:101], v242 offset:17408\n"
"s_add_u32 m0, s81, 36864\n"
"v_mfma_f32_16x16x32_bf16 a[152:155], a[164:167], v[208:211], a[152:155]\n"
"v_exp_f32 v182, v182\n"
"v_mfma_f32_16x16x32_bf16 v[12:15], a[112:115], v[212:215], v[12:15]\n"
"ds_read_b64_tr_b16 a[102:103], v242 offset:21504\n"
"v_cvt_pk_bf16_f32 v208, v176, v177\n"
"v_cvt_pk_bf16_f32 v209, v178, v179\n"
"v_mfma_f32_16x16x32_bf16 v[32:35], a[116:119], v[212:215], v[32:35]\n"
"ds_write_b128 v252, a[228:231] offset:13312\n"
"v_exp_f32 v183, v183\n"
"v_mfma_f32_16x16x32_bf16 v[52:55], a[120:123], v[212:215], v[52:55]\n"
"ds_read_b64_tr_b16 a[104:105], v241 offset:17920\n"
"v_cvt_pk_bf16_f32 v210, v180, v181\n"
"v_mfma_f32_16x16x32_bf16 v[72:75], a[124:127], v[212:215], v[72:75]\n"
"v_exp_f32 v184, v184\n"
"v_mfma_f32_16x16x32_bf16 v[92:95], a[128:131], v[212:215], v[92:95]\n"
"ds_read_b64_tr_b16 a[106:107], v241 offset:22016\n"
"v_cvt_pk_bf16_f32 v211, v182, v183\n"
"v_mfma_f32_16x16x32_bf16 v[112:115], a[132:135], v[212:215], v[112:115]\n"
"v_exp_f32 v185, v185\n"
"v_mfma_f32_16x16x32_bf16 v[132:135], a[136:139], v[212:215], v[132:135]\n"
"ds_write_b128 v252, a[232:235] offset:14336\n"
"v_exp_f32 v186, v186\n"
"v_mfma_f32_16x16x32_bf16 v[152:155], a[140:143], v[212:215], v[152:155]\n"
"v_exp_f32 v187, v187\n"
"v_mfma_f32_16x16x32_bf16 a[156:159], a[164:167], v[212:215], a[156:159]\n"
"ds_read_b64_tr_b16 a[108:109], v242 offset:17920\n"
"v_mfma_f32_16x16x32_bf16 v[16:19], a[112:115], v[216:219], v[16:19]\n"
"v_exp_f32 v188, v188\n"
"v_mfma_f32_16x16x32_bf16 v[36:39], a[116:119], v[216:219], v[36:39]\n"
"v_exp_f32 v189, v189\n"
"v_mfma_f32_16x16x32_bf16 v[56:59], a[120:123], v[216:219], v[56:59]\n"
"ds_write_b128 v252, a[236:239] offset:15360\n"
"v_exp_f32 v190, v190\n"
"v_mfma_f32_16x16x32_bf16 v[76:79], a[124:127], v[216:219], v[76:79]\n"
"ds_read_b64_tr_b16 a[110:111], v242 offset:22016\n"
"v_cvt_pk_bf16_f32 v212, v184, v185\n"
"v_cvt_pk_bf16_f32 v213, v186, v187\n"
"v_mfma_f32_16x16x32_bf16 v[96:99], a[128:131], v[216:219], v[96:99]\n"
"v_exp_f32 v191, v191\n"
"v_mfma_f32_16x16x32_bf16 v[116:119], a[132:135], v[216:219], v[116:119]\n"
"v_exp_f32 v192, v192\n"
"v_mfma_f32_16x16x32_bf16 v[136:139], a[136:139], v[216:219], v[136:139]\n"
"buffer_load_dwordx4 v243, s[52:55], s69 offen lds\n"
"v_mfma_f32_16x16x32_bf16 v[156:159], a[140:143], v[216:219], v[156:159]\n"
"v_exp_f32 v193, v193\n"
"v_mfma_f32_16x16x32_bf16 a[160:163], a[164:167], v[216:219], a[160:163]\n"
"s_waitcnt lgkmcnt(0)\n"
"s_barrier\n"
"v_mfma_f32_16x16x32_bf16 v[0:3], a[80:83], v[200:203], v[0:3]\n"
"v_exp_f32 v194, v194\n"
"v_mfma_f32_16x16x32_bf16 v[20:23], a[84:87], v[200:203], v[20:23]\n"
"ds_write_b128 v247, a[240:243] offset:0\n"
"v_cvt_pk_bf16_f32 v214, v188, v189\n"
"v_cvt_pk_bf16_f32 v215, v190, v191\n"
"s_add_u32 m0, s81, 40960\n"
"v_mfma_f32_16x16x32_bf16 v[40:43], a[88:91], v[200:203], v[40:43]\n"
"v_exp_f32 v195, v195\n"
"v_mfma_f32_16x16x32_bf16 v[60:63], a[92:95], v[200:203], v[60:63]\n"
"v_cvt_pk_bf16_f32 v216, v192, v193\n"
"v_cvt_pk_bf16_f32 v217, v194, v195\n"
"v_mfma_f32_16x16x32_bf16 v[80:83], a[96:99], v[200:203], v[80:83]\n"
"v_exp_f32 v196, v196\n"
"v_mfma_f32_16x16x32_bf16 v[100:103], a[100:103], v[200:203], v[100:103]\n"
"buffer_load_dwordx4 v243, s[52:55], s70 offen lds\n"
"v_mfma_f32_16x16x32_bf16 v[120:123], a[104:107], v[200:203], v[120:123]\n"
"ds_write_b128 v247, a[244:247] offset:1024\n"
"v_exp_f32 v197, v197\n"
"v_mfma_f32_16x16x32_bf16 v[140:143], a[108:111], v[200:203], v[140:143]\n"
"v_cvt_pk_bf16_f32 v218, v196, v197\n"
"s_add_u32 m0, s81, 45056\n"
"v_mfma_f32_16x16x32_bf16 a[144:147], a[164:167], v[200:203], a[144:147]\n"
"v_exp_f32 v198, v198\n"
"v_mfma_f32_16x16x32_bf16 v[4:7], a[80:83], v[204:207], v[4:7]\n"
"buffer_load_dwordx4 v243, s[52:55], s71 offen lds\n"
"v_mfma_f32_16x16x32_bf16 v[24:27], a[84:87], v[204:207], v[24:27]\n"
"ds_write_b128 v247, a[248:251] offset:2048\n"
"v_exp_f32 v199, v199\n"
"v_mfma_f32_16x16x32_bf16 v[44:47], a[88:91], v[204:207], v[44:47]\n"
"v_cvt_pk_bf16_f32 v219, v198, v199\n"
"s_add_u32 m0, s81, 49152\n"
"v_mfma_f32_16x16x32_bf16 v[64:67], a[92:95], v[204:207], v[64:67]\n"
"buffer_load_dwordx4 v244, s[52:55], 0 offen lds\n"
"v_mfma_f32_16x16x32_bf16 v[84:87], a[96:99], v[204:207], v[84:87]\n"
"s_add_u32 m0, s81, 53248\n"
"v_mfma_f32_16x16x32_bf16 v[104:107], a[100:103], v[204:207], v[104:107]\n"
"ds_write_b128 v247, a[252:255] offset:3072\n"
"v_accvgpr_read_b32 v232, a144\n"
"v_mfma_f32_16x16x32_bf16 v[124:127], a[104:107], v[204:207], v[124:127]\n"
"buffer_load_dwordx4 v244, s[52:55], s69 offen lds\n"
"v_mfma_f32_16x16x32_bf16 v[144:147], a[108:111], v[204:207], v[144:147]\n"
"s_add_u32 m0, s81, 57344\n"
"s_nop 0\n"
"v_mfma_f32_16x16x32_bf16 a[148:151], a[164:167], v[204:207], a[148:151]\n"
"v_rcp_f32 v232, v232\n"
"v_mfma_f32_16x16x32_bf16 v[8:11], a[80:83], v[208:211], v[8:11]\n"
"buffer_load_dwordx4 v244, s[52:55], s70 offen lds\n"
"v_mfma_f32_16x16x32_bf16 v[28:31], a[84:87], v[208:211], v[28:31]\n"
"s_add_u32 m0, s81, 61440\n"
"s_nop 0\n"
"v_mfma_f32_16x16x32_bf16 v[48:51], a[88:91], v[208:211], v[48:51]\n"
"v_mul_f32 v0, v232, v0\n"
"v_mul_f32 v1, v232, v1\n"
"v_mfma_f32_16x16x32_bf16 v[68:71], a[92:95], v[208:211], v[68:71]\n"
"buffer_load_dwordx4 v244, s[52:55], s71 offen lds\n"
"v_mfma_f32_16x16x32_bf16 v[88:91], a[96:99], v[208:211], v[88:91]\n"
"s_waitcnt vmcnt(8) lgkmcnt(0)\n"
"s_barrier\n"
"v_mfma_f32_16x16x32_bf16 v[108:111], a[100:103], v[208:211], v[108:111]\n"
"s_add_u32 s18, s18, 1\n"
"v_mov_b32 v242, v249\n"
"v_mfma_f32_16x16x32_bf16 v[128:131], a[104:107], v[208:211], v[128:131]\n"
"s_cmp_eq_u32 s18, 10\n"
"s_cselect_b32 s17, s20, s19\n"
"s_cselect_b32 s18, 0, s18\n"
"v_add_u32 v240, 0, v242\n"
"v_mfma_f32_16x16x32_bf16 v[148:151], a[108:111], v[208:211], v[148:151]\n"
"v_add_u32 v243, s17, v243\n"
"v_xor_b32 v241, 32, v240\n"
"v_mfma_f32_16x16x32_bf16 a[152:155], a[164:167], v[208:211], a[152:155]\n"
"ds_read_b64_tr_b16 v[160:161], v240 offset:0\n"
"v_add_u32 v244, s17, v244\n"
"v_mfma_f32_16x16x32_bf16 v[12:15], a[80:83], v[212:215], v[12:15]\n"
"ds_read_b64_tr_b16 v[162:163], v240 offset:2048\n"
"v_mfma_f32_16x16x32_bf16 v[32:35], a[84:87], v[212:215], v[32:35]\n"
"ds_read_b64_tr_b16 v[164:165], v240 offset:4096\n"
"v_mfma_f32_16x16x32_bf16 v[52:55], a[88:91], v[212:215], v[52:55]\n"
"ds_read_b64_tr_b16 v[166:167], v240 offset:6144\n"
"v_mfma_f32_16x16x32_bf16 v[72:75], a[92:95], v[212:215], v[72:75]\n"
"ds_read_b64_tr_b16 v[168:169], v240 offset:8192\n"
"v_mfma_f32_16x16x32_bf16 v[92:95], a[96:99], v[212:215], v[92:95]\n"
"ds_read_b64_tr_b16 v[170:171], v240 offset:10240\n"
"v_mfma_f32_16x16x32_bf16 v[112:115], a[100:103], v[212:215], v[112:115]\n"
"ds_read_b64_tr_b16 v[172:173], v240 offset:12288\n"
"v_mfma_f32_16x16x32_bf16 v[132:135], a[104:107], v[212:215], v[132:135]\n"
"ds_read_b64_tr_b16 v[174:175], v240 offset:14336\n"
"v_mfma_f32_16x16x32_bf16 v[152:155], a[108:111], v[212:215], v[152:155]\n"
"v_mul_f32 v2, v232, v2\n"
"v_mul_f32 v3, v232, v3\n"
"v_mfma_f32_16x16x32_bf16 a[156:159], a[164:167], v[212:215], a[156:159]\n"
"v_mul_f32 v20, v232, v20\n"
"v_mul_f32 v21, v232, v21\n"
"v_mfma_f32_16x16x32_bf16 v[16:19], a[80:83], v[216:219], v[16:19]\n"
"v_mul_f32 v22, v232, v22\n"
"v_mul_f32 v23, v232, v23\n"
"v_mfma_f32_16x16x32_bf16 v[36:39], a[84:87], v[216:219], v[36:39]\n"
"v_cvt_pk_bf16_f32 v0, v0, v1\n"
"v_cvt_pk_bf16_f32 v1, v2, v3\n"
"v_mfma_f32_16x16x32_bf16 v[56:59], a[88:91], v[216:219], v[56:59]\n"
"v_cvt_pk_bf16_f32 v2, v20, v21\n"
"v_cvt_pk_bf16_f32 v3, v22, v23\n"
"v_mfma_f32_16x16x32_bf16 v[76:79], a[92:95], v[216:219], v[76:79]\n"
"v_mul_f32 v40, v232, v40\n"
"v_mul_f32 v41, v232, v41\n"
"v_mfma_f32_16x16x32_bf16 v[96:99], a[96:99], v[216:219], v[96:99]\n"
"v_mul_f32 v42, v232, v42\n"
"v_mul_f32 v43, v232, v43\n"
"v_mfma_f32_16x16x32_bf16 v[116:119], a[100:103], v[216:219], v[116:119]\n"
"v_mul_f32 v60, v232, v60\n"
"v_mul_f32 v61, v232, v61\n"
"v_mfma_f32_16x16x32_bf16 v[136:139], a[104:107], v[216:219], v[136:139]\n"
"v_mul_f32 v62, v232, v62\n"
"v_mul_f32 v63, v232, v63\n"
"v_mfma_f32_16x16x32_bf16 v[156:159], a[108:111], v[216:219], v[156:159]\n"
"v_cvt_pk_bf16_f32 v40, v40, v41\n"
"v_cvt_pk_bf16_f32 v41, v42, v43\n"
"v_mfma_f32_16x16x32_bf16 a[160:163], a[164:167], v[216:219], a[160:163]\n"
"v_cvt_pk_bf16_f32 v42, v60, v61\n"
"v_cvt_pk_bf16_f32 v43, v62, v63\n"
"s_nop 7\n"
"v_accvgpr_read_b32 v220, a144\n"
"v_accvgpr_read_b32 v221, a148\n"
"v_accvgpr_read_b32 v222, a152\n"
"v_accvgpr_read_b32 v223, a156\n"
"v_accvgpr_read_b32 v224, a160\n"
"v_add_f32 v225, v220, v221\n"
"v_add_f32 v226, v222, v223\n"
"v_add_f32 v225, v225, v224\n"
"v_add_f32 v225, v225, v226\n"
"v_cmp_nge_f32 vcc, s91, v225\n"
"s_nop 3\n"
"s_cbranch_vccnz L_slow1%=\n"
"L_sback1%=:\n"
"s_mov_b32 s86, 1073741696\n"
"s_mov_b32 s75, 9\n"
"s_mov_b32 s68, s21\n"
"s_mov_b32 s87, 1073741824\n"
"s_waitcnt lgkmcnt(0)\n"
"v_mfma_f32_16x16x32_bf16 v[192:195], v[160:163], a[176:179], 0\n"
"ds_read_b64_tr_b16 v[176:177], v241 offset:0\n"
"v_xor_b32 v245, 16384, v240\n"
"v_mfma_f32_16x16x32_bf16 v[192:195], v[164:167], a[196:199], v[192:195]\n"
"ds_read_b64_tr_b16 v[178:179], v241 offset:2048\n"
"v_xor_b32 v246, 16416, v240\n"
"v_mfma_f32_16x16x32_bf16 v[192:195], v[168:171], a[216:219], v[192:195]\n"
"ds_read_b64_tr_b16 v[180:181], v241 offset:4096\n"
"v_mfma_f32_16x16x32_bf16 v[192:195], v[172:175], a[236:239], v[192:195]\n"
"ds_read_b64_tr_b16 v[182:183], v241 offset:6144\n"
"v_mfma_f32_16x16x32_bf16 v[196:199], v[160:163], a[180:183], 0\n"
"ds_read_b64_tr_b16 v[184:185], v241 offset:8192\n"
"v_mfma_f32_16x16x32_bf16 v[196:199], v[164:167], a[200:203], v[196:199]\n"
"ds_read_b64_tr_b16 v[186:187], v241 offset:10240\n"
"v_mfma_f32_16x16x32_bf16 v[196:199], v[168:171], a[220:223], v[196:199]\n"
"ds_read_b64_tr_b16 v[188:189], v241 offset:12288\n"
"v_mfma_f32_16x16x32_bf16 v[196:199], v[172:175], a[240:243], v[196:199]\n"
"ds_read_b64_tr_b16 v[190:191], v241 offset:14336\n"
"v_mfma_f32_16x16x32_bf16 v[200:203], v[160:163], a[184:187], 0\n"
"ds_read_b128 a[0:3], v253 offset:0\n"
"v_mfma_f32_16x16x32_bf16 v[200:203], v[164:167], a[204:207], v[200:203]\n"
"ds_read_b128 a[4:7], v253 offset:1024\n"
"v_mfma_f32_16x16x32_bf16 v[200:203], v[168:171], a[224:227], v[200:203]\n"
"ds_read_b128 a[8:11], v253 offset:2048\n"
"v_mfma_f32_16x16x32_bf16 v[200:203], v[172:175], a[244:247], v[200:203]\n"
"ds_read_b128 a[12:15], v253 offset:3072\n"
"v_mfma_f32_16x16x32_bf16 v[204:207], v[160:163], a[188:191], 0\n"
"ds_read_b128 a[16:19], v253 offset:4096\n"
"v_mfma_f32_16x16x32_bf16 v[204:207], v[164:167], a[208:211], v[204:207]\n"
"ds_read_b128 a[20:23], v253 offset:5120\n"
"v_mfma_f32_16x16x32_bf16 v[204:207], v[168:171], a[228:231], v[204:207]\n"
"ds_read_b128 a[24:27], v253 offset:6144\n"
"v_mfma_f32_16x16x32_bf16 v[204:207], v[172:175], a[248:251], v[204:207]\n"
"ds_read_b128 a[28:31], v253 offset:7168\n"
"v_mfma_f32_16x16x32_bf16 v[208:211], v[160:163], a[192:195], 0\n"
"ds_read_b128 a[32:35], v253 offset:8192\n"
"v_mfma_f32_16x16x32_bf16 v[208:211], v[164:167], a[212:215], v[208:211]\n"
"ds_read_b128 a[36:39], v253 offset:9216\n"
"v_mfma_f32_16x16x32_bf16 v[208:211], v[168:171], a[232:235], v[208:211]\n"
"ds_read_b128 a[40:43], v253 offset:10240\n"
"v_mfma_f32_16x16x32_bf16 v[208:211], v[172:175], a[252:255], v[208:211]\n"
"s_waitcnt lgkmcnt(11)\n"
"ds_read_b128 a[44:47], v253 offset:11264\n"
"v_mfma_f32_16x16x32_bf16 v[212:215], v[176:179], a[176:179], 0\n"
"ds_read_b64_tr_b16 v[160:161], v245 offset:0\n"
"v_mfma_f32_16x16x32_bf16 v[212:215], v[180:183], a[196:199], v[212:215]\n"
"ds_read_b64_tr_b16 v[162:163], v245 offset:2048\n"
"v_mfma_f32_16x16x32_bf16 v[212:215], v[184:187], a[216:219], v[212:215]\n"
"ds_read_b64_tr_b16 v[164:165], v245 offset:4096\n"
"v_mfma_f32_16x16x32_bf16 v[212:215], v[188:191], a[236:239], v[212:215]\n"
"ds_read_b64_tr_b16 v[166:167], v245 offset:6144\n"
"v_mfma_f32_16x16x32_bf16 v[216:219], v[176:179], a[180:183], 0\n"
"ds_read_b64_tr_b16 v[168:169], v245 offset:8192\n"
"v_mfma_f32_16x16x32_bf16 v[216:219], v[180:183], a[200:203], v[216:219]\n"
"ds_read_b64_tr_b16 v[170:171], v245 offset:10240\n"
"v_mfma_f32_16x16x32_bf16 v[216:219], v[184:187], a[220:223], v[216:219]\n"
"ds_read_b64_tr_b16 v[172:173], v245 offset:12288\n"
"v_mfma_f32_16x16x32_bf16 v[216:219], v[188:191], a[240:243], v[216:219]\n"
"ds_read_b64_tr_b16 v[174:175], v245 offset:14336\n"
"v_mfma_f32_16x16x32_bf16 v[220:223], v[176:179], a[184:187], 0\n"
"ds_read_b128 a[48:51], v253 offset:12288\n"
"v_mfma_f32_16x16x32_bf16 v[220:223], v[180:183], a[204:207], v[220:223]\n"
"ds_read_b128 a[52:55], v253 offset:13312\n"
"v_mfma_f32_16x16x32_bf16 v[220:223], v[184:187], a[224:227], v[220:223]\n"
"ds_read_b128 a[56:59], v253 offset:14336\n"
"v_mfma_f32_16x16x32_bf16 v[220:223], v[188:191], a[244:247], v[220:223]\n"
"ds_read_b128 a[60:63], v253 offset:15360\n"
"v_mfma_f32_16x16x32_bf16 v[224:227], v[176:179], a[188:191], 0\n"
"ds_read_b128 a[64:67], v248 offset:0\n"
"v_mfma_f32_16x16x32_bf16 v[224:227], v[180:183], a[208:211], v[224:227]\n"
"ds_read_b128 a[68:71], v248 offset:1024\n"
"v_mfma_f32_16x16x32_bf16 v[224:227], v[184:187], a[228:231], v[224:227]\n"
"ds_read_b128 a[72:75], v248 offset:2048\n"
"v_mfma_f32_16x16x32_bf16 v[224:227], v[188:191], a[248:251], v[224:227]\n"
"ds_read_b128 a[76:79], v248 offset:3072\n"
"v_mfma_f32_16x16x32_bf16 v[228:231], v[176:179], a[192:195], 0\n"
"v_mul_f32 v80, v232, v80\n"
"v_mul_f32 v81, v232, v81\n"
"v_mfma_f32_16x16x32_bf16 v[228:231], v[180:183], a[212:215], v[228:231]\n"
"v_mul_f32 v82, v232, v82\n"
"v_mul_f32 v83, v232, v83\n"
"v_mfma_f32_16x16x32_bf16 v[228:231], v[184:187], a[232:235], v[228:231]\n"
"v_mul_f32 v100, v232, v100\n"
"v_mul_f32 v101, v232, v101\n"
"v_mfma_f32_16x16x32_bf16 v[228:231], v[188:191], a[252:255], v[228:231]\n"
"s_waitcnt lgkmcnt(0)\n"
"v_mfma_f32_16x16x32_bf16 v[192:195], v[160:163], a[0:3], v[192:195]\n"
"ds_read_b64_tr_b16 v[176:177], v246 offset:0\n"
"v_add_u32 v252, s64, v251\n"
"v_mfma_f32_16x16x32_bf16 v[192:195], v[164:167], a[20:23], v[192:195]\n"
"ds_read_b64_tr_b16 v[178:179], v246 offset:2048\n"
"v_add_u32 v253, s65, v251\n"
"v_mfma_f32_16x16x32_bf16 v[192:195], v[168:171], a[40:43], v[192:195]\n"
"ds_read_b64_tr_b16 v[180:181], v246 offset:4096\n"
"v_mfma_f32_16x16x32_bf16 v[192:195], v[172:175], a[60:63], v[192:195]\n"
"ds_read_b64_tr_b16 v[182:183], v246 offset:6144\n"
"v_mfma_f32_16x16x32_bf16 v[196:199], v[160:163], a[4:7], v[196:199]\n"
"ds_read_b64_tr_b16 v[184:185], v246 offset:8192\n"
"v_mfma_f32_16x16x32_bf16 v[196:199], v[164:167], a[24:27], v[196:199]\n"
"ds_read_b64_tr_b16 v[186:187], v246 offset:10240\n"
"v_mfma_f32_16x16x32_bf16 v[196:199], v[168:171], a[44:47], v[196:199]\n"
"ds_read_b64_tr_b16 v[188:189], v246 offset:12288\n"
"v_mfma_f32_16x16x32_bf16 v[196:199], v[172:175], a[64:67], v[196:199]\n"
"ds_read_b64_tr_b16 v[190:191], v246 offset:14336\n"
"v_mfma_f32_16x16x32_bf16 v[200:203], v[160:163], a[8:11], v[200:203]\n"
"v_mul_f32 v102, v232, v102\n"
"v_mul_f32 v103, v232, v103\n"
"v_mfma_f32_16x16x32_bf16 v[200:203], v[164:167], a[28:31], v[200:203]\n"
"v_cvt_pk_bf16_f32 v80, v80, v81\n"
"v_cvt_pk_bf16_f32 v81, v82, v83\n"
"v_mfma_f32_16x16x32_bf16 v[200:203], v[168:171], a[48:51], v[200:203]\n"
"v_cvt_pk_bf16_f32 v82, v100, v101\n"
"v_cvt_pk_bf16_f32 v83, v102, v103\n"
"v_mfma_f32_16x16x32_bf16 v[200:203], v[172:175], a[68:71], v[200:203]\n"
"v_mul_f32 v120, v232, v120\n"
"v_mul_f32 v121, v232, v121\n"
"v_mfma_f32_16x16x32_bf16 v[204:207], v[160:163], a[12:15], v[204:207]\n"
"v_mul_f32 v122, v232, v122\n"
"v_mul_f32 v123, v232, v123\n"
"v_mfma_f32_16x16x32_bf16 v[204:207], v[164:167], a[32:35], v[204:207]\n"
"v_mul_f32 v140, v232, v140\n"
"v_mul_f32 v141, v232, v141\n"
"v_mfma_f32_16x16x32_bf16 v[204:207], v[168:171], a[52:55], v[204:207]\n"
"v_mul_f32 v142, v232, v142\n"
"v_mul_f32 v143, v232, v143\n"
"v_mfma_f32_16x16x32_bf16 v[204:207], v[172:175], a[72:75], v[204:207]\n"
"v_cvt_pk_bf16_f32 v120, v120, v121\n"
"v_cvt_pk_bf16_f32 v121, v122, v123\n"
"v_mfma_f32_16x16x32_bf16 v[208:211], v[160:163], a[16:19], v[208:211]\n"
"v_add_u32 v240, 32768, v242\n"
"v_cvt_pk_bf16_f32 v122, v140, v141\n"
"v_mfma_f32_16x16x32_bf16 v[208:211], v[164:167], a[36:39], v[208:211]\n"
"v_cvt_pk_bf16_f32 v123, v142, v143\n"
"v_accvgpr_read_b32 v233, a148\n"
"v_mfma_f32_16x16x32_bf16 v[208:211], v[168:171], a[56:59], v[208:211]\n"
"v_xor_b32 v241, 32, v240\n"
"s_nop 0\n"
"v_mfma_f32_16x16x32_bf16 v[208:211], v[172:175], a[76:79], v[208:211]\n"
"s_waitcnt vmcnt(0) lgkmcnt(0)\n"
"s_barrier\n"
"v_mfma_f32_16x16x32_bf16 v[212:215], v[176:179], a[0:3], v[212:215]\n"
"ds_write_b128 v252, v[0:3] offset:0\n"
"v_mfma_f32_16x16x32_bf16 v[212:215], v[180:183], a[20:23], v[212:215]\n"
"ds_read_b64_tr_b16 v[160:161], v240 offset:0\n"
"v_xor_b32 v245, 16384, v240\n"
"v_mfma_f32_16x16x32_bf16 v[212:215], v[184:187], a[40:43], v[212:215]\n"
"ds_read_b64_tr_b16 v[162:163], v240 offset:2048\n"
"v_xor_b32 v246, 16416, v240\n"
"v_mfma_f32_16x16x32_bf16 v[212:215], v[188:191], a[60:63], v[212:215]\n"
"ds_read_b64_tr_b16 v[164:165], v240 offset:4096\n"
"v_mfma_f32_16x16x32_bf16 v[216:219], v[176:179], a[4:7], v[216:219]\n"
"ds_read_b64_tr_b16 v[166:167], v240 offset:6144\n"
"v_mfma_f32_16x16x32_bf16 v[216:219], v[180:183], a[24:27], v[216:219]\n"
"ds_read_b64_tr_b16 v[168:169], v240 offset:8192\n"
"v_mfma_f32_16x16x32_bf16 v[216:219], v[184:187], a[44:47], v[216:219]\n"
"ds_read_b64_tr_b16 v[170:171], v240 offset:10240\n"
"v_mfma_f32_16x16x32_bf16 v[216:219], v[188:191], a[64:67], v[216:219]\n"
"ds_read_b64_tr_b16 v[172:173], v240 offset:12288\n"
"s_add_u32 m0, s81, 65536\n"
"v_mfma_f32_16x16x32_bf16 v[220:223], v[176:179], a[8:11], v[220:223]\n"
"ds_read_b64_tr_b16 v[174:175], v240 offset:14336\n"
"v_mfma_f32_16x16x32_bf16 v[220:223], v[180:183], a[28:31], v[220:223]\n"
"buffer_load_dwordx4 v243, s[52:55], 0 offen lds\n"
"v_mfma_f32_16x16x32_bf16 v[220:223], v[184:187], a[48:51], v[220:223]\n"
"s_add_u32 m0, s81, 69632\n"
"v_mfma_f32_16x16x32_bf16 v[220:223], v[188:191], a[68:71], v[220:223]\n"
"buffer_load_dwordx4 v243, s[52:55], s69 offen lds\n"
"v_mfma_f32_16x16x32_bf16 v[224:227], v[176:179], a[12:15], v[224:227]\n"
"s_add_u32 m0, s81, 73728\n"
"v_mfma_f32_16x16x32_bf16 v[224:227], v[180:183], a[32:35], v[224:227]\n"
"buffer_load_dwordx4 v243, s[52:55], s70 offen lds\n"
"v_mfma_f32_16x16x32_bf16 v[224:227], v[184:187], a[52:55], v[224:227]\n"
"s_add_u32 m0, s81, 77824\n"
"v_mfma_f32_16x16x32_bf16 v[224:227], v[188:191], a[72:75], v[224:227]\n"
"buffer_load_dwordx4 v243, s[52:55], s71 offen lds\n"
"v_mfma_f32_16x16x32_bf16 v[228:231], v[176:179], a[16:19], v[228:231]\n"
"s_add_u32 m0, s81, 81920\n"
"v_mfma_f32_16x16x32_bf16 v[228:231], v[180:183], a[36:39], v[228:231]\n"
"buffer_load_dwordx4 v244, s[52:55], 0 offen lds\n"
"v_mfma_f32_16x16x32_bf16 v[228:231], v[184:187], a[56:59], v[228:231]\n"
"ds_write_b128 v252, v[40:43] offset:5120\n"
"v_mfma_f32_16x16x32_bf16 v[228:231], v[188:191], a[76:79], v[228:231]\n"
"s_waitcnt lgkmcnt(0)\n"
"s_add_u32 m0, s81, 86016\n"
"v_mfma_f32_16x16x32_bf16 v[192:195], v[160:163], v[0:3], v[192:195]\n"
"ds_read_b64_tr_b16 v[176:177], v241 offset:0\n"
"v_mfma_f32_16x16x32_bf16 v[192:195], v[164:167], v[40:43], v[192:195]\n"
"ds_read_b64_tr_b16 v[178:179], v241 offset:2048\n"
"v_mfma_f32_16x16x32_bf16 v[192:195], v[168:171], v[80:83], v[192:195]\n"
"ds_read_b64_tr_b16 v[180:181], v241 offset:4096\n"
"v_mfma_f32_16x16x32_bf16 v[192:195], v[172:175], v[120:123], v[192:195]\n"
"ds_read_b64_tr_b16 v[182:183], v241 offset:6144\n"
"s_nop 2\n"
"v_rcp_f32 v233, v233\n"
"ds_write_b128 v252, v[80:83] offset:10240\n"
"ds_write_b128 v252, v[120:123] offset:15360\n"
"s_nop 0\n"
"v_pk_mul_f32 v[4:5], v[4:5], v[232:233] op_sel:[0,1] op_sel_hi:[1,1]\n"
"v_pk_mul_f32 v[6:7], v[6:7], v[232:233] op_sel:[0,1] op_sel_hi:[1,1]\n"
"v_pk_mul_f32 v[24:25], v[24:25], v[232:233] op_sel:[0,1] op_sel_hi:[1,1]\n"
"v_pk_mul_f32 v[26:27], v[26:27], v[232:233] op_sel:[0,1] op_sel_hi:[1,1]\n"
"v_cvt_pk_bf16_f32 v4, v4, v5\n"
"v_cvt_pk_bf16_f32 v5, v6, v7\n"
"v_cvt_pk_bf16_f32 v6, v24, v25\n"
"v_cvt_pk_bf16_f32 v7, v26, v27\n"
"v_pk_mul_f32 v[44:45], v[44:45], v[232:233] op_sel:[0,1] op_sel_hi:[1,1]\n"
"ds_write_b128 v252, v[4:7] offset:1024\n"
"v_pk_mul_f32 v[46:47], v[46:47], v[232:233] op_sel:[0,1] op_sel_hi:[1,1]\n"
"v_pk_mul_f32 v[64:65], v[64:65], v[232:233] op_sel:[0,1] op_sel_hi:[1,1]\n"
"v_pk_mul_f32 v[66:67], v[66:67], v[232:233] op_sel:[0,1] op_sel_hi:[1,1]\n"
"v_cvt_pk_bf16_f32 v44, v44, v45\n"
"v_cvt_pk_bf16_f32 v45, v46, v47\n"
"v_cvt_pk_bf16_f32 v46, v64, v65\n"
"v_cvt_pk_bf16_f32 v47, v66, v67\n"
"v_pk_mul_f32 v[84:85], v[84:85], v[232:233] op_sel:[0,1] op_sel_hi:[1,1]\n"
"ds_write_b128 v252, v[44:47] offset:6144\n"
"v_pk_mul_f32 v[86:87], v[86:87], v[232:233] op_sel:[0,1] op_sel_hi:[1,1]\n"
"v_pk_mul_f32 v[104:105], v[104:105], v[232:233] op_sel:[0,1] op_sel_hi:[1,1]\n"
"v_pk_mul_f32 v[106:107], v[106:107], v[232:233] op_sel:[0,1] op_sel_hi:[1,1]\n"
"v_cvt_pk_bf16_f32 v84, v84, v85\n"
"v_cvt_pk_bf16_f32 v85, v86, v87\n"
"v_cvt_pk_bf16_f32 v86, v104, v105\n"
"v_cvt_pk_bf16_f32 v87, v106, v107\n"
"v_pk_mul_f32 v[124:125], v[124:125], v[232:233] op_sel:[0,1] op_sel_hi:[1,1]\n"
"ds_write_b128 v252, v[84:87] offset:11264\n"
"v_pk_mul_f32 v[126:127], v[126:127], v[232:233] op_sel:[0,1] op_sel_hi:[1,1]\n"
"v_pk_mul_f32 v[144:145], v[144:145], v[232:233] op_sel:[0,1] op_sel_hi:[1,1]\n"
"v_pk_mul_f32 v[146:147], v[146:147], v[232:233] op_sel:[0,1] op_sel_hi:[1,1]\n"
"v_cvt_pk_bf16_f32 v124, v124, v125\n"
"v_cvt_pk_bf16_f32 v125, v126, v127\n"
"v_cvt_pk_bf16_f32 v126, v144, v145\n"
"v_cvt_pk_bf16_f32 v127, v146, v147\n"
"s_nop 1\n"
"v_mfma_f32_16x16x32_bf16 v[196:199], v[160:163], v[4:7], v[196:199]\n"
"ds_read_b64_tr_b16 v[184:185], v241 offset:8192\n"
"v_accvgpr_read_b32 v234, a152\n"
"v_mfma_f32_16x16x32_bf16 v[196:199], v[164:167], v[44:47], v[196:199]\n"
"ds_read_b64_tr_b16 v[186:187], v241 offset:10240\n"
"s_nop 0\n"
"v_mfma_f32_16x16x32_bf16 v[196:199], v[168:171], v[84:87], v[196:199]\n"
"ds_read_b64_tr_b16 v[188:189], v241 offset:12288\n"
"v_mfma_f32_16x16x32_bf16 v[196:199], v[172:175], v[124:127], v[196:199]\n"
"ds_read_b64_tr_b16 v[190:191], v241 offset:14336\n"
"s_nop 2\n"
"v_rcp_f32 v234, v234\n"
"ds_write_b128 v252, v[124:127] offset:16384\n"
"s_nop 0\n"
"v_pk_mul_f32 v[8:9], v[8:9], v[234:235] op_sel:[0,0] op_sel_hi:[1,0]\n"
"v_pk_mul_f32 v[10:11], v[10:11], v[234:235] op_sel:[0,0] op_sel_hi:[1,0]\n"
"v_pk_mul_f32 v[28:29], v[28:29], v[234:235] op_sel:[0,0] op_sel_hi:[1,0]\n"
"v_pk_mul_f32 v[30:31], v[30:31], v[234:235] op_sel:[0,0] op_sel_hi:[1,0]\n"
"v_cvt_pk_bf16_f32 v8, v8, v9\n"
"v_cvt_pk_bf16_f32 v9, v10, v11\n"
"v_cvt_pk_bf16_f32 v10, v28, v29\n"
"v_cvt_pk_bf16_f32 v11, v30, v31\n"
"v_pk_mul_f32 v[48:49], v[48:49], v[234:235] op_sel:[0,0] op_sel_hi:[1,0]\n"
"ds_write_b128 v252, v[8:11] offset:2048\n"
"v_pk_mul_f32 v[50:51], v[50:51], v[234:235] op_sel:[0,0] op_sel_hi:[1,0]\n"
"v_pk_mul_f32 v[68:69], v[68:69], v[234:235] op_sel:[0,0] op_sel_hi:[1,0]\n"
"v_pk_mul_f32 v[70:71], v[70:71], v[234:235] op_sel:[0,0] op_sel_hi:[1,0]\n"
"v_cvt_pk_bf16_f32 v48, v48, v49\n"
"v_cvt_pk_bf16_f32 v49, v50, v51\n"
"v_cvt_pk_bf16_f32 v50, v68, v69\n"
"v_cvt_pk_bf16_f32 v51, v70, v71\n"
"v_pk_mul_f32 v[88:89], v[88:89], v[234:235] op_sel:[0,0] op_sel_hi:[1,0]\n"
"ds_write_b128 v252, v[48:51] offset:7168\n"
"v_pk_mul_f32 v[90:91], v[90:91], v[234:235] op_sel:[0,0] op_sel_hi:[1,0]\n"
"v_pk_mul_f32 v[108:109], v[108:109], v[234:235] op_sel:[0,0] op_sel_hi:[1,0]\n"
"v_pk_mul_f32 v[110:111], v[110:111], v[234:235] op_sel:[0,0] op_sel_hi:[1,0]\n"
"v_cvt_pk_bf16_f32 v88, v88, v89\n"
"v_cvt_pk_bf16_f32 v89, v90, v91\n"
"v_cvt_pk_bf16_f32 v90, v108, v109\n"
"v_cvt_pk_bf16_f32 v91, v110, v111\n"
"v_pk_mul_f32 v[128:129], v[128:129], v[234:235] op_sel:[0,0] op_sel_hi:[1,0]\n"
"ds_write_b128 v252, v[88:91] offset:12288\n"
"v_pk_mul_f32 v[130:131], v[130:131], v[234:235] op_sel:[0,0] op_sel_hi:[1,0]\n"
"v_pk_mul_f32 v[148:149], v[148:149], v[234:235] op_sel:[0,0] op_sel_hi:[1,0]\n"
"v_pk_mul_f32 v[150:151], v[150:151], v[234:235] op_sel:[0,0] op_sel_hi:[1,0]\n"
"v_cvt_pk_bf16_f32 v128, v128, v129\n"
"v_cvt_pk_bf16_f32 v129, v130, v131\n"
"v_cvt_pk_bf16_f32 v130, v148, v149\n"
"v_cvt_pk_bf16_f32 v131, v150, v151\n"
"s_nop 1\n"
"v_mfma_f32_16x16x32_bf16 v[200:203], v[160:163], v[8:11], v[200:203]\n"
"v_accvgpr_read_b32 v235, a156\n"
"s_nop 0\n"
"v_mfma_f32_16x16x32_bf16 v[200:203], v[164:167], v[48:51], v[200:203]\n"
"buffer_load_dwordx4 v244, s[52:55], s69 offen lds\n"
"v_mfma_f32_16x16x32_bf16 v[200:203], v[168:171], v[88:91], v[200:203]\n"
"s_add_u32 m0, s81, 90112\n"
"v_mfma_f32_16x16x32_bf16 v[200:203], v[172:175], v[128:131], v[200:203]\n"
"ds_write_b128 v252, v[128:131] offset:17408\n"
"s_nop 2\n"
"v_rcp_f32 v235, v235\n"
"s_nop 0\n"
"v_pk_mul_f32 v[12:13], v[12:13], v[234:235] op_sel:[0,1] op_sel_hi:[1,1]\n"
"v_pk_mul_f32 v[14:15], v[14:15], v[234:235] op_sel:[0,1] op_sel_hi:[1,1]\n"
"v_pk_mul_f32 v[32:33], v[32:33], v[234:235] op_sel:[0,1] op_sel_hi:[1,1]\n"
"v_pk_mul_f32 v[34:35], v[34:35], v[234:235] op_sel:[0,1] op_sel_hi:[1,1]\n"
"v_cvt_pk_bf16_f32 v12, v12, v13\n"
"v_cvt_pk_bf16_f32 v13, v14, v15\n"
"v_cvt_pk_bf16_f32 v14, v32, v33\n"
"v_cvt_pk_bf16_f32 v15, v34, v35\n"
"v_pk_mul_f32 v[52:53], v[52:53], v[234:235] op_sel:[0,1] op_sel_hi:[1,1]\n"
"ds_write_b128 v252, v[12:15] offset:3072\n"
"v_pk_mul_f32 v[54:55], v[54:55], v[234:235] op_sel:[0,1] op_sel_hi:[1,1]\n"
"v_pk_mul_f32 v[72:73], v[72:73], v[234:235] op_sel:[0,1] op_sel_hi:[1,1]\n"
"v_pk_mul_f32 v[74:75], v[74:75], v[234:235] op_sel:[0,1] op_sel_hi:[1,1]\n"
"v_cvt_pk_bf16_f32 v52, v52, v53\n"
"v_cvt_pk_bf16_f32 v53, v54, v55\n"
"v_cvt_pk_bf16_f32 v54, v72, v73\n"
"v_cvt_pk_bf16_f32 v55, v74, v75\n"
"v_pk_mul_f32 v[92:93], v[92:93], v[234:235] op_sel:[0,1] op_sel_hi:[1,1]\n"
"ds_write_b128 v252, v[52:55] offset:8192\n"
"v_pk_mul_f32 v[94:95], v[94:95], v[234:235] op_sel:[0,1] op_sel_hi:[1,1]\n"
"v_pk_mul_f32 v[112:113], v[112:113], v[234:235] op_sel:[0,1] op_sel_hi:[1,1]\n"
"v_pk_mul_f32 v[114:115], v[114:115], v[234:235] op_sel:[0,1] op_sel_hi:[1,1]\n"
"v_cvt_pk_bf16_f32 v92, v92, v93\n"
"v_cvt_pk_bf16_f32 v93, v94, v95\n"
"v_cvt_pk_bf16_f32 v94, v112, v113\n"
"v_cvt_pk_bf16_f32 v95, v114, v115\n"
"v_pk_mul_f32 v[132:133], v[132:133], v[234:235] op_sel:[0,1] op_sel_hi:[1,1]\n"
"ds_write_b128 v252, v[92:95] offset:13312\n"
"v_pk_mul_f32 v[134:135], v[134:135], v[234:235] op_sel:[0,1] op_sel_hi:[1,1]\n"
"v_pk_mul_f32 v[152:153], v[152:153], v[234:235] op_sel:[0,1] op_sel_hi:[1,1]\n"
"v_pk_mul_f32 v[154:155], v[154:155], v[234:235] op_sel:[0,1] op_sel_hi:[1,1]\n"
"v_cvt_pk_bf16_f32 v132, v132, v133\n"
"v_cvt_pk_bf16_f32 v133, v134, v135\n"
"v_cvt_pk_bf16_f32 v134, v152, v153\n"
"v_cvt_pk_bf16_f32 v135, v154, v155\n"
"s_nop 1\n"
"v_mfma_f32_16x16x32_bf16 v[204:207], v[160:163], v[12:15], v[204:207]\n"
"buffer_load_dwordx4 v244, s[52:55], s70 offen lds\n"
"v_mfma_f32_16x16x32_bf16 v[204:207], v[164:167], v[52:55], v[204:207]\n"
"s_add_u32 m0, s81, 94208\n"
"v_accvgpr_read_b32 v236, a160\n"
"v_mfma_f32_16x16x32_bf16 v[204:207], v[168:171], v[92:95], v[204:207]\n"
"ds_write_b128 v252, v[132:135] offset:18432\n"
"v_mfma_f32_16x16x32_bf16 v[204:207], v[172:175], v[132:135], v[204:207]\n"
"buffer_load_dwordx4 v244, s[52:55], s71 offen lds\n"
"s_nop 2\n"
"s_nop 0\n"
"v_rcp_f32 v236, v236\n"
"s_nop 0\n"
"v_pk_mul_f32 v[16:17], v[16:17], v[236:237] op_sel:[0,0] op_sel_hi:[1,0]\n"
"v_pk_mul_f32 v[18:19], v[18:19], v[236:237] op_sel:[0,0] op_sel_hi:[1,0]\n"
"v_pk_mul_f32 v[36:37], v[36:37], v[236:237] op_sel:[0,0] op_sel_hi:[1,0]\n"
"v_pk_mul_f32 v[38:39], v[38:39], v[236:237] op_sel:[0,0] op_sel_hi:[1,0]\n"
"v_cvt_pk_bf16_f32 v16, v16, v17\n"
"v_cvt_pk_bf16_f32 v17, v18, v19\n"
"v_cvt_pk_bf16_f32 v18, v36, v37\n"
"v_cvt_pk_bf16_f32 v19, v38, v39\n"
"v_pk_mul_f32 v[56:57], v[56:57], v[236:237] op_sel:[0,0] op_sel_hi:[1,0]\n"
"ds_write_b128 v252, v[16:19] offset:4096\n"
"v_pk_mul_f32 v[58:59], v[58:59], v[236:237] op_sel:[0,0] op_sel_hi:[1,0]\n"
"v_pk_mul_f32 v[76:77], v[76:77], v[236:237] op_sel:[0,0] op_sel_hi:[1,0]\n"
"v_pk_mul_f32 v[78:79], v[78:79], v[236:237] op_sel:[0,0] op_sel_hi:[1,0]\n"
"v_cvt_pk_bf16_f32 v56, v56, v57\n"
"v_cvt_pk_bf16_f32 v57, v58, v59\n"
"v_cvt_pk_bf16_f32 v58, v76, v77\n"
"v_cvt_pk_bf16_f32 v59, v78, v79\n"
"v_pk_mul_f32 v[96:97], v[96:97], v[236:237] op_sel:[0,0] op_sel_hi:[1,0]\n"
"ds_write_b128 v252, v[56:59] offset:9216\n"
"v_pk_mul_f32 v[98:99], v[98:99], v[236:237] op_sel:[0,0] op_sel_hi:[1,0]\n"
"v_pk_mul_f32 v[116:117], v[116:117], v[236:237] op_sel:[0,0] op_sel_hi:[1,0]\n"
"v_pk_mul_f32 v[118:119], v[118:119], v[236:237] op_sel:[0,0] op_sel_hi:[1,0]\n"
"v_cvt_pk_bf16_f32 v96, v96, v97\n"
"v_cvt_pk_bf16_f32 v97, v98, v99\n"
"v_cvt_pk_bf16_f32 v98, v116, v117\n"
"v_cvt_pk_bf16_f32 v99, v118, v119\n"
"v_pk_mul_f32 v[136:137], v[136:137], v[236:237] op_sel:[0,0] op_sel_hi:[1,0]\n"
"ds_write_b128 v252, v[96:99] offset:14336\n"
"v_pk_mul_f32 v[138:139], v[138:139], v[236:237] op_sel:[0,0] op_sel_hi:[1,0]\n"
"v_pk_mul_f32 v[156:157], v[156:157], v[236:237] op_sel:[0,0] op_sel_hi:[1,0]\n"
"v_pk_mul_f32 v[158:159], v[158:159], v[236:237] op_sel:[0,0] op_sel_hi:[1,0]\n"
"v_cvt_pk_bf16_f32 v136, v136, v137\n"
"v_cvt_pk_bf16_f32 v137, v138, v139\n"
"v_cvt_pk_bf16_f32 v138, v156, v157\n"
"v_cvt_pk_bf16_f32 v139, v158, v159\n"
"s_nop 1\n"
"v_mfma_f32_16x16x32_bf16 v[208:211], v[160:163], v[16:19], v[208:211]\n"
"v_add_u32 v243, s16, v243\n"
"v_add_u32 v244, s16, v244\n"
"v_mfma_f32_16x16x32_bf16 v[208:211], v[164:167], v[56:59], v[208:211]\n"
"v_mfma_f32_16x16x32_bf16 v[208:211], v[168:171], v[96:99], v[208:211]\n"
"ds_write_b128 v252, v[136:139] offset:19456\n"
"v_mfma_f32_16x16x32_bf16 v[208:211], v[172:175], v[136:139], v[208:211]\n"
"s_waitcnt lgkmcnt(0)\n"
"s_barrier\n"
"v_mfma_f32_16x16x32_bf16 v[212:215], v[176:179], v[0:3], v[212:215]\n"
"ds_read_b64_tr_b16 v[160:161], v245 offset:0\n"
"v_mfma_f32_16x16x32_bf16 v[212:215], v[180:183], v[40:43], v[212:215]\n"
"ds_read_b64_tr_b16 v[162:163], v245 offset:2048\n"
"v_mfma_f32_16x16x32_bf16 v[212:215], v[184:187], v[80:83], v[212:215]\n"
"ds_read_b64_tr_b16 v[164:165], v245 offset:4096\n"
"v_mfma_f32_16x16x32_bf16 v[212:215], v[188:191], v[120:123], v[212:215]\n"
"ds_read_b64_tr_b16 v[166:167], v245 offset:6144\n"
"v_mfma_f32_16x16x32_bf16 v[216:219], v[176:179], v[4:7], v[216:219]\n"
"ds_read_b64_tr_b16 v[168:169], v245 offset:8192\n"
"v_mfma_f32_16x16x32_bf16 v[216:219], v[180:183], v[44:47], v[216:219]\n"
"ds_read_b64_tr_b16 v[170:171], v245 offset:10240\n"
"v_mfma_f32_16x16x32_bf16 v[216:219], v[184:187], v[84:87], v[216:219]\n"
"ds_read_b64_tr_b16 v[172:173], v245 offset:12288\n"
"v_mfma_f32_16x16x32_bf16 v[216:219], v[188:191], v[124:127], v[216:219]\n"
"ds_read_b64_tr_b16 v[174:175], v245 offset:14336\n"
"v_mfma_f32_16x16x32_bf16 v[220:223], v[176:179], v[8:11], v[220:223]\n"
"ds_read_b128 a[80:83], v253 offset:0\n"
"v_mfma_f32_16x16x32_bf16 v[220:223], v[180:183], v[48:51], v[220:223]\n"
"ds_read_b128 a[100:103], v253 offset:5120\n"
"v_mfma_f32_16x16x32_bf16 v[220:223], v[184:187], v[88:91], v[220:223]\n"
"ds_read_b128 a[120:123], v253 offset:10240\n"
"v_mfma_f32_16x16x32_bf16 v[220:223], v[188:191], v[128:131], v[220:223]\n"
"ds_read_b128 a[140:143], v253 offset:15360\n"
"v_mfma_f32_16x16x32_bf16 v[224:227], v[176:179], v[12:15], v[224:227]\n"
"ds_read_b128 a[84:87], v253 offset:1024\n"
"v_mfma_f32_16x16x32_bf16 v[224:227], v[180:183], v[52:55], v[224:227]\n"
"ds_read_b128 a[104:107], v253 offset:6144\n"
"v_mfma_f32_16x16x32_bf16 v[224:227], v[184:187], v[92:95], v[224:227]\n"
"ds_read_b128 a[124:127], v253 offset:11264\n"
"v_mfma_f32_16x16x32_bf16 v[224:227], v[188:191], v[132:135], v[224:227]\n"
"ds_read_b128 a[144:147], v253 offset:16384\n"
"v_mfma_f32_16x16x32_bf16 v[228:231], v[176:179], v[16:19], v[228:231]\n"
"ds_read_b128 a[88:91], v253 offset:2048\n"
"v_mfma_f32_16x16x32_bf16 v[228:231], v[180:183], v[56:59], v[228:231]\n"
"ds_read_b128 a[108:111], v253 offset:7168\n"
"v_mfma_f32_16x16x32_bf16 v[228:231], v[184:187], v[96:99], v[228:231]\n"
"ds_read_b128 a[128:131], v253 offset:12288\n"
"v_mfma_f32_16x16x32_bf16 v[228:231], v[188:191], v[136:139], v[228:231]\n"
"s_waitcnt lgkmcnt(11)\n"
"ds_read_b128 a[148:151], v253 offset:17408\n"
"s_waitcnt lgkmcnt(8)\n"
"v_mfma_f32_16x16x32_bf16 v[192:195], v[160:163], a[80:83], v[192:195]\n"
"ds_read_b64_tr_b16 v[176:177], v246 offset:0\n"
"v_mfma_f32_16x16x32_bf16 v[192:195], v[164:167], a[100:103], v[192:195]\n"
"ds_read_b64_tr_b16 v[178:179], v246 offset:2048\n"
"v_mfma_f32_16x16x32_bf16 v[192:195], v[168:171], a[120:123], v[192:195]\n"
"ds_read_b64_tr_b16 v[180:181], v246 offset:4096\n"
"v_mfma_f32_16x16x32_bf16 v[192:195], v[172:175], a[140:143], v[192:195]\n"
"ds_read_b64_tr_b16 v[182:183], v246 offset:6144\n"
"s_waitcnt lgkmcnt(8)\n"
"v_mfma_f32_16x16x32_bf16 v[196:199], v[160:163], a[84:87], v[196:199]\n"
"ds_read_b64_tr_b16 v[184:185], v246 offset:8192\n"
"v_mfma_f32_16x16x32_bf16 v[196:199], v[164:167], a[104:107], v[196:199]\n"
"ds_read_b64_tr_b16 v[186:187], v246 offset:10240\n"
"v_mfma_f32_16x16x32_bf16 v[196:199], v[168:171], a[124:127], v[196:199]\n"
"ds_read_b64_tr_b16 v[188:189], v246 offset:12288\n"
"v_mfma_f32_16x16x32_bf16 v[196:199], v[172:175], a[144:147], v[196:199]\n"
"ds_read_b64_tr_b16 v[190:191], v246 offset:14336\n"
"s_waitcnt lgkmcnt(8)\n"
"v_mfma_f32_16x16x32_bf16 v[200:203], v[160:163], a[88:91], v[200:203]\n"
"ds_read_b128 a[92:95], v253 offset:3072\n"
"v_mfma_f32_16x16x32_bf16 v[200:203], v[164:167], a[108:111], v[200:203]\n"
"ds_read_b128 a[112:115], v253 offset:8192\n"
"v_mfma_f32_16x16x32_bf16 v[200:203], v[168:171], a[128:131], v[200:203]\n"
"ds_read_b128 a[132:135], v253 offset:13312\n"
"v_mfma_f32_16x16x32_bf16 v[200:203], v[172:175], a[148:151], v[200:203]\n"
"ds_read_b128 a[152:155], v253 offset:18432\n"
"s_waitcnt lgkmcnt(0)\n"
"v_mfma_f32_16x16x32_bf16 v[204:207], v[160:163], a[92:95], v[204:207]\n"
"ds_read_b128 a[96:99], v253 offset:4096\n"
"v_mfma_f32_16x16x32_bf16 v[204:207], v[164:167], a[112:115], v[204:207]\n"
"ds_read_b128 a[116:119], v253 offset:9216\n"
"v_mfma_f32_16x16x32_bf16 v[204:207], v[168:171], a[132:135], v[204:207]\n"
"ds_read_b128 a[136:139], v253 offset:14336\n"
"v_mfma_f32_16x16x32_bf16 v[204:207], v[172:175], a[152:155], v[204:207]\n"
"ds_read_b128 a[156:159], v253 offset:19456\n"
"s_waitcnt lgkmcnt(0)\n"
"v_mfma_f32_16x16x32_bf16 v[208:211], v[160:163], a[96:99], v[208:211]\n"
"v_add_u32 v240, 65536, v242\n"
"v_mfma_f32_16x16x32_bf16 v[208:211], v[164:167], a[116:119], v[208:211]\n"
"v_mfma_f32_16x16x32_bf16 v[208:211], v[168:171], a[136:139], v[208:211]\n"
"v_xor_b32 v241, 32, v240\n"
"s_add_u32 m0, s81, 0\n"
"v_mfma_f32_16x16x32_bf16 v[208:211], v[172:175], a[156:159], v[208:211]\n"
"s_waitcnt vmcnt(0) lgkmcnt(0)\n"
"s_barrier\n"
"v_mfma_f32_16x16x32_bf16 v[212:215], v[176:179], a[80:83], v[212:215]\n"
"buffer_load_dwordx4 v243, s[52:55], 0 offen lds\n"
"v_mfma_f32_16x16x32_bf16 v[212:215], v[180:183], a[100:103], v[212:215]\n"
"ds_read_b64_tr_b16 v[160:161], v240 offset:0\n"
"v_xor_b32 v245, 16384, v240\n"
"v_mfma_f32_16x16x32_bf16 v[212:215], v[184:187], a[120:123], v[212:215]\n"
"ds_read_b64_tr_b16 v[162:163], v240 offset:2048\n"
"v_xor_b32 v246, 16416, v240\n"
"v_mfma_f32_16x16x32_bf16 v[212:215], v[188:191], a[140:143], v[212:215]\n"
"ds_read_b64_tr_b16 v[164:165], v240 offset:4096\n"
"s_add_u32 m0, s81, 4096\n"
"v_mfma_f32_16x16x32_bf16 v[216:219], v[176:179], a[84:87], v[216:219]\n"
"ds_read_b64_tr_b16 v[166:167], v240 offset:6144\n"
"v_cvt_pk_bf16_f32 v20, v192, v193\n"
"v_mfma_f32_16x16x32_bf16 v[216:219], v[180:183], a[104:107], v[216:219]\n"
"ds_read_b64_tr_b16 v[168:169], v240 offset:8192\n"
"v_cvt_pk_bf16_f32 v21, v194, v195\n"
"v_mfma_f32_16x16x32_bf16 v[216:219], v[184:187], a[124:127], v[216:219]\n"
"ds_read_b64_tr_b16 v[170:171], v240 offset:10240\n"
"v_cvt_pk_bf16_f32 v24, v196, v197\n"
"v_mfma_f32_16x16x32_bf16 v[216:219], v[188:191], a[144:147], v[216:219]\n"
"ds_read_b64_tr_b16 v[172:173], v240 offset:12288\n"
"v_cvt_pk_bf16_f32 v25, v198, v199\n"
"v_mfma_f32_16x16x32_bf16 v[220:223], v[176:179], a[88:91], v[220:223]\n"
"ds_read_b64_tr_b16 v[174:175], v240 offset:14336\n"
"v_cvt_pk_bf16_f32 v28, v200, v201\n"
"v_mfma_f32_16x16x32_bf16 v[220:223], v[180:183], a[108:111], v[220:223]\n"
"buffer_load_dwordx4 v243, s[52:55], s69 offen lds\n"
"v_mfma_f32_16x16x32_bf16 v[220:223], v[184:187], a[128:131], v[220:223]\n"
"s_add_u32 m0, s81, 8192\n"
"v_mfma_f32_16x16x32_bf16 v[220:223], v[188:191], a[148:151], v[220:223]\n"
"buffer_load_dwordx4 v243, s[52:55], s70 offen lds\n"
"v_mfma_f32_16x16x32_bf16 v[224:227], v[176:179], a[92:95], v[224:227]\n"
"s_add_u32 m0, s81, 12288\n"
"v_mfma_f32_16x16x32_bf16 v[224:227], v[180:183], a[112:115], v[224:227]\n"
"buffer_load_dwordx4 v243, s[52:55], s71 offen lds\n"
"v_mfma_f32_16x16x32_bf16 v[224:227], v[184:187], a[132:135], v[224:227]\n"
"s_add_u32 m0, s81, 16384\n"
"v_mfma_f32_16x16x32_bf16 v[224:227], v[188:191], a[152:155], v[224:227]\n"
"buffer_load_dwordx4 v244, s[52:55], 0 offen lds\n"
"v_mfma_f32_16x16x32_bf16 v[228:231], v[176:179], a[96:99], v[228:231]\n"
"s_add_u32 m0, s81, 20480\n"
"v_mfma_f32_16x16x32_bf16 v[228:231], v[180:183], a[116:119], v[228:231]\n"
"buffer_load_dwordx4 v244, s[52:55], s69 offen lds\n"
"v_mfma_f32_16x16x32_bf16 v[228:231], v[184:187], a[136:139], v[228:231]\n"
"v_mfma_f32_16x16x32_bf16 v[228:231], v[188:191], a[156:159], v[228:231]\n"
"s_waitcnt lgkmcnt(0)\n"
"v_cvt_pk_bf16_f32 v29, v202, v203\n"
"s_add_u32 m0, s81, 24576\n"
"s_nop 0\n"
"buffer_load_dwordx4 v244, s[52:55], s70 offen lds\n"
"s_add_u32 m0, s81, 28672\n"
"s_nop 0\n"
"buffer_load_dwordx4 v244, s[52:55], s71 offen lds\n"
"s_add_u32 s18, s18, 1\n"
"s_cmp_eq_u32 s18, 10\n"
"s_cselect_b32 s17, s20, s19\n"
"s_cselect_b32 s18, 0, s18\n"
"v_add_u32 v243, s17, v243\n"
"v_add_u32 v244, s17, v244\n"
"v_mov_b32 v247, s87\n"
"s_mul_i32 s26, s21, 5120\n"
"s_add_u32 s26, s26, s25\n"
"s_add_u32 s51, s26, 4096\n"
"s_add_u32 s21, s21, 1\n"
"s_cmp_eq_u32 s21, 10\n"
"s_cselect_b32 s21, 0, s21\n"
"s_mov_b32 s22, 65536\n"
"s_mov_b32 s23, 0\n"
"s_mov_b32 s24, 32768\n"
"L_p0%=:\n"
"v_mfma_f32_16x16x32_bf16 v[192:195], v[160:163], a[176:179], 0\n"
"ds_read_b64_tr_b16 v[176:177], v241 offset:0\n"
"s_add_u32 s83, s81, s24\n"
"v_mfma_f32_16x16x32_bf16 v[192:195], v[164:167], a[196:199], v[192:195]\n"
"ds_read_b64_tr_b16 v[178:179], v241 offset:2048\n"
"v_cvt_pk_bf16_f32 v32, v204, v205\n"
"v_mfma_f32_16x16x32_bf16 v[192:195], v[168:171], a[216:219], v[192:195]\n"
"ds_read_b64_tr_b16 v[180:181], v241 offset:4096\n"
"s_add_u32 m0, s83, 0\n"
"v_mfma_f32_16x16x32_bf16 v[192:195], v[172:175], a[236:239], v[192:195]\n"
"ds_read_b64_tr_b16 v[182:183], v241 offset:6144\n"
"v_cvt_pk_bf16_f32 v33, v206, v207\n"
"v_mfma_f32_16x16x32_bf16 v[196:199], v[160:163], a[180:183], 0\n"
"ds_read_b64_tr_b16 v[184:185], v241 offset:8192\n"
"v_cvt_pk_bf16_f32 v36, v208, v209\n"
"v_mfma_f32_16x16x32_bf16 v[196:199], v[164:167], a[200:203], v[196:199]\n"
"ds_read_b64_tr_b16 v[186:187], v241 offset:10240\n"
"v_cvt_pk_bf16_f32 v37, v210, v211\n"
"v_mfma_f32_16x16x32_bf16 v[196:199], v[168:171], a[220:223], v[196:199]\n"
"ds_read_b64_tr_b16 v[188:189], v241 offset:12288\n"
"v_cvt_pk_bf16_f32 v22, v212, v213\n"
"v_mfma_f32_16x16x32_bf16 v[196:199], v[172:175], a[240:243], v[196:199]\n"
"ds_read_b64_tr_b16 v[190:191], v241 offset:14336\n"
"v_cvt_pk_bf16_f32 v23, v214, v215\n"
"v_mfma_f32_16x16x32_bf16 v[200:203], v[160:163], a[184:187], 0\n"
"v_mfma_f32_16x16x32_bf16 v[200:203], v[164:167], a[204:207], v[200:203]\n"
"buffer_load_dwordx4 v243, s[52:55], 0 offen lds\n"
"v_mfma_f32_16x16x32_bf16 v[200:203], v[168:171], a[224:227], v[200:203]\n"
"s_add_u32 m0, s83, 4096\n"
"v_cvt_pk_bf16_f32 v26, v216, v217\n"
"v_mfma_f32_16x16x32_bf16 v[200:203], v[172:175], a[244:247], v[200:203]\n"
"buffer_store_dwordx4 v[20:23], v251, s[60:63], s26 offen sc1\n"
"v_mfma_f32_16x16x32_bf16 v[204:207], v[160:163], a[188:191], 0\n"
"v_mfma_f32_16x16x32_bf16 v[204:207], v[164:167], a[208:211], v[204:207]\n"
"buffer_load_dwordx4 v243, s[52:55], s69 offen lds\n"
"v_mfma_f32_16x16x32_bf16 v[204:207], v[168:171], a[228:231], v[204:207]\n"
"s_add_u32 m0, s83, 8192\n"
"v_cvt_pk_bf16_f32 v27, v218, v219\n"
"v_mfma_f32_16x16x32_bf16 v[204:207], v[172:175], a[248:251], v[204:207]\n"
"buffer_store_dwordx4 v[24:27], v251, s[60:63], s26 offen offset:1024 sc1\n"
"v_mfma_f32_16x16x32_bf16 v[208:211], v[160:163], a[192:195], 0\n"
"v_mfma_f32_16x16x32_bf16 v[208:211], v[164:167], a[212:215], v[208:211]\n"
"buffer_load_dwordx4 v243, s[52:55], s70 offen lds\n"
"v_mfma_f32_16x16x32_bf16 v[208:211], v[168:171], a[232:235], v[208:211]\n"
"v_mfma_f32_16x16x32_bf16 v[208:211], v[172:175], a[252:255], v[208:211]\n"
"s_waitcnt lgkmcnt(0)\n"
"s_add_u32 m0, s83, 12288\n"
"v_mfma_f32_16x16x32_bf16 v[212:215], v[176:179], a[176:179], 0\n"
"ds_read_b64_tr_b16 v[160:161], v245 offset:0\n"
"v_cvt_pk_bf16_f32 v30, v220, v221\n"
"v_mfma_f32_16x16x32_bf16 v[212:215], v[180:183], a[196:199], v[212:215]\n"
"ds_read_b64_tr_b16 v[162:163], v245 offset:2048\n"
"v_cvt_pk_bf16_f32 v31, v222, v223\n"
"v_mfma_f32_16x16x32_bf16 v[212:215], v[184:187], a[216:219], v[212:215]\n"
"ds_read_b64_tr_b16 v[164:165], v245 offset:4096\n"
"v_cvt_pk_bf16_f32 v34, v224, v225\n"
"v_mfma_f32_16x16x32_bf16 v[212:215], v[188:191], a[236:239], v[212:215]\n"
"ds_read_b64_tr_b16 v[166:167], v245 offset:6144\n"
"v_cvt_pk_bf16_f32 v35, v226, v227\n"
"v_mfma_f32_16x16x32_bf16 v[216:219], v[176:179], a[180:183], 0\n"
"ds_read_b64_tr_b16 v[168:169], v245 offset:8192\n"
"v_cvt_pk_bf16_f32 v38, v228, v229\n"
"v_mfma_f32_16x16x32_bf16 v[216:219], v[180:183], a[200:203], v[216:219]\n"
"ds_read_b64_tr_b16 v[170:171], v245 offset:10240\n"
"v_cvt_pk_bf16_f32 v39, v230, v231\n"
"v_mfma_f32_16x16x32_bf16 v[216:219], v[184:187], a[220:223], v[216:219]\n"
"ds_read_b64_tr_b16 v[172:173], v245 offset:12288\n"
"s_mov_b64 s[36:37], %[out]\n"
"v_mfma_f32_16x16x32_bf16 v[216:219], v[188:191], a[240:243], v[216:219]\n"
"ds_read_b64_tr_b16 v[174:175], v245 offset:14336\n"
"s_and_b32 s37, s37, 0xffff\n"
"v_mfma_f32_16x16x32_bf16 v[220:223], v[176:179], a[184:187], 0\n"
"s_add_u32 s86, s86, 64\n"
"s_cmp_eq_u32 s86, 1073741824\n"
"s_cselect_b32 s86, 0, s86\n"
"v_mfma_f32_16x16x32_bf16 v[220:223], v[180:183], a[204:207], v[220:223]\n"
"buffer_load_dwordx4 v243, s[52:55], s71 offen lds\n"
"v_mfma_f32_16x16x32_bf16 v[220:223], v[184:187], a[224:227], v[220:223]\n"
"s_add_u32 m0, s83, 16384\n"
"s_mov_b32 s38, 204800\n"
"s_mov_b32 s39, 0x20000\n"
"v_mfma_f32_16x16x32_bf16 v[220:223], v[188:191], a[244:247], v[220:223]\n"
"buffer_store_dwordx4 v[28:31], v251, s[60:63], s26 offen offset:2048 sc1\n"
"v_mfma_f32_16x16x32_bf16 v[224:227], v[176:179], a[188:191], 0\n"
"s_mov_b32 s40, 204800\n"
"s_mov_b32 s41, 409600\n"
"s_mov_b32 s42, 614400\n"
"v_mfma_f32_16x16x32_bf16 v[224:227], v[180:183], a[208:211], v[224:227]\n"
"buffer_load_dwordx4 v244, s[52:55], 0 offen lds\n"
"v_mfma_f32_16x16x32_bf16 v[224:227], v[184:187], a[228:231], v[224:227]\n"
"s_add_u32 m0, s83, 20480\n"
"s_mov_b32 s43, 819200\n"
"s_mov_b32 s44, 1024000\n"
"v_mfma_f32_16x16x32_bf16 v[224:227], v[188:191], a[248:251], v[224:227]\n"
"buffer_store_dwordx4 v[32:35], v251, s[60:63], s26 offen offset:3072 sc1\n"
"v_mfma_f32_16x16x32_bf16 v[228:231], v[176:179], a[192:195], 0\n"
"s_mov_b32 s45, 1228800\n"
"s_mov_b32 s46, 1433600\n"
"v_mfma_f32_16x16x32_bf16 v[228:231], v[180:183], a[212:215], v[228:231]\n"
"buffer_load_dwordx4 v244, s[52:55], s69 offen lds\n"
"v_mfma_f32_16x16x32_bf16 v[228:231], v[184:187], a[232:235], v[228:231]\n"
"v_lshrrev_b32 v140, 4, v251\n"
"v_mov_b32 v248, s30\n"
"v_mfma_f32_16x16x32_bf16 v[228:231], v[188:191], a[252:255], v[228:231]\n"
"s_waitcnt lgkmcnt(0)\n"
"s_add_u32 m0, s83, 24576\n"
"v_mfma_f32_16x16x32_bf16 v[192:195], v[160:163], a[0:3], v[192:195]\n"
"ds_read_b64_tr_b16 v[176:177], v246 offset:0\n"
"v_mov_b32 v249, s31\n"
"v_mfma_f32_16x16x32_bf16 v[192:195], v[164:167], a[20:23], v[192:195]\n"
"ds_read_b64_tr_b16 v[178:179], v246 offset:2048\n"
"v_and_b32 v250, 7, v140\n"
"v_mfma_f32_16x16x32_bf16 v[192:195], v[168:171], a[40:43], v[192:195]\n"
"ds_read_b64_tr_b16 v[180:181], v246 offset:4096\n"
"v_lshlrev_b32 v250, 3, v250\n"
"v_mfma_f32_16x16x32_bf16 v[192:195], v[172:175], a[60:63], v[192:195]\n"
"ds_read_b64_tr_b16 v[182:183], v246 offset:6144\n"
"v_add_u32 v250, 128, v250\n"
"v_mfma_f32_16x16x32_bf16 v[196:199], v[160:163], a[4:7], v[196:199]\n"
"ds_read_b64_tr_b16 v[184:185], v246 offset:8192\n"
"s_bfe_u32 s93, %[info], 0x30003\n"
"v_mfma_f32_16x16x32_bf16 v[196:199], v[164:167], a[24:27], v[196:199]\n"
"ds_read_b64_tr_b16 v[186:187], v246 offset:10240\n"
"s_lshl_b32 s84, s93, 2\n"
"v_mfma_f32_16x16x32_bf16 v[196:199], v[168:171], a[44:47], v[196:199]\n"
"ds_read_b64_tr_b16 v[188:189], v246 offset:12288\n"
"s_lshl_b32 s93, s93, 3\n"
"v_mfma_f32_16x16x32_bf16 v[196:199], v[172:175], a[64:67], v[196:199]\n"
"ds_read_b64_tr_b16 v[190:191], v246 offset:14336\n"
"s_add_u32 s93, s93, 128\n"
"v_mfma_f32_16x16x32_bf16 v[200:203], v[160:163], a[8:11], v[200:203]\n"
"s_add_u32 s84, s84, s73\n"
"v_mfma_f32_16x16x32_bf16 v[200:203], v[164:167], a[28:31], v[200:203]\n"
"buffer_load_dwordx4 v244, s[52:55], s70 offen lds\n"
"v_mfma_f32_16x16x32_bf16 v[200:203], v[168:171], a[48:51], v[200:203]\n"
"s_add_u32 m0, s83, 28672\n"
"v_mfma_f32_16x16x32_bf16 v[200:203], v[172:175], a[68:71], v[200:203]\n"
"buffer_store_dwordx4 v[36:39], v251, s[60:63], s51 offen sc1\n"
"v_mfma_f32_16x16x32_bf16 v[204:207], v[160:163], a[12:15], v[204:207]\n"
"s_cmp_eq_u32 s73, 0\n"
"s_cselect_b32 s93, s93, s87\n"
"v_mfma_f32_16x16x32_bf16 v[204:207], v[164:167], a[32:35], v[204:207]\n"
"buffer_load_dwordx4 v244, s[52:55], s71 offen lds\n"
"v_mfma_f32_16x16x32_bf16 v[204:207], v[168:171], a[52:55], v[204:207]\n"
"v_mov_b32 v141, s87\n"
"v_mov_b32 v142, s93\n"
"v_mfma_f32_16x16x32_bf16 v[204:207], v[172:175], a[72:75], v[204:207]\n"
"v_mov_b32 v145, 8\n"
"v_cmp_eq_u32 vcc, 0, v140\n"
"v_mfma_f32_16x16x32_bf16 v[208:211], v[160:163], a[16:19], v[208:211]\n"
"v_add_u32 v240, s23, v242\n"
"s_mov_b64 s[90:91], 0\n"
"v_mfma_f32_16x16x32_bf16 v[208:211], v[164:167], a[36:39], v[208:211]\n"
"s_mov_b32 s88, 576\n"
"v_cndmask_b32 v247, v141, v142, vcc\n"
"v_mfma_f32_16x16x32_bf16 v[208:211], v[168:171], a[56:59], v[208:211]\n"
"v_xor_b32 v241, 32, v240\n"
"v_cndmask_b32 v75, v141, v145, vcc\n"
"v_mfma_f32_16x16x32_bf16 v[208:211], v[172:175], a[76:79], v[208:211]\n"
"s_waitcnt vmcnt(13) lgkmcnt(0)\n"
"s_barrier\n"
"v_mfma_f32_16x16x32_bf16 v[212:215], v[176:179], a[0:3], v[212:215]\n"
"buffer_store_dwordx2 v[248:249], v247, s[56:59], s86 offen sc1\n"
"v_mfma_f32_16x16x32_bf16 v[212:215], v[180:183], a[20:23], v[212:215]\n"
"ds_read_b64_tr_b16 v[160:161], v240 offset:0\n"
"v_xor_b32 v245, 16384, v240\n"
"v_mfma_f32_16x16x32_bf16 v[212:215], v[184:187], a[40:43], v[212:215]\n"
"ds_read_b64_tr_b16 v[162:163], v240 offset:2048\n"
"v_xor_b32 v246, 16416, v240\n"
"v_mfma_f32_16x16x32_bf16 v[212:215], v[188:191], a[60:63], v[212:215]\n"
"ds_read_b64_tr_b16 v[164:165], v240 offset:4096\n"
"v_add_u32 v243, s16, v243\n"
"v_mfma_f32_16x16x32_bf16 v[216:219], v[176:179], a[4:7], v[216:219]\n"
"ds_read_b64_tr_b16 v[166:167], v240 offset:6144\n"
"v_add_u32 v244, s16, v244\n"
"v_mfma_f32_16x16x32_bf16 v[216:219], v[180:183], a[24:27], v[216:219]\n"
"ds_read_b64_tr_b16 v[168:169], v240 offset:8192\n"
"v_and_b32 v141, 15, v140\n"
"v_mfma_f32_16x16x32_bf16 v[216:219], v[184:187], a[44:47], v[216:219]\n"
"ds_read_b64_tr_b16 v[170:171], v240 offset:10240\n"
"v_mul_u32_u24 v254, 1280, v141\n"
"v_mfma_f32_16x16x32_bf16 v[216:219], v[188:191], a[64:67], v[216:219]\n"
"ds_read_b64_tr_b16 v[172:173], v240 offset:12288\n"
"v_bfe_u32 v142, v140, 4, 1\n"
"v_mfma_f32_16x16x32_bf16 v[220:223], v[176:179], a[8:11], v[220:223]\n"
"ds_read_b64_tr_b16 v[174:175], v240 offset:14336\n"
"v_lshl_add_u32 v254, v142, 5, v254\n"
"v_mfma_f32_16x16x32_bf16 v[220:223], v[180:183], a[28:31], v[220:223]\n"
"s_mov_b32 s79, s22\n"
"s_mov_b32 s22, s23\n"
"s_mov_b32 s23, s24\n"
"v_mfma_f32_16x16x32_bf16 v[220:223], v[184:187], a[48:51], v[220:223]\n"
"s_mov_b32 s24, s79\n"
"v_lshrrev_b32 v142, 5, v140\n"
"v_mfma_f32_16x16x32_bf16 v[220:223], v[188:191], a[68:71], v[220:223]\n"
"v_lshl_add_u32 v254, v142, 4, v254\n"
"v_lshrrev_b32 v78, 1, v251\n"
"v_mfma_f32_16x16x32_bf16 v[224:227], v[176:179], a[12:15], v[224:227]\n"
"v_add_u32 v78, 128, v78\n"
"v_and_b32 v142, 1, v140\n"
"v_mfma_f32_16x16x32_bf16 v[224:227], v[180:183], a[32:35], v[224:227]\n"
"v_lshlrev_b32 v142, 4, v142\n"
"v_mov_b32 v144, 0x3f80\n"
"v_mfma_f32_16x16x32_bf16 v[224:227], v[184:187], a[52:55], v[224:227]\n"
"v_lshlrev_b32 v144, v142, v144\n"
"v_lshrrev_b32 v142, 2, v141\n"
"v_mfma_f32_16x16x32_bf16 v[224:227], v[188:191], a[72:75], v[224:227]\n"
"v_lshrrev_b32 v145, 4, v140\n"
"v_cmp_eq_u32 vcc, v142, v145\n"
"v_mfma_f32_16x16x32_bf16 v[228:231], v[176:179], a[16:19], v[228:231]\n"
"v_mov_b32 v143, 0\n"
"v_and_b32 v142, 2, v140\n"
"v_mfma_f32_16x16x32_bf16 v[228:231], v[180:183], a[36:39], v[228:231]\n"
"v_cndmask_b32 v144, v143, v144, vcc\n"
"v_cmp_eq_u32 vcc, 0, v142\n"
"v_mfma_f32_16x16x32_bf16 v[228:231], v[184:187], a[56:59], v[228:231]\n"
"v_mov_b32 v68, 0\n"
"v_mov_b32 v69, 0\n"
"v_mfma_f32_16x16x32_bf16 v[228:231], v[188:191], a[76:79], v[228:231]\n"
"s_waitcnt lgkmcnt(0)\n"
"v_cndmask_b32 v70, v143, v144, vcc\n"
"v_mfma_f32_16x16x32_bf16 v[192:195], v[160:163], v[0:3], v[192:195]\n"
"ds_read_b64_tr_b16 v[176:177], v241 offset:0\n"
"s_add_u32 s83, s81, s24\n"
"v_mfma_f32_16x16x32_bf16 v[192:195], v[164:167], v[40:43], v[192:195]\n"
"ds_read_b64_tr_b16 v[178:179], v241 offset:2048\n"
"v_cndmask_b32 v71, v144, v143, vcc\n"
"v_mfma_f32_16x16x32_bf16 v[192:195], v[168:171], v[80:83], v[192:195]\n"
"ds_read_b64_tr_b16 v[180:181], v241 offset:4096\n"
"s_add_u32 m0, s83, 0\n"
"v_mfma_f32_16x16x32_bf16 v[192:195], v[172:175], v[120:123], v[192:195]\n"
"ds_read_b64_tr_b16 v[182:183], v241 offset:6144\n"
"v_mov_b32 v72, 0\n"
"v_mfma_f32_16x16x32_bf16 v[196:199], v[160:163], v[4:7], v[196:199]\n"
"ds_read_b64_tr_b16 v[184:185], v241 offset:8192\n"
"v_mov_b32 v73, 0\n"
"v_mfma_f32_16x16x32_bf16 v[196:199], v[164:167], v[44:47], v[196:199]\n"
"ds_read_b64_tr_b16 v[186:187], v241 offset:10240\n"
"v_mfma_f32_16x16x32_bf16 v[196:199], v[168:171], v[84:87], v[196:199]\n"
"ds_read_b64_tr_b16 v[188:189], v241 offset:12288\n"
"v_mfma_f32_16x16x32_bf16 v[196:199], v[172:175], v[124:127], v[196:199]\n"
"ds_read_b64_tr_b16 v[190:191], v241 offset:14336\n"
"v_mfma_f32_16x16x32_bf16 v[200:203], v[160:163], v[8:11], v[200:203]\n"
"v_mfma_f32_16x16x32_bf16 v[200:203], v[164:167], v[48:51], v[200:203]\n"
"buffer_load_dwordx4 v243, s[52:55], 0 offen lds\n"
"v_mfma_f32_16x16x32_bf16 v[200:203], v[168:171], v[88:91], v[200:203]\n"
"s_add_u32 m0, s83, 4096\n"
"v_mfma_f32_16x16x32_bf16 v[200:203], v[172:175], v[128:131], v[200:203]\n"
"v_mfma_f32_16x16x32_bf16 v[204:207], v[160:163], v[12:15], v[204:207]\n"
"v_mfma_f32_16x16x32_bf16 v[204:207], v[164:167], v[52:55], v[204:207]\n"
"buffer_load_dwordx4 v243, s[52:55], s69 offen lds\n"
"v_mfma_f32_16x16x32_bf16 v[204:207], v[168:171], v[92:95], v[204:207]\n"
"s_add_u32 m0, s83, 8192\n"
"v_mfma_f32_16x16x32_bf16 v[204:207], v[172:175], v[132:135], v[204:207]\n"
"v_mfma_f32_16x16x32_bf16 v[208:211], v[160:163], v[16:19], v[208:211]\n"
"v_mfma_f32_16x16x32_bf16 v[208:211], v[164:167], v[56:59], v[208:211]\n"
"buffer_load_dwordx4 v243, s[52:55], s70 offen lds\n"
"v_mfma_f32_16x16x32_bf16 v[208:211], v[168:171], v[96:99], v[208:211]\n"
"v_mfma_f32_16x16x32_bf16 v[208:211], v[172:175], v[136:139], v[208:211]\n"
"s_waitcnt lgkmcnt(0)\n"
"s_add_u32 m0, s83, 12288\n"
"v_mfma_f32_16x16x32_bf16 v[212:215], v[176:179], v[0:3], v[212:215]\n"
"ds_read_b64_tr_b16 v[160:161], v245 offset:0\n"
"s_mul_i32 s26, s21, 5120\n"
"v_mfma_f32_16x16x32_bf16 v[212:215], v[180:183], v[40:43], v[212:215]\n"
"ds_read_b64_tr_b16 v[162:163], v245 offset:2048\n"
"s_add_u32 s26, s26, s25\n"
"v_mfma_f32_16x16x32_bf16 v[212:215], v[184:187], v[80:83], v[212:215]\n"
"ds_read_b64_tr_b16 v[164:165], v245 offset:4096\n"
"s_add_u32 s51, s26, 4096\n"
"v_mfma_f32_16x16x32_bf16 v[212:215], v[188:191], v[120:123], v[212:215]\n"
"ds_read_b64_tr_b16 v[166:167], v245 offset:6144\n"
"s_add_u32 s21, s21, 1\n"
"v_mfma_f32_16x16x32_bf16 v[216:219], v[176:179], v[4:7], v[216:219]\n"
"ds_read_b64_tr_b16 v[168:169], v245 offset:8192\n"
"s_sub_u32 s75, s75, 1\n"
"v_mfma_f32_16x16x32_bf16 v[216:219], v[180:183], v[44:47], v[216:219]\n"
"ds_read_b64_tr_b16 v[170:171], v245 offset:10240\n"
"v_mfma_f32_16x16x32_bf16 v[216:219], v[184:187], v[84:87], v[216:219]\n"
"ds_read_b64_tr_b16 v[172:173], v245 offset:12288\n"
"v_mfma_f32_16x16x32_bf16 v[216:219], v[188:191], v[124:127], v[216:219]\n"
"ds_read_b64_tr_b16 v[174:175], v245 offset:14336\n"
"v_mfma_f32_16x16x32_bf16 v[220:223], v[176:179], v[8:11], v[220:223]\n"
"v_mfma_f32_16x16x32_bf16 v[220:223], v[180:183], v[48:51], v[220:223]\n"
"buffer_load_dwordx4 v243, s[52:55], s71 offen lds\n"
"v_mfma_f32_16x16x32_bf16 v[220:223], v[184:187], v[88:91], v[220:223]\n"
"s_add_u32 m0, s83, 16384\n"
"v_mfma_f32_16x16x32_bf16 v[220:223], v[188:191], v[128:131], v[220:223]\n"
"v_mfma_f32_16x16x32_bf16 v[224:227], v[176:179], v[12:15], v[224:227]\n"
"v_mfma_f32_16x16x32_bf16 v[224:227], v[180:183], v[52:55], v[224:227]\n"
"buffer_load_dwordx4 v244, s[52:55], 0 offen lds\n"
"v_mfma_f32_16x16x32_bf16 v[224:227], v[184:187], v[92:95], v[224:227]\n"
"s_add_u32 m0, s83, 20480\n"
"v_mfma_f32_16x16x32_bf16 v[224:227], v[188:191], v[132:135], v[224:227]\n"
"v_mfma_f32_16x16x32_bf16 v[228:231], v[176:179], v[16:19], v[228:231]\n"
"v_mfma_f32_16x16x32_bf16 v[228:231], v[180:183], v[56:59], v[228:231]\n"
"buffer_load_dwordx4 v244, s[52:55], s69 offen lds\n"
"v_mfma_f32_16x16x32_bf16 v[228:231], v[184:187], v[96:99], v[228:231]\n"
"v_mfma_f32_16x16x32_bf16 v[228:231], v[188:191], v[136:139], v[228:231]\n"
"s_waitcnt lgkmcnt(0)\n"
"s_add_u32 m0, s83, 24576\n"
"v_mfma_f32_16x16x32_bf16 v[192:195], v[160:163], a[80:83], v[192:195]\n"
"ds_read_b64_tr_b16 v[176:177], v246 offset:0\n"
"v_mfma_f32_16x16x32_bf16 v[192:195], v[164:167], a[100:103], v[192:195]\n"
"ds_read_b64_tr_b16 v[178:179], v246 offset:2048\n"
"v_mfma_f32_16x16x32_bf16 v[192:195], v[168:171], a[120:123], v[192:195]\n"
"ds_read_b64_tr_b16 v[180:181], v246 offset:4096\n"
"v_mfma_f32_16x16x32_bf16 v[192:195], v[172:175], a[140:143], v[192:195]\n"
"ds_read_b64_tr_b16 v[182:183], v246 offset:6144\n"
"v_mfma_f32_16x16x32_bf16 v[196:199], v[160:163], a[84:87], v[196:199]\n"
"ds_read_b64_tr_b16 v[184:185], v246 offset:8192\n"
"v_mfma_f32_16x16x32_bf16 v[196:199], v[164:167], a[104:107], v[196:199]\n"
"ds_read_b64_tr_b16 v[186:187], v246 offset:10240\n"
"v_mfma_f32_16x16x32_bf16 v[196:199], v[168:171], a[124:127], v[196:199]\n"
"ds_read_b64_tr_b16 v[188:189], v246 offset:12288\n"
"v_mfma_f32_16x16x32_bf16 v[196:199], v[172:175], a[144:147], v[196:199]\n"
"ds_read_b64_tr_b16 v[190:191], v246 offset:14336\n"
"v_mfma_f32_16x16x32_bf16 v[200:203], v[160:163], a[88:91], v[200:203]\n"
"s_cmp_eq_u32 s21, 10\n"
"s_cselect_b32 s21, 0, s21\n"
"v_mfma_f32_16x16x32_bf16 v[200:203], v[164:167], a[108:111], v[200:203]\n"
"buffer_load_dwordx4 v244, s[52:55], s70 offen lds\n"
"v_mfma_f32_16x16x32_bf16 v[200:203], v[168:171], a[128:131], v[200:203]\n"
"s_add_u32 m0, s83, 28672\n"
"v_mfma_f32_16x16x32_bf16 v[200:203], v[172:175], a[148:151], v[200:203]\n"
"v_mfma_f32_16x16x32_bf16 v[204:207], v[160:163], a[92:95], v[204:207]\n"
"v_mfma_f32_16x16x32_bf16 v[204:207], v[164:167], a[112:115], v[204:207]\n"
"buffer_load_dwordx4 v244, s[52:55], s71 offen lds\n"
"v_mfma_f32_16x16x32_bf16 v[204:207], v[168:171], a[132:135], v[204:207]\n"
"v_mfma_f32_16x16x32_bf16 v[204:207], v[172:175], a[152:155], v[204:207]\n"
"v_mfma_f32_16x16x32_bf16 v[208:211], v[160:163], a[96:99], v[208:211]\n"
"v_add_u32 v240, s23, v242\n"
"v_mfma_f32_16x16x32_bf16 v[208:211], v[164:167], a[116:119], v[208:211]\n"
"v_mfma_f32_16x16x32_bf16 v[208:211], v[168:171], a[136:139], v[208:211]\n"
"v_xor_b32 v241, 32, v240\n"
"v_mfma_f32_16x16x32_bf16 v[208:211], v[172:175], a[156:159], v[208:211]\n"
"s_waitcnt vmcnt(9) lgkmcnt(0)\n"
"s_barrier\n"
"v_mfma_f32_16x16x32_bf16 v[212:215], v[176:179], a[80:83], v[212:215]\n"
"v_mfma_f32_16x16x32_bf16 v[212:215], v[180:183], a[100:103], v[212:215]\n"
"ds_read_b64_tr_b16 v[160:161], v240 offset:0\n"
"v_xor_b32 v245, 16384, v240\n"
"v_mfma_f32_16x16x32_bf16 v[212:215], v[184:187], a[120:123], v[212:215]\n"
"ds_read_b64_tr_b16 v[162:163], v240 offset:2048\n"
"v_xor_b32 v246, 16416, v240\n"
"v_mfma_f32_16x16x32_bf16 v[212:215], v[188:191], a[140:143], v[212:215]\n"
"ds_read_b64_tr_b16 v[164:165], v240 offset:4096\n"
"s_add_u32 s18, s18, 1\n"
"v_mfma_f32_16x16x32_bf16 v[216:219], v[176:179], a[84:87], v[216:219]\n"
"ds_read_b64_tr_b16 v[166:167], v240 offset:6144\n"
"v_cvt_pk_bf16_f32 v20, v192, v193\n"
"v_mfma_f32_16x16x32_bf16 v[216:219], v[180:183], a[104:107], v[216:219]\n"
"ds_read_b64_tr_b16 v[168:169], v240 offset:8192\n"
"v_cvt_pk_bf16_f32 v21, v194, v195\n"
"v_mfma_f32_16x16x32_bf16 v[216:219], v[184:187], a[124:127], v[216:219]\n"
"ds_read_b64_tr_b16 v[170:171], v240 offset:10240\n"
"v_cvt_pk_bf16_f32 v24, v196, v197\n"
"v_mfma_f32_16x16x32_bf16 v[216:219], v[188:191], a[144:147], v[216:219]\n"
"ds_read_b64_tr_b16 v[172:173], v240 offset:12288\n"
"v_cvt_pk_bf16_f32 v25, v198, v199\n"
"v_mfma_f32_16x16x32_bf16 v[220:223], v[176:179], a[88:91], v[220:223]\n"
"ds_read_b64_tr_b16 v[174:175], v240 offset:14336\n"
"v_cvt_pk_bf16_f32 v28, v200, v201\n"
"v_mfma_f32_16x16x32_bf16 v[220:223], v[180:183], a[108:111], v[220:223]\n"
"s_cmp_eq_u32 s18, 10\n"
"s_cselect_b32 s17, s20, s19\n"
"s_cselect_b32 s18, 0, s18\n"
"v_mfma_f32_16x16x32_bf16 v[220:223], v[184:187], a[128:131], v[220:223]\n"
"v_add_u32 v243, s17, v243\n"
"v_add_u32 v244, s17, v244\n"
"v_mfma_f32_16x16x32_bf16 v[220:223], v[188:191], a[148:151], v[220:223]\n"
"s_mov_b32 s79, s22\n"
"s_mov_b32 s22, s23\n"
"s_mov_b32 s23, s24\n"
"v_mfma_f32_16x16x32_bf16 v[224:227], v[176:179], a[92:95], v[224:227]\n"
"s_mov_b32 s24, s79\n"
"v_mfma_f32_16x16x32_bf16 v[224:227], v[180:183], a[112:115], v[224:227]\n"
"v_mfma_f32_16x16x32_bf16 v[224:227], v[184:187], a[132:135], v[224:227]\n"
"v_mfma_f32_16x16x32_bf16 v[224:227], v[188:191], a[152:155], v[224:227]\n"
"v_mfma_f32_16x16x32_bf16 v[228:231], v[176:179], a[96:99], v[228:231]\n"
"v_mfma_f32_16x16x32_bf16 v[228:231], v[180:183], a[116:119], v[228:231]\n"
"v_mfma_f32_16x16x32_bf16 v[228:231], v[184:187], a[136:139], v[228:231]\n"
"v_mfma_f32_16x16x32_bf16 v[228:231], v[188:191], a[156:159], v[228:231]\n"
"s_waitcnt lgkmcnt(0)\n"
"v_cvt_pk_bf16_f32 v29, v202, v203\n"
"s_cmp_lg_u32 s75, 0\n"
"s_cbranch_scc1 L_p0%=\n"
"buffer_load_dwordx2 v[76:77], v78, s[56:59], 0 offen sc1\n"
"s_nop 7\n"
"s_waitcnt lgkmcnt(0)\n"
"v_cvt_pk_bf16_f32 v32, v204, v205\n"
"v_cvt_pk_bf16_f32 v33, v206, v207\n"
"v_cvt_pk_bf16_f32 v36, v208, v209\n"
"v_cvt_pk_bf16_f32 v37, v210, v211\n"
"v_cvt_pk_bf16_f32 v22, v212, v213\n"
"v_cvt_pk_bf16_f32 v23, v214, v215\n"
"v_cvt_pk_bf16_f32 v26, v216, v217\n"
"buffer_store_dwordx4 v[20:23], v251, s[60:63], s26 offen sc1\n"
"v_cvt_pk_bf16_f32 v27, v218, v219\n"
"buffer_store_dwordx4 v[24:27], v251, s[60:63], s26 offen offset:1024 sc1\n"
"v_cvt_pk_bf16_f32 v30, v220, v221\n"
"v_cvt_pk_bf16_f32 v31, v222, v223\n"
"v_cvt_pk_bf16_f32 v34, v224, v225\n"
"v_cvt_pk_bf16_f32 v35, v226, v227\n"
"v_cvt_pk_bf16_f32 v38, v228, v229\n"
"v_cvt_pk_bf16_f32 v39, v230, v231\n"
"s_add_u32 s86, s86, 64\n"
"s_cmp_eq_u32 s86, 1073741824\n"
"s_cselect_b32 s86, 0, s86\n"
"buffer_store_dwordx4 v[28:31], v251, s[60:63], s26 offen offset:2048 sc1\n"
"buffer_store_dwordx4 v[32:35], v251, s[60:63], s26 offen offset:3072 sc1\n"
"buffer_store_dwordx4 v[36:39], v251, s[60:63], s51 offen sc1\n"
"L_p_end%=:\n"
"s_mov_b32 s24, 0\n"
"s_waitcnt vmcnt(5)\n"
"L_g0%=:\n"
"v_cmp_eq_u64 vcc, v[76:77], v[248:249]\n"
"s_nop 1\n"
"s_and_b32 s21, vcc_lo, 0xff\n"
"s_cmp_eq_u32 s21, 255\n"
"s_cbranch_scc0 L_spg%=\n"
"L_bkg%=:\n"
"v_mov_b32 v74, 1\n"
"s_nop 0\n"
"buffer_atomic_add v74, v75, s[56:59], 0 offen sc0 sc1\n"
"buffer_load_dwordx2 v[76:77], v78, s[56:59], 0 offen sc1\n"
"buffer_load_dwordx2 v[100:101], v250, s[56:59], s88 offen sc1\n"
"s_waitcnt vmcnt(0)\n"
"s_barrier\n"
"s_mov_b32 s86, 576\n"
"buffer_store_dwordx2 v[248:249], v247, s[56:59], s86 offen sc1\n"
"L_dyn%=:\n"
"v_readfirstlane_b32 s20, v74\n"
"v_cmp_eq_u64 vcc, v[76:77], v[248:249]\n"
"s_nop 2\n"
"s_sub_u32 s20, s20, s29\n"
"s_cmp_ge_u32 s20, 80\n"
"s_cbranch_scc1 L_stat%=\n"
"s_lshl_b32 s20, s20, 1\n"
"s_mul_i32 s21, s20, 205\n"
"s_lshr_b32 s21, s21, 12\n"
"s_lshl_b32 s21, s21, 3\n"
"L_chk%=:\n"
"s_lshr_b64 s[22:23], vcc, s21\n"
"s_and_b32 s22, s22, 255\n"
"s_cmp_eq_u32 s22, 255\n"
"s_cbranch_scc0 L_spd%=\n"
"L_bkd%=:\n"
"s_add_u32 s76, s20, 0\n"
"s_mul_i32 s93, s76, 205\n"
"s_lshr_b32 s93, s93, 12\n"
"s_add_u32 s89, s93, s68\n"
"s_mul_i32 s93, s93, 20\n"
"s_cmp_lt_u32 s76, 200\n"
"s_cselect_b32 s95, 20, 0\n"
"s_sub_u32 s76, s76, s93\n"
"s_cmp_ge_u32 s89, 10\n"
"s_cselect_b32 s93, 10, 0\n"
"s_sub_u32 s89, s89, s93\n"
"s_mul_i32 s93, s76, 13\n"
"s_lshr_b32 s93, s93, 6\n"
"s_mul_i32 s77, s93, 45\n"
"s_add_u32 s77, s77, s76\n"
"s_mul_i32 s85, s89, 5\n"
"s_add_u32 s77, s77, s85\n"
"s_lshl_b32 s77, s77, 10\n"
"s_mul_i32 s85, s93, -5\n"
"s_add_u32 s85, s85, s76\n"
"s_mul_i32 s85, s85, 20480\n"
"s_bitcmp1_b32 s93, 0\n"
"s_cselect_b32 s94, 102400, 0\n"
"s_add_u32 s85, s85, s94\n"
"s_lshr_b32 s93, s93, 1\n"
"s_lshl_b32 s93, s93, 6\n"
"s_add_u32 s85, s85, s93\n"
"s_lshl_b32 s93, s89, 7\n"
"s_add_u32 s85, s85, s93\n"
"s_cmp_lt_u32 s76, s95\n"
"s_cselect_b32 s77, s77, s87\n"
"s_cselect_b32 s85, s85, s87\n"
"s_mov_b32 s16, s85\n"
"v_add_u32 v255, s77, v251\n"
"buffer_load_dwordx4 a[0:3], v255, s[60:63], 0 offen sc1\n"
"buffer_load_dwordx4 a[4:7], v255, s[60:63], s40 offen sc1\n"
"buffer_load_dwordx4 a[8:11], v255, s[60:63], s41 offen sc1\n"
"buffer_load_dwordx4 a[12:15], v255, s[60:63], s42 offen sc1\n"
"buffer_load_dwordx4 a[16:19], v255, s[60:63], s43 offen sc1\n"
"buffer_load_dwordx4 a[20:23], v255, s[60:63], s44 offen sc1\n"
"buffer_load_dwordx4 a[24:27], v255, s[60:63], s45 offen sc1\n"
"buffer_load_dwordx4 a[28:31], v255, s[60:63], s46 offen sc1\n"
"s_add_u32 s76, s20, 1\n"
"s_mul_i32 s93, s76, 205\n"
"s_lshr_b32 s93, s93, 12\n"
"s_add_u32 s89, s93, s68\n"
"s_mul_i32 s93, s93, 20\n"
"s_cmp_lt_u32 s76, 200\n"
"s_cselect_b32 s95, 20, 0\n"
"s_sub_u32 s76, s76, s93\n"
"s_cmp_ge_u32 s89, 10\n"
"s_cselect_b32 s93, 10, 0\n"
"s_sub_u32 s89, s89, s93\n"
"s_mul_i32 s93, s76, 13\n"
"s_lshr_b32 s93, s93, 6\n"
"s_mul_i32 s77, s93, 45\n"
"s_add_u32 s77, s77, s76\n"
"s_mul_i32 s85, s89, 5\n"
"s_add_u32 s77, s77, s85\n"
"s_lshl_b32 s77, s77, 10\n"
"s_mul_i32 s85, s93, -5\n"
"s_add_u32 s85, s85, s76\n"
"s_mul_i32 s85, s85, 20480\n"
"s_bitcmp1_b32 s93, 0\n"
"s_cselect_b32 s94, 102400, 0\n"
"s_add_u32 s85, s85, s94\n"
"s_lshr_b32 s93, s93, 1\n"
"s_lshl_b32 s93, s93, 6\n"
"s_add_u32 s85, s85, s93\n"
"s_lshl_b32 s93, s89, 7\n"
"s_add_u32 s85, s85, s93\n"
"s_cmp_lt_u32 s76, s95\n"
"s_cselect_b32 s77, s77, s87\n"
"s_cselect_b32 s85, s85, s87\n"
"s_mov_b32 s17, s85\n"
"v_add_u32 v255, s77, v251\n"
"buffer_load_dwordx4 a[32:35], v255, s[60:63], 0 offen sc1\n"
"buffer_load_dwordx4 a[36:39], v255, s[60:63], s40 offen sc1\n"
"buffer_load_dwordx4 a[40:43], v255, s[60:63], s41 offen sc1\n"
"buffer_load_dwordx4 a[44:47], v255, s[60:63], s42 offen sc1\n"
"buffer_load_dwordx4 a[48:51], v255, s[60:63], s43 offen sc1\n"
"buffer_load_dwordx4 a[52:55], v255, s[60:63], s44 offen sc1\n"
"buffer_load_dwordx4 a[56:59], v255, s[60:63], s45 offen sc1\n"
"buffer_load_dwordx4 a[60:63], v255, s[60:63], s46 offen sc1\n"
"v_mov_b32 v74, 1\n"
"s_nop 0\n"
"buffer_atomic_add v74, v75, s[56:59], 0 offen sc0 sc1\n"
"buffer_load_dwordx2 v[76:77], v78, s[56:59], 0 offen sc1\n"
"buffer_load_dwordx2 v[100:101], v250, s[56:59], s88 offen sc1\n"
"s_waitcnt vmcnt(0)\n"
"v_mfma_f32_16x16x32_bf16 v[60:63], v[70:73], a[0:3], 0\n"
"v_mfma_f32_16x16x32_bf16 v[60:63], v[70:73], a[4:7], v[60:63]\n"
"v_mfma_f32_16x16x32_bf16 v[60:63], v[70:73], a[8:11], v[60:63]\n"
"v_mfma_f32_16x16x32_bf16 v[60:63], v[70:73], a[12:15], v[60:63]\n"
"v_mfma_f32_16x16x32_bf16 v[60:63], v[70:73], a[16:19], v[60:63]\n"
"v_mfma_f32_16x16x32_bf16 v[60:63], v[70:73], a[20:23], v[60:63]\n"
"v_mfma_f32_16x16x32_bf16 v[60:63], v[70:73], a[24:27], v[60:63]\n"
"v_mfma_f32_16x16x32_bf16 v[60:63], v[70:73], a[28:31], v[60:63]\n"
"v_mfma_f32_16x16x32_bf16 v[64:67], v[68:71], a[0:3], 0\n"
"v_mfma_f32_16x16x32_bf16 v[64:67], v[68:71], a[4:7], v[64:67]\n"
"v_mfma_f32_16x16x32_bf16 v[64:67], v[68:71], a[8:11], v[64:67]\n"
"v_mfma_f32_16x16x32_bf16 v[64:67], v[68:71], a[12:15], v[64:67]\n"
"v_mfma_f32_16x16x32_bf16 v[64:67], v[68:71], a[16:19], v[64:67]\n"
"v_mfma_f32_16x16x32_bf16 v[64:67], v[68:71], a[20:23], v[64:67]\n"
"v_mfma_f32_16x16x32_bf16 v[64:67], v[68:71], a[24:27], v[64:67]\n"
"v_mfma_f32_16x16x32_bf16 v[64:67], v[68:71], a[28:31], v[64:67]\n"
"s_nop 7\n"
"v_cvt_pk_bf16_f32 v60, v60, v61\n"
"v_cvt_pk_bf16_f32 v61, v62, v63\n"
"v_cvt_pk_bf16_f32 v62, v64, v65\n"
"v_cvt_pk_bf16_f32 v63, v66, v67\n"
"s_nop 1\n"
"v_permlane16_swap_b32 v60, v62\n"
"v_permlane16_swap_b32 v61, v63\n"
"buffer_store_dwordx4 v[60:63], v254, s[36:39], s16 offen nt\n"
"s_nop 1\n"
"v_mfma_f32_16x16x32_bf16 v[60:63], v[70:73], a[32:35], 0\n"
"v_mfma_f32_16x16x32_bf16 v[60:63], v[70:73], a[36:39], v[60:63]\n"
"v_mfma_f32_16x16x32_bf16 v[60:63], v[70:73], a[40:43], v[60:63]\n"
"v_mfma_f32_16x16x32_bf16 v[60:63], v[70:73], a[44:47], v[60:63]\n"
"v_mfma_f32_16x16x32_bf16 v[60:63], v[70:73], a[48:51], v[60:63]\n"
"v_mfma_f32_16x16x32_bf16 v[60:63], v[70:73], a[52:55], v[60:63]\n"
"v_mfma_f32_16x16x32_bf16 v[60:63], v[70:73], a[56:59], v[60:63]\n"
"v_mfma_f32_16x16x32_bf16 v[60:63], v[70:73], a[60:63], v[60:63]\n"
"v_mfma_f32_16x16x32_bf16 v[64:67], v[68:71], a[32:35], 0\n"
"v_mfma_f32_16x16x32_bf16 v[64:67], v[68:71], a[36:39], v[64:67]\n"
"v_mfma_f32_16x16x32_bf16 v[64:67], v[68:71], a[40:43], v[64:67]\n"
"v_mfma_f32_16x16x32_bf16 v[64:67], v[68:71], a[44:47], v[64:67]\n"
"v_mfma_f32_16x16x32_bf16 v[64:67], v[68:71], a[48:51], v[64:67]\n"
"v_mfma_f32_16x16x32_bf16 v[64:67], v[68:71], a[52:55], v[64:67]\n"
"v_mfma_f32_16x16x32_bf16 v[64:67], v[68:71], a[56:59], v[64:67]\n"
"v_mfma_f32_16x16x32_bf16 v[64:67], v[68:71], a[60:63], v[64:67]\n"
"s_nop 7\n"
"v_cvt_pk_bf16_f32 v60, v60, v61\n"
"v_cvt_pk_bf16_f32 v61, v62, v63\n"
"v_cvt_pk_bf16_f32 v62, v64, v65\n"
"v_cvt_pk_bf16_f32 v63, v66, v67\n"
"s_nop 1\n"
"v_permlane16_swap_b32 v60, v62\n"
"v_permlane16_swap_b32 v61, v63\n"
"buffer_store_dwordx4 v[60:63], v254, s[36:39], s17 offen nt\n"
"s_nop 1\n"
"s_branch L_dyn%=\n"
"L_stat%=:\n"
"s_add_u32 s76, s84, 160\n"
"s_mul_i32 s93, s76, 205\n"
"s_lshr_b32 s93, s93, 12\n"
"s_add_u32 s89, s93, s68\n"
"s_mul_i32 s93, s93, 20\n"
"s_cmp_lt_u32 s76, 200\n"
"s_cselect_b32 s95, 20, 0\n"
"s_sub_u32 s76, s76, s93\n"
"s_cmp_ge_u32 s89, 10\n"
"s_cselect_b32 s93, 10, 0\n"
"s_sub_u32 s89, s89, s93\n"
"s_mul_i32 s93, s76, 13\n"
"s_lshr_b32 s93, s93, 6\n"
"s_mul_i32 s77, s93, 45\n"
"s_add_u32 s77, s77, s76\n"
"s_mul_i32 s85, s89, 5\n"
"s_add_u32 s77, s77, s85\n"
"s_lshl_b32 s77, s77, 10\n"
"s_mul_i32 s85, s93, -5\n"
"s_add_u32 s85, s85, s76\n"
"s_mul_i32 s85, s85, 20480\n"
"s_bitcmp1_b32 s93, 0\n"
"s_cselect_b32 s94, 102400, 0\n"
"s_add_u32 s85, s85, s94\n"
"s_lshr_b32 s93, s93, 1\n"
"s_lshl_b32 s93, s93, 6\n"
"s_add_u32 s85, s85, s93\n"
"s_lshl_b32 s93, s89, 7\n"
"s_add_u32 s85, s85, s93\n"
"s_cmp_lt_u32 s76, s95\n"
"s_cselect_b32 s77, s77, s87\n"
"s_cselect_b32 s85, s85, s87\n"
"s_mov_b32 s18, s85\n"
"v_cmp_eq_u64 vcc, v[100:101], v[248:249]\n"
"s_nop 1\n"
"s_or_b64 vcc, vcc, s[90:91]\n"
"s_cmp_eq_u64 vcc, -1\n"
"s_cbranch_scc0 L_spinf1%=\n"
"L_backf1%=:\n"
"v_add_u32 v255, s77, v251\n"
"buffer_load_dwordx4 a[160:163], v255, s[60:63], 0 offen sc1\n"
"buffer_load_dwordx4 a[164:167], v255, s[60:63], s40 offen sc1\n"
"buffer_load_dwordx4 a[168:171], v255, s[60:63], s41 offen sc1\n"
"buffer_load_dwordx4 a[172:175], v255, s[60:63], s42 offen sc1\n"
"buffer_load_dwordx4 a[176:179], v255, s[60:63], s43 offen sc1\n"
"buffer_load_dwordx4 a[180:183], v255, s[60:63], s44 offen sc1\n"
"buffer_load_dwordx4 a[184:187], v255, s[60:63], s45 offen sc1\n"
"buffer_load_dwordx4 a[188:191], v255, s[60:63], s46 offen sc1\n"
"s_add_u32 s76, s84, 192\n"
"s_mul_i32 s93, s76, 205\n"
"s_lshr_b32 s93, s93, 12\n"
"s_add_u32 s89, s93, s68\n"
"s_mul_i32 s93, s93, 20\n"
"s_cmp_lt_u32 s76, 200\n"
"s_cselect_b32 s95, 20, 0\n"
"s_sub_u32 s76, s76, s93\n"
"s_cmp_ge_u32 s89, 10\n"
"s_cselect_b32 s93, 10, 0\n"
"s_sub_u32 s89, s89, s93\n"
"s_mul_i32 s93, s76, 13\n"
"s_lshr_b32 s93, s93, 6\n"
"s_mul_i32 s77, s93, 45\n"
"s_add_u32 s77, s77, s76\n"
"s_mul_i32 s85, s89, 5\n"
"s_add_u32 s77, s77, s85\n"
"s_lshl_b32 s77, s77, 10\n"
"s_mul_i32 s85, s93, -5\n"
"s_add_u32 s85, s85, s76\n"
"s_mul_i32 s85, s85, 20480\n"
"s_bitcmp1_b32 s93, 0\n"
"s_cselect_b32 s94, 102400, 0\n"
"s_add_u32 s85, s85, s94\n"
"s_lshr_b32 s93, s93, 1\n"
"s_lshl_b32 s93, s93, 6\n"
"s_add_u32 s85, s85, s93\n"
"s_lshl_b32 s93, s89, 7\n"
"s_add_u32 s85, s85, s93\n"
"s_cmp_lt_u32 s76, s95\n"
"s_cselect_b32 s77, s77, s87\n"
"s_cselect_b32 s85, s85, s87\n"
"s_mov_b32 s19, s85\n"
"v_add_u32 v255, s77, v251\n"
"buffer_load_dwordx4 a[192:195], v255, s[60:63], 0 offen sc1\n"
"buffer_load_dwordx4 a[196:199], v255, s[60:63], s40 offen sc1\n"
"buffer_load_dwordx4 a[200:203], v255, s[60:63], s41 offen sc1\n"
"buffer_load_dwordx4 a[204:207], v255, s[60:63], s42 offen sc1\n"
"buffer_load_dwordx4 a[208:211], v255, s[60:63], s43 offen sc1\n"
"buffer_load_dwordx4 a[212:215], v255, s[60:63], s44 offen sc1\n"
"buffer_load_dwordx4 a[216:219], v255, s[60:63], s45 offen sc1\n"
"buffer_load_dwordx4 a[220:223], v255, s[60:63], s46 offen sc1\n"
"s_waitcnt vmcnt(8)\n"
"v_mfma_f32_16x16x32_bf16 v[60:63], v[70:73], a[160:163], 0\n"
"v_mfma_f32_16x16x32_bf16 v[60:63], v[70:73], a[164:167], v[60:63]\n"
"v_mfma_f32_16x16x32_bf16 v[60:63], v[70:73], a[168:171], v[60:63]\n"
"v_mfma_f32_16x16x32_bf16 v[60:63], v[70:73], a[172:175], v[60:63]\n"
"v_mfma_f32_16x16x32_bf16 v[60:63], v[70:73], a[176:179], v[60:63]\n"
"v_mfma_f32_16x16x32_bf16 v[60:63], v[70:73], a[180:183], v[60:63]\n"
"v_mfma_f32_16x16x32_bf16 v[60:63], v[70:73], a[184:187], v[60:63]\n"
"v_mfma_f32_16x16x32_bf16 v[60:63], v[70:73], a[188:191], v[60:63]\n"
"v_mfma_f32_16x16x32_bf16 v[64:67], v[68:71], a[160:163], 0\n"
"v_mfma_f32_16x16x32_bf16 v[64:67], v[68:71], a[164:167], v[64:67]\n"
"v_mfma_f32_16x16x32_bf16 v[64:67], v[68:71], a[168:171], v[64:67]\n"
"v_mfma_f32_16x16x32_bf16 v[64:67], v[68:71], a[172:175], v[64:67]\n"
"v_mfma_f32_16x16x32_bf16 v[64:67], v[68:71], a[176:179], v[64:67]\n"
"v_mfma_f32_16x16x32_bf16 v[64:67], v[68:71], a[180:183], v[64:67]\n"
"v_mfma_f32_16x16x32_bf16 v[64:67], v[68:71], a[184:187], v[64:67]\n"
"v_mfma_f32_16x16x32_bf16 v[64:67], v[68:71], a[188:191], v[64:67]\n"
"s_nop 7\n"
"v_cvt_pk_bf16_f32 v60, v60, v61\n"
"v_cvt_pk_bf16_f32 v61, v62, v63\n"
"v_cvt_pk_bf16_f32 v62, v64, v65\n"
"v_cvt_pk_bf16_f32 v63, v66, v67\n"
"s_nop 1\n"
"v_permlane16_swap_b32 v60, v62\n"
"v_permlane16_swap_b32 v61, v63\n"
"buffer_store_dwordx4 v[60:63], v254, s[36:39], s18 offen nt\n"
"s_nop 1\n"
"s_waitcnt vmcnt(1)\n"
"v_mfma_f32_16x16x32_bf16 v[60:63], v[70:73], a[192:195], 0\n"
"v_mfma_f32_16x16x32_bf16 v[60:63], v[70:73], a[196:199], v[60:63]\n"
"v_mfma_f32_16x16x32_bf16 v[60:63], v[70:73], a[200:203], v[60:63]\n"
"v_mfma_f32_16x16x32_bf16 v[60:63], v[70:73], a[204:207], v[60:63]\n"
"v_mfma_f32_16x16x32_bf16 v[60:63], v[70:73], a[208:211], v[60:63]\n"
"v_mfma_f32_16x16x32_bf16 v[60:63], v[70:73], a[212:215], v[60:63]\n"
"v_mfma_f32_16x16x32_bf16 v[60:63], v[70:73], a[216:219], v[60:63]\n"
"v_mfma_f32_16x16x32_bf16 v[60:63], v[70:73], a[220:223], v[60:63]\n"
"v_mfma_f32_16x16x32_bf16 v[64:67], v[68:71], a[192:195], 0\n"
"v_mfma_f32_16x16x32_bf16 v[64:67], v[68:71], a[196:199], v[64:67]\n"
"v_mfma_f32_16x16x32_bf16 v[64:67], v[68:71], a[200:203], v[64:67]\n"
"v_mfma_f32_16x16x32_bf16 v[64:67], v[68:71], a[204:207], v[64:67]\n"
"v_mfma_f32_16x16x32_bf16 v[64:67], v[68:71], a[208:211], v[64:67]\n"
"v_mfma_f32_16x16x32_bf16 v[64:67], v[68:71], a[212:215], v[64:67]\n"
"v_mfma_f32_16x16x32_bf16 v[64:67], v[68:71], a[216:219], v[64:67]\n"
"v_mfma_f32_16x16x32_bf16 v[64:67], v[68:71], a[220:223], v[64:67]\n"
"s_nop 7\n"
"v_cvt_pk_bf16_f32 v60, v60, v61\n"
"v_cvt_pk_bf16_f32 v61, v62, v63\n"
"v_cvt_pk_bf16_f32 v62, v64, v65\n"
"v_cvt_pk_bf16_f32 v63, v66, v67\n"
"s_nop 1\n"
"v_permlane16_swap_b32 v60, v62\n"
"v_permlane16_swap_b32 v61, v63\n"
"buffer_store_dwordx4 v[60:63], v254, s[36:39], s19 offen nt\n"
"s_nop 1\n"
"s_bfe_u32 s93, %[info], 0x30003\n"
"s_cmp_eq_u32 s93, 0\n"
"s_cselect_b32 s93, -128, s87\n"
"v_add_u32 v247, s93, v247\n"
"s_nop 0\n"
"buffer_store_dwordx2 v[248:249], v247, s[56:59], 0 offen sc1\n"
"s_branch L_end%=\n"
"L_slow0%=:\n"
"s_waitcnt vmcnt(0) lgkmcnt(0)\n"
"v_and_b32 v254, 240, v251\n"
"v_lshlrev_b32 v254, 4, v254\n"
"v_lshrrev_b32 v255, 8, v251\n"
"v_lshl_add_u32 v254, v255, 4, v254\n"
"v_lshlrev_b32 v255, 10, v255\n"
"v_bfe_u32 v160, v251, 4, 4\n"
"v_lshl_add_u32 v255, v160, 1, v255\n"
"v_mov_b32 v220, 0xff800000\n"
"v_mov_b32 v224, 0xff800000\n"
"v_mov_b32 v228, 0xff800000\n"
"v_mov_b32 v232, 0xff800000\n"
"v_mov_b32 v236, 0xff800000\n"
"s_lshr_b32 s78, s73, 1\n"
"s_lshl_b32 s78, s78, 17\n"
"s_add_u32 s78, s78, s84\n"
"s_add_u32 s83, s78, 4096\n"
"s_mov_b32 s75, 16\n"
"L_sla0%=:\n"
"buffer_load_dwordx4 a[112:115], v254, s[44:47], s78 offen\n"
"buffer_load_dwordx4 a[116:119], v254, s[44:47], s78 offen offset:64\n"
"buffer_load_dwordx4 a[120:123], v254, s[44:47], s78 offen offset:128\n"
"buffer_load_dwordx4 a[124:127], v254, s[44:47], s78 offen offset:192\n"
"buffer_load_dwordx4 a[128:131], v254, s[44:47], s83 offen\n"
"buffer_load_dwordx4 a[132:135], v254, s[44:47], s83 offen offset:64\n"
"buffer_load_dwordx4 a[136:139], v254, s[44:47], s83 offen offset:128\n"
"buffer_load_dwordx4 a[140:143], v254, s[44:47], s83 offen offset:192\n"
"s_waitcnt vmcnt(0)\n"
"v_mfma_f32_16x16x32_bf16 v[160:163], a[112:115], a[0:3], 0\n"
"v_mfma_f32_16x16x32_bf16 v[160:163], a[116:119], a[20:23], v[160:163]\n"
"v_mfma_f32_16x16x32_bf16 v[160:163], a[120:123], a[40:43], v[160:163]\n"
"v_mfma_f32_16x16x32_bf16 v[160:163], a[124:127], a[60:63], v[160:163]\n"
"v_mfma_f32_16x16x32_bf16 v[164:167], a[128:131], a[0:3], 0\n"
"v_mfma_f32_16x16x32_bf16 v[164:167], a[132:135], a[20:23], v[164:167]\n"
"v_mfma_f32_16x16x32_bf16 v[164:167], a[136:139], a[40:43], v[164:167]\n"
"v_mfma_f32_16x16x32_bf16 v[164:167], a[140:143], a[60:63], v[164:167]\n"
"v_mfma_f32_16x16x32_bf16 v[168:171], a[112:115], a[4:7], 0\n"
"v_mfma_f32_16x16x32_bf16 v[168:171], a[116:119], a[24:27], v[168:171]\n"
"v_mfma_f32_16x16x32_bf16 v[168:171], a[120:123], a[44:47], v[168:171]\n"
"v_mfma_f32_16x16x32_bf16 v[168:171], a[124:127], a[64:67], v[168:171]\n"
"v_mfma_f32_16x16x32_bf16 v[172:175], a[128:131], a[4:7], 0\n"
"v_mfma_f32_16x16x32_bf16 v[172:175], a[132:135], a[24:27], v[172:175]\n"
"v_mfma_f32_16x16x32_bf16 v[172:175], a[136:139], a[44:47], v[172:175]\n"
"v_mfma_f32_16x16x32_bf16 v[172:175], a[140:143], a[64:67], v[172:175]\n"
"v_mfma_f32_16x16x32_bf16 v[176:179], a[112:115], a[8:11], 0\n"
"v_mfma_f32_16x16x32_bf16 v[176:179], a[116:119], a[28:31], v[176:179]\n"
"v_mfma_f32_16x16x32_bf16 v[176:179], a[120:123], a[48:51], v[176:179]\n"
"v_mfma_f32_16x16x32_bf16 v[176:179], a[124:127], a[68:71], v[176:179]\n"
"v_mfma_f32_16x16x32_bf16 v[180:183], a[128:131], a[8:11], 0\n"
"v_mfma_f32_16x16x32_bf16 v[180:183], a[132:135], a[28:31], v[180:183]\n"
"v_mfma_f32_16x16x32_bf16 v[180:183], a[136:139], a[48:51], v[180:183]\n"
"v_mfma_f32_16x16x32_bf16 v[180:183], a[140:143], a[68:71], v[180:183]\n"
"v_mfma_f32_16x16x32_bf16 v[184:187], a[112:115], a[12:15], 0\n"
"v_mfma_f32_16x16x32_bf16 v[184:187], a[116:119], a[32:35], v[184:187]\n"
"v_mfma_f32_16x16x32_bf16 v[184:187], a[120:123], a[52:55], v[184:187]\n"
"v_mfma_f32_16x16x32_bf16 v[184:187], a[124:127], a[72:75], v[184:187]\n"
"v_mfma_f32_16x16x32_bf16 v[188:191], a[128:131], a[12:15], 0\n"
"v_mfma_f32_16x16x32_bf16 v[188:191], a[132:135], a[32:35], v[188:191]\n"
"v_mfma_f32_16x16x32_bf16 v[188:191], a[136:139], a[52:55], v[188:191]\n"
"v_mfma_f32_16x16x32_bf16 v[188:191], a[140:143], a[72:75], v[188:191]\n"
"v_mfma_f32_16x16x32_bf16 v[192:195], a[112:115], a[16:19], 0\n"
"v_mfma_f32_16x16x32_bf16 v[192:195], a[116:119], a[36:39], v[192:195]\n"
"v_mfma_f32_16x16x32_bf16 v[192:195], a[120:123], a[56:59], v[192:195]\n"
"v_mfma_f32_16x16x32_bf16 v[192:195], a[124:127], a[76:79], v[192:195]\n"
"v_mfma_f32_16x16x32_bf16 v[196:199], a[128:131], a[16:19], 0\n"
"v_mfma_f32_16x16x32_bf16 v[196:199], a[132:135], a[36:39], v[196:199]\n"
"v_mfma_f32_16x16x32_bf16 v[196:199], a[136:139], a[56:59], v[196:199]\n"
"v_mfma_f32_16x16x32_bf16 v[196:199], a[140:143], a[76:79], v[196:199]\n"
"s_nop 7\n"
"v_max3_f32 v220, v220, v160, v161\n"
"v_max3_f32 v220, v220, v162, v163\n"
"v_max3_f32 v220, v220, v164, v165\n"
"v_max3_f32 v220, v220, v166, v167\n"
"v_max3_f32 v224, v224, v168, v169\n"
"v_max3_f32 v224, v224, v170, v171\n"
"v_max3_f32 v224, v224, v172, v173\n"
"v_max3_f32 v224, v224, v174, v175\n"
"v_max3_f32 v228, v228, v176, v177\n"
"v_max3_f32 v228, v228, v178, v179\n"
"v_max3_f32 v228, v228, v180, v181\n"
"v_max3_f32 v228, v228, v182, v183\n"
"v_max3_f32 v232, v232, v184, v185\n"
"v_max3_f32 v232, v232, v186, v187\n"
"v_max3_f32 v232, v232, v188, v189\n"
"v_max3_f32 v232, v232, v190, v191\n"
"v_max3_f32 v236, v236, v192, v193\n"
"v_max3_f32 v236, v236, v194, v195\n"
"v_max3_f32 v236, v236, v196, v197\n"
"v_max3_f32 v236, v236, v198, v199\n"
"s_add_u32 s78, s78, 8192\n"
"s_add_u32 s83, s83, 8192\n"
"s_sub_u32 s75, s75, 1\n"
"s_cmp_lg_u32 s75, 0\n"
"s_cbranch_scc1 L_sla0%=\n"
"v_mov_b32 v221, v220\n"
"s_nop 1\n"
"v_permlane32_swap_b32 v221, v220\n"
"v_max_f32 v220, v220, v221\n"
"v_mov_b32 v221, v220\n"
"s_nop 1\n"
"v_permlane16_swap_b32 v221, v220\n"
"v_max_f32 v220, v220, v221\n"
"v_sub_f32 v223, 0, v220\n"
"v_sub_f32 v222, 0, v220\n"
"v_sub_f32 v221, 0, v220\n"
"v_sub_f32 v220, 0, v220\n"
"v_mov_b32 v225, v224\n"
"s_nop 1\n"
"v_permlane32_swap_b32 v225, v224\n"
"v_max_f32 v224, v224, v225\n"
"v_mov_b32 v225, v224\n"
"s_nop 1\n"
"v_permlane16_swap_b32 v225, v224\n"
"v_max_f32 v224, v224, v225\n"
"v_sub_f32 v227, 0, v224\n"
"v_sub_f32 v226, 0, v224\n"
"v_sub_f32 v225, 0, v224\n"
"v_sub_f32 v224, 0, v224\n"
"v_mov_b32 v229, v228\n"
"s_nop 1\n"
"v_permlane32_swap_b32 v229, v228\n"
"v_max_f32 v228, v228, v229\n"
"v_mov_b32 v229, v228\n"
"s_nop 1\n"
"v_permlane16_swap_b32 v229, v228\n"
"v_max_f32 v228, v228, v229\n"
"v_sub_f32 v231, 0, v228\n"
"v_sub_f32 v230, 0, v228\n"
"v_sub_f32 v229, 0, v228\n"
"v_sub_f32 v228, 0, v228\n"
"v_mov_b32 v233, v232\n"
"s_nop 1\n"
"v_permlane32_swap_b32 v233, v232\n"
"v_max_f32 v232, v232, v233\n"
"v_mov_b32 v233, v232\n"
"s_nop 1\n"
"v_permlane16_swap_b32 v233, v232\n"
"v_max_f32 v232, v232, v233\n"
"v_sub_f32 v235, 0, v232\n"
"v_sub_f32 v234, 0, v232\n"
"v_sub_f32 v233, 0, v232\n"
"v_sub_f32 v232, 0, v232\n"
"v_mov_b32 v237, v236\n"
"s_nop 1\n"
"v_permlane32_swap_b32 v237, v236\n"
"v_max_f32 v236, v236, v237\n"
"v_mov_b32 v237, v236\n"
"s_nop 1\n"
"v_permlane16_swap_b32 v237, v236\n"
"v_max_f32 v236, v236, v237\n"
"v_sub_f32 v239, 0, v236\n"
"v_sub_f32 v238, 0, v236\n"
"v_sub_f32 v237, 0, v236\n"
"v_sub_f32 v236, 0, v236\n"
"v_mov_b64 v[0:1], 0\n"
"v_mov_b64 v[2:3], 0\n"
"v_mov_b64 v[4:5], 0\n"
"v_mov_b64 v[6:7], 0\n"
"v_mov_b64 v[8:9], 0\n"
"v_mov_b64 v[10:11], 0\n"
"v_mov_b64 v[12:13], 0\n"
"v_mov_b64 v[14:15], 0\n"
"v_mov_b64 v[16:17], 0\n"
"v_mov_b64 v[18:19], 0\n"
"v_mov_b64 v[20:21], 0\n"
"v_mov_b64 v[22:23], 0\n"
"v_mov_b64 v[24:25], 0\n"
"v_mov_b64 v[26:27], 0\n"
"v_mov_b64 v[28:29], 0\n"
"v_mov_b64 v[30:31], 0\n"
"v_mov_b64 v[32:33], 0\n"
"v_mov_b64 v[34:35], 0\n"
"v_mov_b64 v[36:37], 0\n"
"v_mov_b64 v[38:39], 0\n"
"v_mov_b64 v[40:41], 0\n"
"v_mov_b64 v[42:43], 0\n"
"v_mov_b64 v[44:45], 0\n"
"v_mov_b64 v[46:47], 0\n"
"v_mov_b64 v[48:49], 0\n"
"v_mov_b64 v[50:51], 0\n"
"v_mov_b64 v[52:53], 0\n"
"v_mov_b64 v[54:55], 0\n"
"v_mov_b64 v[56:57], 0\n"
"v_mov_b64 v[58:59], 0\n"
"v_mov_b64 v[60:61], 0\n"
"v_mov_b64 v[62:63], 0\n"
"v_mov_b64 v[64:65], 0\n"
"v_mov_b64 v[66:67], 0\n"
"v_mov_b64 v[68:69], 0\n"
"v_mov_b64 v[70:71], 0\n"
"v_mov_b64 v[72:73], 0\n"
"v_mov_b64 v[74:75], 0\n"
"v_mov_b64 v[76:77], 0\n"
"v_mov_b64 v[78:79], 0\n"
"v_mov_b64 v[80:81], 0\n"
"v_mov_b64 v[82:83], 0\n"
"v_mov_b64 v[84:85], 0\n"
"v_mov_b64 v[86:87], 0\n"
"v_mov_b64 v[88:89], 0\n"
"v_mov_b64 v[90:91], 0\n"
"v_mov_b64 v[92:93], 0\n"
"v_mov_b64 v[94:95], 0\n"
"v_mov_b64 v[96:97], 0\n"
"v_mov_b64 v[98:99], 0\n"
"v_mov_b64 v[100:101], 0\n"
"v_mov_b64 v[102:103], 0\n"
"v_mov_b64 v[104:105], 0\n"
"v_mov_b64 v[106:107], 0\n"
"v_mov_b64 v[108:109], 0\n"
"v_mov_b64 v[110:111], 0\n"
"v_mov_b64 v[112:113], 0\n"
"v_mov_b64 v[114:115], 0\n"
"v_mov_b64 v[116:117], 0\n"
"v_mov_b64 v[118:119], 0\n"
"v_mov_b64 v[120:121], 0\n"
"v_mov_b64 v[122:123], 0\n"
"v_mov_b64 v[124:125], 0\n"
"v_mov_b64 v[126:127], 0\n"
"v_mov_b64 v[128:129], 0\n"
"v_mov_b64 v[130:131], 0\n"
"v_mov_b64 v[132:133], 0\n"
"v_mov_b64 v[134:135], 0\n"
"v_mov_b64 v[136:137], 0\n"
"v_mov_b64 v[138:139], 0\n"
"v_mov_b64 v[140:141], 0\n"
"v_mov_b64 v[142:143], 0\n"
"v_mov_b64 v[144:145], 0\n"
"v_mov_b64 v[146:147], 0\n"
"v_mov_b64 v[148:149], 0\n"
"v_mov_b64 v[150:151], 0\n"
"v_mov_b64 v[152:153], 0\n"
"v_mov_b64 v[154:155], 0\n"
"v_mov_b64 v[156:157], 0\n"
"v_mov_b64 v[158:159], 0\n"
"v_accvgpr_write_b32 a144, 0\n"
"v_accvgpr_write_b32 a145, 0\n"
"v_accvgpr_write_b32 a146, 0\n"
"v_accvgpr_write_b32 a147, 0\n"
"v_accvgpr_write_b32 a148, 0\n"
"v_accvgpr_write_b32 a149, 0\n"
"v_accvgpr_write_b32 a150, 0\n"
"v_accvgpr_write_b32 a151, 0\n"
"v_accvgpr_write_b32 a152, 0\n"
"v_accvgpr_write_b32 a153, 0\n"
"v_accvgpr_write_b32 a154, 0\n"
"v_accvgpr_write_b32 a155, 0\n"
"v_accvgpr_write_b32 a156, 0\n"
"v_accvgpr_write_b32 a157, 0\n"
"v_accvgpr_write_b32 a158, 0\n"
"v_accvgpr_write_b32 a159, 0\n"
"v_accvgpr_write_b32 a160, 0\n"
"v_accvgpr_write_b32 a161, 0\n"
"v_accvgpr_write_b32 a162, 0\n"
"v_accvgpr_write_b32 a163, 0\n"
"s_lshr_b32 s78, s73, 1\n"
"s_lshl_b32 s78, s78, 17\n"
"s_add_u32 s78, s78, s84\n"
"s_add_u32 s83, s78, 4096\n"
"s_mov_b32 s75, 16\n"
"L_slb0%=:\n"
"buffer_load_dwordx4 a[112:115], v254, s[44:47], s78 offen\n"
"buffer_load_dwordx4 a[116:119], v254, s[44:47], s78 offen offset:64\n"
"buffer_load_dwordx4 a[120:123], v254, s[44:47], s78 offen offset:128\n"
"buffer_load_dwordx4 a[124:127], v254, s[44:47], s78 offen offset:192\n"
"buffer_load_dwordx4 a[128:131], v254, s[44:47], s83 offen\n"
"buffer_load_dwordx4 a[132:135], v254, s[44:47], s83 offen offset:64\n"
"buffer_load_dwordx4 a[136:139], v254, s[44:47], s83 offen offset:128\n"
"buffer_load_dwordx4 a[140:143], v254, s[44:47], s83 offen offset:192\n"
"s_waitcnt vmcnt(0)\n"
"v_mfma_f32_16x16x32_bf16 v[160:163], a[112:115], a[0:3], v[220:223]\n"
"v_mfma_f32_16x16x32_bf16 v[160:163], a[116:119], a[20:23], v[160:163]\n"
"v_mfma_f32_16x16x32_bf16 v[160:163], a[120:123], a[40:43], v[160:163]\n"
"v_mfma_f32_16x16x32_bf16 v[160:163], a[124:127], a[60:63], v[160:163]\n"
"v_mfma_f32_16x16x32_bf16 v[164:167], a[128:131], a[0:3], v[220:223]\n"
"v_mfma_f32_16x16x32_bf16 v[164:167], a[132:135], a[20:23], v[164:167]\n"
"v_mfma_f32_16x16x32_bf16 v[164:167], a[136:139], a[40:43], v[164:167]\n"
"v_mfma_f32_16x16x32_bf16 v[164:167], a[140:143], a[60:63], v[164:167]\n"
"v_mfma_f32_16x16x32_bf16 v[168:171], a[112:115], a[4:7], v[224:227]\n"
"v_mfma_f32_16x16x32_bf16 v[168:171], a[116:119], a[24:27], v[168:171]\n"
"v_mfma_f32_16x16x32_bf16 v[168:171], a[120:123], a[44:47], v[168:171]\n"
"v_mfma_f32_16x16x32_bf16 v[168:171], a[124:127], a[64:67], v[168:171]\n"
"v_mfma_f32_16x16x32_bf16 v[172:175], a[128:131], a[4:7], v[224:227]\n"
"v_mfma_f32_16x16x32_bf16 v[172:175], a[132:135], a[24:27], v[172:175]\n"
"v_mfma_f32_16x16x32_bf16 v[172:175], a[136:139], a[44:47], v[172:175]\n"
"v_mfma_f32_16x16x32_bf16 v[172:175], a[140:143], a[64:67], v[172:175]\n"
"v_mfma_f32_16x16x32_bf16 v[176:179], a[112:115], a[8:11], v[228:231]\n"
"v_mfma_f32_16x16x32_bf16 v[176:179], a[116:119], a[28:31], v[176:179]\n"
"v_mfma_f32_16x16x32_bf16 v[176:179], a[120:123], a[48:51], v[176:179]\n"
"v_mfma_f32_16x16x32_bf16 v[176:179], a[124:127], a[68:71], v[176:179]\n"
"v_mfma_f32_16x16x32_bf16 v[180:183], a[128:131], a[8:11], v[228:231]\n"
"v_mfma_f32_16x16x32_bf16 v[180:183], a[132:135], a[28:31], v[180:183]\n"
"v_mfma_f32_16x16x32_bf16 v[180:183], a[136:139], a[48:51], v[180:183]\n"
"v_mfma_f32_16x16x32_bf16 v[180:183], a[140:143], a[68:71], v[180:183]\n"
"v_mfma_f32_16x16x32_bf16 v[184:187], a[112:115], a[12:15], v[232:235]\n"
"v_mfma_f32_16x16x32_bf16 v[184:187], a[116:119], a[32:35], v[184:187]\n"
"v_mfma_f32_16x16x32_bf16 v[184:187], a[120:123], a[52:55], v[184:187]\n"
"v_mfma_f32_16x16x32_bf16 v[184:187], a[124:127], a[72:75], v[184:187]\n"
"v_mfma_f32_16x16x32_bf16 v[188:191], a[128:131], a[12:15], v[232:235]\n"
"v_mfma_f32_16x16x32_bf16 v[188:191], a[132:135], a[32:35], v[188:191]\n"
"v_mfma_f32_16x16x32_bf16 v[188:191], a[136:139], a[52:55], v[188:191]\n"
"v_mfma_f32_16x16x32_bf16 v[188:191], a[140:143], a[72:75], v[188:191]\n"
"v_mfma_f32_16x16x32_bf16 v[192:195], a[112:115], a[16:19], v[236:239]\n"
"v_mfma_f32_16x16x32_bf16 v[192:195], a[116:119], a[36:39], v[192:195]\n"
"v_mfma_f32_16x16x32_bf16 v[192:195], a[120:123], a[56:59], v[192:195]\n"
"v_mfma_f32_16x16x32_bf16 v[192:195], a[124:127], a[76:79], v[192:195]\n"
"v_mfma_f32_16x16x32_bf16 v[196:199], a[128:131], a[16:19], v[236:239]\n"
"v_mfma_f32_16x16x32_bf16 v[196:199], a[132:135], a[36:39], v[196:199]\n"
"v_mfma_f32_16x16x32_bf16 v[196:199], a[136:139], a[56:59], v[196:199]\n"
"v_mfma_f32_16x16x32_bf16 v[196:199], a[140:143], a[76:79], v[196:199]\n"
"s_nop 7\n"
"v_exp_f32 v160, v160\n"
"v_exp_f32 v161, v161\n"
"v_exp_f32 v162, v162\n"
"v_exp_f32 v163, v163\n"
"v_exp_f32 v164, v164\n"
"v_exp_f32 v165, v165\n"
"v_exp_f32 v166, v166\n"
"v_exp_f32 v167, v167\n"
"v_exp_f32 v168, v168\n"
"v_exp_f32 v169, v169\n"
"v_exp_f32 v170, v170\n"
"v_exp_f32 v171, v171\n"
"v_exp_f32 v172, v172\n"
"v_exp_f32 v173, v173\n"
"v_exp_f32 v174, v174\n"
"v_exp_f32 v175, v175\n"
"v_exp_f32 v176, v176\n"
"v_exp_f32 v177, v177\n"
"v_exp_f32 v178, v178\n"
"v_exp_f32 v179, v179\n"
"v_exp_f32 v180, v180\n"
"v_exp_f32 v181, v181\n"
"v_exp_f32 v182, v182\n"
"v_exp_f32 v183, v183\n"
"v_exp_f32 v184, v184\n"
"v_exp_f32 v185, v185\n"
"v_exp_f32 v186, v186\n"
"v_exp_f32 v187, v187\n"
"v_exp_f32 v188, v188\n"
"v_exp_f32 v189, v189\n"
"v_exp_f32 v190, v190\n"
"v_exp_f32 v191, v191\n"
"v_exp_f32 v192, v192\n"
"v_exp_f32 v193, v193\n"
"v_exp_f32 v194, v194\n"
"v_exp_f32 v195, v195\n"
"v_exp_f32 v196, v196\n"
"v_exp_f32 v197, v197\n"
"v_exp_f32 v198, v198\n"
"v_exp_f32 v199, v199\n"
"s_nop 0\n"
"v_cvt_pk_bf16_f32 v200, v160, v161\n"
"v_cvt_pk_bf16_f32 v201, v162, v163\n"
"v_cvt_pk_bf16_f32 v202, v164, v165\n"
"v_cvt_pk_bf16_f32 v203, v166, v167\n"
"v_cvt_pk_bf16_f32 v204, v168, v169\n"
"v_cvt_pk_bf16_f32 v205, v170, v171\n"
"v_cvt_pk_bf16_f32 v206, v172, v173\n"
"v_cvt_pk_bf16_f32 v207, v174, v175\n"
"v_cvt_pk_bf16_f32 v208, v176, v177\n"
"v_cvt_pk_bf16_f32 v209, v178, v179\n"
"v_cvt_pk_bf16_f32 v210, v180, v181\n"
"v_cvt_pk_bf16_f32 v211, v182, v183\n"
"v_cvt_pk_bf16_f32 v212, v184, v185\n"
"v_cvt_pk_bf16_f32 v213, v186, v187\n"
"v_cvt_pk_bf16_f32 v214, v188, v189\n"
"v_cvt_pk_bf16_f32 v215, v190, v191\n"
"v_cvt_pk_bf16_f32 v216, v192, v193\n"
"v_cvt_pk_bf16_f32 v217, v194, v195\n"
"v_cvt_pk_bf16_f32 v218, v196, v197\n"
"v_cvt_pk_bf16_f32 v219, v198, v199\n"
"buffer_load_ushort v164, v255, s[48:51], s78 offen offset:0\n"
"buffer_load_ushort v165, v255, s[48:51], s78 offen offset:256\n"
"buffer_load_ushort v166, v255, s[48:51], s78 offen offset:512\n"
"buffer_load_ushort v167, v255, s[48:51], s78 offen offset:768\n"
"buffer_load_ushort v168, v255, s[48:51], s83 offen offset:0\n"
"buffer_load_ushort v169, v255, s[48:51], s83 offen offset:256\n"
"buffer_load_ushort v170, v255, s[48:51], s83 offen offset:512\n"
"buffer_load_ushort v171, v255, s[48:51], s83 offen offset:768\n"
"s_waitcnt vmcnt(0)\n"
"v_lshl_or_b32 v160, v165, 16, v164\n"
"v_lshl_or_b32 v161, v167, 16, v166\n"
"v_lshl_or_b32 v162, v169, 16, v168\n"
"v_lshl_or_b32 v163, v171, 16, v170\n"
"s_nop 1\n"
"v_mfma_f32_16x16x32_bf16 v[0:3], v[160:163], v[200:203], v[0:3]\n"
"v_mfma_f32_16x16x32_bf16 v[4:7], v[160:163], v[204:207], v[4:7]\n"
"v_mfma_f32_16x16x32_bf16 v[8:11], v[160:163], v[208:211], v[8:11]\n"
"v_mfma_f32_16x16x32_bf16 v[12:15], v[160:163], v[212:215], v[12:15]\n"
"v_mfma_f32_16x16x32_bf16 v[16:19], v[160:163], v[216:219], v[16:19]\n"
"buffer_load_ushort v164, v255, s[48:51], s78 offen offset:32\n"
"buffer_load_ushort v165, v255, s[48:51], s78 offen offset:288\n"
"buffer_load_ushort v166, v255, s[48:51], s78 offen offset:544\n"
"buffer_load_ushort v167, v255, s[48:51], s78 offen offset:800\n"
"buffer_load_ushort v168, v255, s[48:51], s83 offen offset:32\n"
"buffer_load_ushort v169, v255, s[48:51], s83 offen offset:288\n"
"buffer_load_ushort v170, v255, s[48:51], s83 offen offset:544\n"
"buffer_load_ushort v171, v255, s[48:51], s83 offen offset:800\n"
"s_waitcnt vmcnt(0)\n"
"v_lshl_or_b32 v160, v165, 16, v164\n"
"v_lshl_or_b32 v161, v167, 16, v166\n"
"v_lshl_or_b32 v162, v169, 16, v168\n"
"v_lshl_or_b32 v163, v171, 16, v170\n"
"s_nop 1\n"
"v_mfma_f32_16x16x32_bf16 v[20:23], v[160:163], v[200:203], v[20:23]\n"
"v_mfma_f32_16x16x32_bf16 v[24:27], v[160:163], v[204:207], v[24:27]\n"
"v_mfma_f32_16x16x32_bf16 v[28:31], v[160:163], v[208:211], v[28:31]\n"
"v_mfma_f32_16x16x32_bf16 v[32:35], v[160:163], v[212:215], v[32:35]\n"
"v_mfma_f32_16x16x32_bf16 v[36:39], v[160:163], v[216:219], v[36:39]\n"
"buffer_load_ushort v164, v255, s[48:51], s78 offen offset:64\n"
"buffer_load_ushort v165, v255, s[48:51], s78 offen offset:320\n"
"buffer_load_ushort v166, v255, s[48:51], s78 offen offset:576\n"
"buffer_load_ushort v167, v255, s[48:51], s78 offen offset:832\n"
"buffer_load_ushort v168, v255, s[48:51], s83 offen offset:64\n"
"buffer_load_ushort v169, v255, s[48:51], s83 offen offset:320\n"
"buffer_load_ushort v170, v255, s[48:51], s83 offen offset:576\n"
"buffer_load_ushort v171, v255, s[48:51], s83 offen offset:832\n"
"s_waitcnt vmcnt(0)\n"
"v_lshl_or_b32 v160, v165, 16, v164\n"
"v_lshl_or_b32 v161, v167, 16, v166\n"
"v_lshl_or_b32 v162, v169, 16, v168\n"
"v_lshl_or_b32 v163, v171, 16, v170\n"
"s_nop 1\n"
"v_mfma_f32_16x16x32_bf16 v[40:43], v[160:163], v[200:203], v[40:43]\n"
"v_mfma_f32_16x16x32_bf16 v[44:47], v[160:163], v[204:207], v[44:47]\n"
"v_mfma_f32_16x16x32_bf16 v[48:51], v[160:163], v[208:211], v[48:51]\n"
"v_mfma_f32_16x16x32_bf16 v[52:55], v[160:163], v[212:215], v[52:55]\n"
"v_mfma_f32_16x16x32_bf16 v[56:59], v[160:163], v[216:219], v[56:59]\n"
"buffer_load_ushort v164, v255, s[48:51], s78 offen offset:96\n"
"buffer_load_ushort v165, v255, s[48:51], s78 offen offset:352\n"
"buffer_load_ushort v166, v255, s[48:51], s78 offen offset:608\n"
"buffer_load_ushort v167, v255, s[48:51], s78 offen offset:864\n"
"buffer_load_ushort v168, v255, s[48:51], s83 offen offset:96\n"
"buffer_load_ushort v169, v255, s[48:51], s83 offen offset:352\n"
"buffer_load_ushort v170, v255, s[48:51], s83 offen offset:608\n"
"buffer_load_ushort v171, v255, s[48:51], s83 offen offset:864\n"
"s_waitcnt vmcnt(0)\n"
"v_lshl_or_b32 v160, v165, 16, v164\n"
"v_lshl_or_b32 v161, v167, 16, v166\n"
"v_lshl_or_b32 v162, v169, 16, v168\n"
"v_lshl_or_b32 v163, v171, 16, v170\n"
"s_nop 1\n"
"v_mfma_f32_16x16x32_bf16 v[60:63], v[160:163], v[200:203], v[60:63]\n"
"v_mfma_f32_16x16x32_bf16 v[64:67], v[160:163], v[204:207], v[64:67]\n"
"v_mfma_f32_16x16x32_bf16 v[68:71], v[160:163], v[208:211], v[68:71]\n"
"v_mfma_f32_16x16x32_bf16 v[72:75], v[160:163], v[212:215], v[72:75]\n"
"v_mfma_f32_16x16x32_bf16 v[76:79], v[160:163], v[216:219], v[76:79]\n"
"buffer_load_ushort v164, v255, s[48:51], s78 offen offset:128\n"
"buffer_load_ushort v165, v255, s[48:51], s78 offen offset:384\n"
"buffer_load_ushort v166, v255, s[48:51], s78 offen offset:640\n"
"buffer_load_ushort v167, v255, s[48:51], s78 offen offset:896\n"
"buffer_load_ushort v168, v255, s[48:51], s83 offen offset:128\n"
"buffer_load_ushort v169, v255, s[48:51], s83 offen offset:384\n"
"buffer_load_ushort v170, v255, s[48:51], s83 offen offset:640\n"
"buffer_load_ushort v171, v255, s[48:51], s83 offen offset:896\n"
"s_waitcnt vmcnt(0)\n"
"v_lshl_or_b32 v160, v165, 16, v164\n"
"v_lshl_or_b32 v161, v167, 16, v166\n"
"v_lshl_or_b32 v162, v169, 16, v168\n"
"v_lshl_or_b32 v163, v171, 16, v170\n"
"s_nop 1\n"
"v_mfma_f32_16x16x32_bf16 v[80:83], v[160:163], v[200:203], v[80:83]\n"
"v_mfma_f32_16x16x32_bf16 v[84:87], v[160:163], v[204:207], v[84:87]\n"
"v_mfma_f32_16x16x32_bf16 v[88:91], v[160:163], v[208:211], v[88:91]\n"
"v_mfma_f32_16x16x32_bf16 v[92:95], v[160:163], v[212:215], v[92:95]\n"
"v_mfma_f32_16x16x32_bf16 v[96:99], v[160:163], v[216:219], v[96:99]\n"
"buffer_load_ushort v164, v255, s[48:51], s78 offen offset:160\n"
"buffer_load_ushort v165, v255, s[48:51], s78 offen offset:416\n"
"buffer_load_ushort v166, v255, s[48:51], s78 offen offset:672\n"
"buffer_load_ushort v167, v255, s[48:51], s78 offen offset:928\n"
"buffer_load_ushort v168, v255, s[48:51], s83 offen offset:160\n"
"buffer_load_ushort v169, v255, s[48:51], s83 offen offset:416\n"
"buffer_load_ushort v170, v255, s[48:51], s83 offen offset:672\n"
"buffer_load_ushort v171, v255, s[48:51], s83 offen offset:928\n"
"s_waitcnt vmcnt(0)\n"
"v_lshl_or_b32 v160, v165, 16, v164\n"
"v_lshl_or_b32 v161, v167, 16, v166\n"
"v_lshl_or_b32 v162, v169, 16, v168\n"
"v_lshl_or_b32 v163, v171, 16, v170\n"
"s_nop 1\n"
"v_mfma_f32_16x16x32_bf16 v[100:103], v[160:163], v[200:203], v[100:103]\n"
"v_mfma_f32_16x16x32_bf16 v[104:107], v[160:163], v[204:207], v[104:107]\n"
"v_mfma_f32_16x16x32_bf16 v[108:111], v[160:163], v[208:211], v[108:111]\n"
"v_mfma_f32_16x16x32_bf16 v[112:115], v[160:163], v[212:215], v[112:115]\n"
"v_mfma_f32_16x16x32_bf16 v[116:119], v[160:163], v[216:219], v[116:119]\n"
"buffer_load_ushort v164, v255, s[48:51], s78 offen offset:192\n"
"buffer_load_ushort v165, v255, s[48:51], s78 offen offset:448\n"
"buffer_load_ushort v166, v255, s[48:51], s78 offen offset:704\n"
"buffer_load_ushort v167, v255, s[48:51], s78 offen offset:960\n"
"buffer_load_ushort v168, v255, s[48:51], s83 offen offset:192\n"
"buffer_load_ushort v169, v255, s[48:51], s83 offen offset:448\n"
"buffer_load_ushort v170, v255, s[48:51], s83 offen offset:704\n"
"buffer_load_ushort v171, v255, s[48:51], s83 offen offset:960\n"
"s_waitcnt vmcnt(0)\n"
"v_lshl_or_b32 v160, v165, 16, v164\n"
"v_lshl_or_b32 v161, v167, 16, v166\n"
"v_lshl_or_b32 v162, v169, 16, v168\n"
"v_lshl_or_b32 v163, v171, 16, v170\n"
"s_nop 1\n"
"v_mfma_f32_16x16x32_bf16 v[120:123], v[160:163], v[200:203], v[120:123]\n"
"v_mfma_f32_16x16x32_bf16 v[124:127], v[160:163], v[204:207], v[124:127]\n"
"v_mfma_f32_16x16x32_bf16 v[128:131], v[160:163], v[208:211], v[128:131]\n"
"v_mfma_f32_16x16x32_bf16 v[132:135], v[160:163], v[212:215], v[132:135]\n"
"v_mfma_f32_16x16x32_bf16 v[136:139], v[160:163], v[216:219], v[136:139]\n"
"buffer_load_ushort v164, v255, s[48:51], s78 offen offset:224\n"
"buffer_load_ushort v165, v255, s[48:51], s78 offen offset:480\n"
"buffer_load_ushort v166, v255, s[48:51], s78 offen offset:736\n"
"buffer_load_ushort v167, v255, s[48:51], s78 offen offset:992\n"
"buffer_load_ushort v168, v255, s[48:51], s83 offen offset:224\n"
"buffer_load_ushort v169, v255, s[48:51], s83 offen offset:480\n"
"buffer_load_ushort v170, v255, s[48:51], s83 offen offset:736\n"
"buffer_load_ushort v171, v255, s[48:51], s83 offen offset:992\n"
"s_waitcnt vmcnt(0)\n"
"v_lshl_or_b32 v160, v165, 16, v164\n"
"v_lshl_or_b32 v161, v167, 16, v166\n"
"v_lshl_or_b32 v162, v169, 16, v168\n"
"v_lshl_or_b32 v163, v171, 16, v170\n"
"s_nop 1\n"
"v_mfma_f32_16x16x32_bf16 v[140:143], v[160:163], v[200:203], v[140:143]\n"
"v_mfma_f32_16x16x32_bf16 v[144:147], v[160:163], v[204:207], v[144:147]\n"
"v_mfma_f32_16x16x32_bf16 v[148:151], v[160:163], v[208:211], v[148:151]\n"
"v_mfma_f32_16x16x32_bf16 v[152:155], v[160:163], v[212:215], v[152:155]\n"
"v_mfma_f32_16x16x32_bf16 v[156:159], v[160:163], v[216:219], v[156:159]\n"
"v_mfma_f32_16x16x32_bf16 a[144:147], a[164:167], v[200:203], a[144:147]\n"
"v_mfma_f32_16x16x32_bf16 a[148:151], a[164:167], v[204:207], a[148:151]\n"
"v_mfma_f32_16x16x32_bf16 a[152:155], a[164:167], v[208:211], a[152:155]\n"
"v_mfma_f32_16x16x32_bf16 a[156:159], a[164:167], v[212:215], a[156:159]\n"
"v_mfma_f32_16x16x32_bf16 a[160:163], a[164:167], v[216:219], a[160:163]\n"
"s_add_u32 s78, s78, 8192\n"
"s_add_u32 s83, s83, 8192\n"
"s_sub_u32 s75, s75, 1\n"
"s_cmp_lg_u32 s75, 0\n"
"s_cbranch_scc1 L_slb0%=\n"
"s_nop 7\n"
"v_accvgpr_read_b32 v232, a144\n"
"s_nop 0\n"
"v_rcp_f32 v232, v232\n"
"s_nop 0\n"
"v_pk_mul_f32 v[0:1], v[0:1], v[232:233] op_sel:[0,0] op_sel_hi:[1,0]\n"
"ds_read_b128 v[160:163], v246 offset:0\n"
"ds_read_b128 v[164:167], v246 offset:2048\n"
"ds_read_b128 v[168:171], v246 offset:4096\n"
"ds_read_b128 v[172:175], v246 offset:6144\n"
"ds_read_b128 v[176:179], v246 offset:8192\n"
"ds_read_b64_tr_b16 a[80:81], v245 offset:0\n"
"ds_read_b64_tr_b16 a[82:83], v245 offset:256\n"
"ds_read_b64_tr_b16 a[84:85], v245 offset:8\n"
"ds_read_b64_tr_b16 a[86:87], v245 offset:264\n"
"ds_read_b64_tr_b16 a[88:89], v245 offset:512\n"
"ds_read_b64_tr_b16 a[90:91], v245 offset:768\n"
"ds_read_b64_tr_b16 a[92:93], v245 offset:520\n"
"ds_read_b64_tr_b16 a[94:95], v245 offset:776\n"
"ds_read_b64_tr_b16 a[96:97], v245 offset:1024\n"
"ds_read_b64_tr_b16 a[98:99], v245 offset:1280\n"
"ds_read_b64_tr_b16 a[100:101], v245 offset:1032\n"
"ds_read_b64_tr_b16 a[102:103], v245 offset:1288\n"
"ds_read_b64_tr_b16 a[104:105], v245 offset:1536\n"
"ds_read_b64_tr_b16 a[106:107], v245 offset:1792\n"
"ds_read_b64_tr_b16 a[108:109], v245 offset:1544\n"
"ds_read_b64_tr_b16 a[110:111], v245 offset:1800\n"
"s_branch L_sback0%=\n"
"L_slow1%=:\n"
"s_waitcnt vmcnt(0) lgkmcnt(0)\n"
"v_and_b32 v254, 240, v251\n"
"v_lshlrev_b32 v254, 4, v254\n"
"v_lshrrev_b32 v255, 8, v251\n"
"v_lshl_add_u32 v254, v255, 4, v254\n"
"v_lshlrev_b32 v255, 10, v255\n"
"v_bfe_u32 v160, v251, 4, 4\n"
"v_lshl_add_u32 v255, v160, 1, v255\n"
"v_mov_b32 v220, 0xff800000\n"
"v_mov_b32 v224, 0xff800000\n"
"v_mov_b32 v228, 0xff800000\n"
"v_mov_b32 v232, 0xff800000\n"
"v_mov_b32 v236, 0xff800000\n"
"s_lshr_b32 s78, s73, 1\n"
"s_lshl_b32 s78, s78, 17\n"
"s_add_u32 s78, s78, s84\n"
"s_add_u32 s83, s78, 4096\n"
"s_mov_b32 s75, 16\n"
"L_sla1%=:\n"
"buffer_load_dwordx4 a[112:115], v254, s[44:47], s78 offen\n"
"buffer_load_dwordx4 a[116:119], v254, s[44:47], s78 offen offset:64\n"
"buffer_load_dwordx4 a[120:123], v254, s[44:47], s78 offen offset:128\n"
"buffer_load_dwordx4 a[124:127], v254, s[44:47], s78 offen offset:192\n"
"buffer_load_dwordx4 a[128:131], v254, s[44:47], s83 offen\n"
"buffer_load_dwordx4 a[132:135], v254, s[44:47], s83 offen offset:64\n"
"buffer_load_dwordx4 a[136:139], v254, s[44:47], s83 offen offset:128\n"
"buffer_load_dwordx4 a[140:143], v254, s[44:47], s83 offen offset:192\n"
"s_waitcnt vmcnt(0)\n"
"v_mfma_f32_16x16x32_bf16 v[160:163], a[112:115], a[0:3], 0\n"
"v_mfma_f32_16x16x32_bf16 v[160:163], a[116:119], a[20:23], v[160:163]\n"
"v_mfma_f32_16x16x32_bf16 v[160:163], a[120:123], a[40:43], v[160:163]\n"
"v_mfma_f32_16x16x32_bf16 v[160:163], a[124:127], a[60:63], v[160:163]\n"
"v_mfma_f32_16x16x32_bf16 v[164:167], a[128:131], a[0:3], 0\n"
"v_mfma_f32_16x16x32_bf16 v[164:167], a[132:135], a[20:23], v[164:167]\n"
"v_mfma_f32_16x16x32_bf16 v[164:167], a[136:139], a[40:43], v[164:167]\n"
"v_mfma_f32_16x16x32_bf16 v[164:167], a[140:143], a[60:63], v[164:167]\n"
"v_mfma_f32_16x16x32_bf16 v[168:171], a[112:115], a[4:7], 0\n"
"v_mfma_f32_16x16x32_bf16 v[168:171], a[116:119], a[24:27], v[168:171]\n"
"v_mfma_f32_16x16x32_bf16 v[168:171], a[120:123], a[44:47], v[168:171]\n"
"v_mfma_f32_16x16x32_bf16 v[168:171], a[124:127], a[64:67], v[168:171]\n"
"v_mfma_f32_16x16x32_bf16 v[172:175], a[128:131], a[4:7], 0\n"
"v_mfma_f32_16x16x32_bf16 v[172:175], a[132:135], a[24:27], v[172:175]\n"
"v_mfma_f32_16x16x32_bf16 v[172:175], a[136:139], a[44:47], v[172:175]\n"
"v_mfma_f32_16x16x32_bf16 v[172:175], a[140:143], a[64:67], v[172:175]\n"
"v_mfma_f32_16x16x32_bf16 v[176:179], a[112:115], a[8:11], 0\n"
"v_mfma_f32_16x16x32_bf16 v[176:179], a[116:119], a[28:31], v[176:179]\n"
"v_mfma_f32_16x16x32_bf16 v[176:179], a[120:123], a[48:51], v[176:179]\n"
"v_mfma_f32_16x16x32_bf16 v[176:179], a[124:127], a[68:71], v[176:179]\n"
"v_mfma_f32_16x16x32_bf16 v[180:183], a[128:131], a[8:11], 0\n"
"v_mfma_f32_16x16x32_bf16 v[180:183], a[132:135], a[28:31], v[180:183]\n"
"v_mfma_f32_16x16x32_bf16 v[180:183], a[136:139], a[48:51], v[180:183]\n"
"v_mfma_f32_16x16x32_bf16 v[180:183], a[140:143], a[68:71], v[180:183]\n"
"v_mfma_f32_16x16x32_bf16 v[184:187], a[112:115], a[12:15], 0\n"
"v_mfma_f32_16x16x32_bf16 v[184:187], a[116:119], a[32:35], v[184:187]\n"
"v_mfma_f32_16x16x32_bf16 v[184:187], a[120:123], a[52:55], v[184:187]\n"
"v_mfma_f32_16x16x32_bf16 v[184:187], a[124:127], a[72:75], v[184:187]\n"
"v_mfma_f32_16x16x32_bf16 v[188:191], a[128:131], a[12:15], 0\n"
"v_mfma_f32_16x16x32_bf16 v[188:191], a[132:135], a[32:35], v[188:191]\n"
"v_mfma_f32_16x16x32_bf16 v[188:191], a[136:139], a[52:55], v[188:191]\n"
"v_mfma_f32_16x16x32_bf16 v[188:191], a[140:143], a[72:75], v[188:191]\n"
"v_mfma_f32_16x16x32_bf16 v[192:195], a[112:115], a[16:19], 0\n"
"v_mfma_f32_16x16x32_bf16 v[192:195], a[116:119], a[36:39], v[192:195]\n"
"v_mfma_f32_16x16x32_bf16 v[192:195], a[120:123], a[56:59], v[192:195]\n"
"v_mfma_f32_16x16x32_bf16 v[192:195], a[124:127], a[76:79], v[192:195]\n"
"v_mfma_f32_16x16x32_bf16 v[196:199], a[128:131], a[16:19], 0\n"
"v_mfma_f32_16x16x32_bf16 v[196:199], a[132:135], a[36:39], v[196:199]\n"
"v_mfma_f32_16x16x32_bf16 v[196:199], a[136:139], a[56:59], v[196:199]\n"
"v_mfma_f32_16x16x32_bf16 v[196:199], a[140:143], a[76:79], v[196:199]\n"
"s_nop 7\n"
"v_max3_f32 v220, v220, v160, v161\n"
"v_max3_f32 v220, v220, v162, v163\n"
"v_max3_f32 v220, v220, v164, v165\n"
"v_max3_f32 v220, v220, v166, v167\n"
"v_max3_f32 v224, v224, v168, v169\n"
"v_max3_f32 v224, v224, v170, v171\n"
"v_max3_f32 v224, v224, v172, v173\n"
"v_max3_f32 v224, v224, v174, v175\n"
"v_max3_f32 v228, v228, v176, v177\n"
"v_max3_f32 v228, v228, v178, v179\n"
"v_max3_f32 v228, v228, v180, v181\n"
"v_max3_f32 v228, v228, v182, v183\n"
"v_max3_f32 v232, v232, v184, v185\n"
"v_max3_f32 v232, v232, v186, v187\n"
"v_max3_f32 v232, v232, v188, v189\n"
"v_max3_f32 v232, v232, v190, v191\n"
"v_max3_f32 v236, v236, v192, v193\n"
"v_max3_f32 v236, v236, v194, v195\n"
"v_max3_f32 v236, v236, v196, v197\n"
"v_max3_f32 v236, v236, v198, v199\n"
"s_add_u32 s78, s78, 8192\n"
"s_add_u32 s83, s83, 8192\n"
"s_sub_u32 s75, s75, 1\n"
"s_cmp_lg_u32 s75, 0\n"
"s_cbranch_scc1 L_sla1%=\n"
"v_mov_b32 v221, v220\n"
"s_nop 1\n"
"v_permlane32_swap_b32 v221, v220\n"
"v_max_f32 v220, v220, v221\n"
"v_mov_b32 v221, v220\n"
"s_nop 1\n"
"v_permlane16_swap_b32 v221, v220\n"
"v_max_f32 v220, v220, v221\n"
"v_sub_f32 v223, 0, v220\n"
"v_sub_f32 v222, 0, v220\n"
"v_sub_f32 v221, 0, v220\n"
"v_sub_f32 v220, 0, v220\n"
"v_mov_b32 v225, v224\n"
"s_nop 1\n"
"v_permlane32_swap_b32 v225, v224\n"
"v_max_f32 v224, v224, v225\n"
"v_mov_b32 v225, v224\n"
"s_nop 1\n"
"v_permlane16_swap_b32 v225, v224\n"
"v_max_f32 v224, v224, v225\n"
"v_sub_f32 v227, 0, v224\n"
"v_sub_f32 v226, 0, v224\n"
"v_sub_f32 v225, 0, v224\n"
"v_sub_f32 v224, 0, v224\n"
"v_mov_b32 v229, v228\n"
"s_nop 1\n"
"v_permlane32_swap_b32 v229, v228\n"
"v_max_f32 v228, v228, v229\n"
"v_mov_b32 v229, v228\n"
"s_nop 1\n"
"v_permlane16_swap_b32 v229, v228\n"
"v_max_f32 v228, v228, v229\n"
"v_sub_f32 v231, 0, v228\n"
"v_sub_f32 v230, 0, v228\n"
"v_sub_f32 v229, 0, v228\n"
"v_sub_f32 v228, 0, v228\n"
"v_mov_b32 v233, v232\n"
"s_nop 1\n"
"v_permlane32_swap_b32 v233, v232\n"
"v_max_f32 v232, v232, v233\n"
"v_mov_b32 v233, v232\n"
"s_nop 1\n"
"v_permlane16_swap_b32 v233, v232\n"
"v_max_f32 v232, v232, v233\n"
"v_sub_f32 v235, 0, v232\n"
"v_sub_f32 v234, 0, v232\n"
"v_sub_f32 v233, 0, v232\n"
"v_sub_f32 v232, 0, v232\n"
"v_mov_b32 v237, v236\n"
"s_nop 1\n"
"v_permlane32_swap_b32 v237, v236\n"
"v_max_f32 v236, v236, v237\n"
"v_mov_b32 v237, v236\n"
"s_nop 1\n"
"v_permlane16_swap_b32 v237, v236\n"
"v_max_f32 v236, v236, v237\n"
"v_sub_f32 v239, 0, v236\n"
"v_sub_f32 v238, 0, v236\n"
"v_sub_f32 v237, 0, v236\n"
"v_sub_f32 v236, 0, v236\n"
"v_mov_b64 v[0:1], 0\n"
"v_mov_b64 v[2:3], 0\n"
"v_mov_b64 v[4:5], 0\n"
"v_mov_b64 v[6:7], 0\n"
"v_mov_b64 v[8:9], 0\n"
"v_mov_b64 v[10:11], 0\n"
"v_mov_b64 v[12:13], 0\n"
"v_mov_b64 v[14:15], 0\n"
"v_mov_b64 v[16:17], 0\n"
"v_mov_b64 v[18:19], 0\n"
"v_mov_b64 v[20:21], 0\n"
"v_mov_b64 v[22:23], 0\n"
"v_mov_b64 v[24:25], 0\n"
"v_mov_b64 v[26:27], 0\n"
"v_mov_b64 v[28:29], 0\n"
"v_mov_b64 v[30:31], 0\n"
"v_mov_b64 v[32:33], 0\n"
"v_mov_b64 v[34:35], 0\n"
"v_mov_b64 v[36:37], 0\n"
"v_mov_b64 v[38:39], 0\n"
"v_mov_b64 v[40:41], 0\n"
"v_mov_b64 v[42:43], 0\n"
"v_mov_b64 v[44:45], 0\n"
"v_mov_b64 v[46:47], 0\n"
"v_mov_b64 v[48:49], 0\n"
"v_mov_b64 v[50:51], 0\n"
"v_mov_b64 v[52:53], 0\n"
"v_mov_b64 v[54:55], 0\n"
"v_mov_b64 v[56:57], 0\n"
"v_mov_b64 v[58:59], 0\n"
"v_mov_b64 v[60:61], 0\n"
"v_mov_b64 v[62:63], 0\n"
"v_mov_b64 v[64:65], 0\n"
"v_mov_b64 v[66:67], 0\n"
"v_mov_b64 v[68:69], 0\n"
"v_mov_b64 v[70:71], 0\n"
"v_mov_b64 v[72:73], 0\n"
"v_mov_b64 v[74:75], 0\n"
"v_mov_b64 v[76:77], 0\n"
"v_mov_b64 v[78:79], 0\n"
"v_mov_b64 v[80:81], 0\n"
"v_mov_b64 v[82:83], 0\n"
"v_mov_b64 v[84:85], 0\n"
"v_mov_b64 v[86:87], 0\n"
"v_mov_b64 v[88:89], 0\n"
"v_mov_b64 v[90:91], 0\n"
"v_mov_b64 v[92:93], 0\n"
"v_mov_b64 v[94:95], 0\n"
"v_mov_b64 v[96:97], 0\n"
"v_mov_b64 v[98:99], 0\n"
"v_mov_b64 v[100:101], 0\n"
"v_mov_b64 v[102:103], 0\n"
"v_mov_b64 v[104:105], 0\n"
"v_mov_b64 v[106:107], 0\n"
"v_mov_b64 v[108:109], 0\n"
"v_mov_b64 v[110:111], 0\n"
"v_mov_b64 v[112:113], 0\n"
"v_mov_b64 v[114:115], 0\n"
"v_mov_b64 v[116:117], 0\n"
"v_mov_b64 v[118:119], 0\n"
"v_mov_b64 v[120:121], 0\n"
"v_mov_b64 v[122:123], 0\n"
"v_mov_b64 v[124:125], 0\n"
"v_mov_b64 v[126:127], 0\n"
"v_mov_b64 v[128:129], 0\n"
"v_mov_b64 v[130:131], 0\n"
"v_mov_b64 v[132:133], 0\n"
"v_mov_b64 v[134:135], 0\n"
"v_mov_b64 v[136:137], 0\n"
"v_mov_b64 v[138:139], 0\n"
"v_mov_b64 v[140:141], 0\n"
"v_mov_b64 v[142:143], 0\n"
"v_mov_b64 v[144:145], 0\n"
"v_mov_b64 v[146:147], 0\n"
"v_mov_b64 v[148:149], 0\n"
"v_mov_b64 v[150:151], 0\n"
"v_mov_b64 v[152:153], 0\n"
"v_mov_b64 v[154:155], 0\n"
"v_mov_b64 v[156:157], 0\n"
"v_mov_b64 v[158:159], 0\n"
"v_accvgpr_write_b32 a144, 0\n"
"v_accvgpr_write_b32 a145, 0\n"
"v_accvgpr_write_b32 a146, 0\n"
"v_accvgpr_write_b32 a147, 0\n"
"v_accvgpr_write_b32 a148, 0\n"
"v_accvgpr_write_b32 a149, 0\n"
"v_accvgpr_write_b32 a150, 0\n"
"v_accvgpr_write_b32 a151, 0\n"
"v_accvgpr_write_b32 a152, 0\n"
"v_accvgpr_write_b32 a153, 0\n"
"v_accvgpr_write_b32 a154, 0\n"
"v_accvgpr_write_b32 a155, 0\n"
"v_accvgpr_write_b32 a156, 0\n"
"v_accvgpr_write_b32 a157, 0\n"
"v_accvgpr_write_b32 a158, 0\n"
"v_accvgpr_write_b32 a159, 0\n"
"v_accvgpr_write_b32 a160, 0\n"
"v_accvgpr_write_b32 a161, 0\n"
"v_accvgpr_write_b32 a162, 0\n"
"v_accvgpr_write_b32 a163, 0\n"
"s_lshr_b32 s78, s73, 1\n"
"s_lshl_b32 s78, s78, 17\n"
"s_add_u32 s78, s78, s84\n"
"s_add_u32 s83, s78, 4096\n"
"s_mov_b32 s75, 16\n"
"L_slb1%=:\n"
"buffer_load_dwordx4 a[112:115], v254, s[44:47], s78 offen\n"
"buffer_load_dwordx4 a[116:119], v254, s[44:47], s78 offen offset:64\n"
"buffer_load_dwordx4 a[120:123], v254, s[44:47], s78 offen offset:128\n"
"buffer_load_dwordx4 a[124:127], v254, s[44:47], s78 offen offset:192\n"
"buffer_load_dwordx4 a[128:131], v254, s[44:47], s83 offen\n"
"buffer_load_dwordx4 a[132:135], v254, s[44:47], s83 offen offset:64\n"
"buffer_load_dwordx4 a[136:139], v254, s[44:47], s83 offen offset:128\n"
"buffer_load_dwordx4 a[140:143], v254, s[44:47], s83 offen offset:192\n"
"s_waitcnt vmcnt(0)\n"
"v_mfma_f32_16x16x32_bf16 v[160:163], a[112:115], a[0:3], v[220:223]\n"
"v_mfma_f32_16x16x32_bf16 v[160:163], a[116:119], a[20:23], v[160:163]\n"
"v_mfma_f32_16x16x32_bf16 v[160:163], a[120:123], a[40:43], v[160:163]\n"
"v_mfma_f32_16x16x32_bf16 v[160:163], a[124:127], a[60:63], v[160:163]\n"
"v_mfma_f32_16x16x32_bf16 v[164:167], a[128:131], a[0:3], v[220:223]\n"
"v_mfma_f32_16x16x32_bf16 v[164:167], a[132:135], a[20:23], v[164:167]\n"
"v_mfma_f32_16x16x32_bf16 v[164:167], a[136:139], a[40:43], v[164:167]\n"
"v_mfma_f32_16x16x32_bf16 v[164:167], a[140:143], a[60:63], v[164:167]\n"
"v_mfma_f32_16x16x32_bf16 v[168:171], a[112:115], a[4:7], v[224:227]\n"
"v_mfma_f32_16x16x32_bf16 v[168:171], a[116:119], a[24:27], v[168:171]\n"
"v_mfma_f32_16x16x32_bf16 v[168:171], a[120:123], a[44:47], v[168:171]\n"
"v_mfma_f32_16x16x32_bf16 v[168:171], a[124:127], a[64:67], v[168:171]\n"
"v_mfma_f32_16x16x32_bf16 v[172:175], a[128:131], a[4:7], v[224:227]\n"
"v_mfma_f32_16x16x32_bf16 v[172:175], a[132:135], a[24:27], v[172:175]\n"
"v_mfma_f32_16x16x32_bf16 v[172:175], a[136:139], a[44:47], v[172:175]\n"
"v_mfma_f32_16x16x32_bf16 v[172:175], a[140:143], a[64:67], v[172:175]\n"
"v_mfma_f32_16x16x32_bf16 v[176:179], a[112:115], a[8:11], v[228:231]\n"
"v_mfma_f32_16x16x32_bf16 v[176:179], a[116:119], a[28:31], v[176:179]\n"
"v_mfma_f32_16x16x32_bf16 v[176:179], a[120:123], a[48:51], v[176:179]\n"
"v_mfma_f32_16x16x32_bf16 v[176:179], a[124:127], a[68:71], v[176:179]\n"
"v_mfma_f32_16x16x32_bf16 v[180:183], a[128:131], a[8:11], v[228:231]\n"
"v_mfma_f32_16x16x32_bf16 v[180:183], a[132:135], a[28:31], v[180:183]\n"
"v_mfma_f32_16x16x32_bf16 v[180:183], a[136:139], a[48:51], v[180:183]\n"
"v_mfma_f32_16x16x32_bf16 v[180:183], a[140:143], a[68:71], v[180:183]\n"
"v_mfma_f32_16x16x32_bf16 v[184:187], a[112:115], a[12:15], v[232:235]\n"
"v_mfma_f32_16x16x32_bf16 v[184:187], a[116:119], a[32:35], v[184:187]\n"
"v_mfma_f32_16x16x32_bf16 v[184:187], a[120:123], a[52:55], v[184:187]\n"
"v_mfma_f32_16x16x32_bf16 v[184:187], a[124:127], a[72:75], v[184:187]\n"
"v_mfma_f32_16x16x32_bf16 v[188:191], a[128:131], a[12:15], v[232:235]\n"
"v_mfma_f32_16x16x32_bf16 v[188:191], a[132:135], a[32:35], v[188:191]\n"
"v_mfma_f32_16x16x32_bf16 v[188:191], a[136:139], a[52:55], v[188:191]\n"
"v_mfma_f32_16x16x32_bf16 v[188:191], a[140:143], a[72:75], v[188:191]\n"
"v_mfma_f32_16x16x32_bf16 v[192:195], a[112:115], a[16:19], v[236:239]\n"
"v_mfma_f32_16x16x32_bf16 v[192:195], a[116:119], a[36:39], v[192:195]\n"
"v_mfma_f32_16x16x32_bf16 v[192:195], a[120:123], a[56:59], v[192:195]\n"
"v_mfma_f32_16x16x32_bf16 v[192:195], a[124:127], a[76:79], v[192:195]\n"
"v_mfma_f32_16x16x32_bf16 v[196:199], a[128:131], a[16:19], v[236:239]\n"
"v_mfma_f32_16x16x32_bf16 v[196:199], a[132:135], a[36:39], v[196:199]\n"
"v_mfma_f32_16x16x32_bf16 v[196:199], a[136:139], a[56:59], v[196:199]\n"
"v_mfma_f32_16x16x32_bf16 v[196:199], a[140:143], a[76:79], v[196:199]\n"
"s_nop 7\n"
"v_exp_f32 v160, v160\n"
"v_exp_f32 v161, v161\n"
"v_exp_f32 v162, v162\n"
"v_exp_f32 v163, v163\n"
"v_exp_f32 v164, v164\n"
"v_exp_f32 v165, v165\n"
"v_exp_f32 v166, v166\n"
"v_exp_f32 v167, v167\n"
"v_exp_f32 v168, v168\n"
"v_exp_f32 v169, v169\n"
"v_exp_f32 v170, v170\n"
"v_exp_f32 v171, v171\n"
"v_exp_f32 v172, v172\n"
"v_exp_f32 v173, v173\n"
"v_exp_f32 v174, v174\n"
"v_exp_f32 v175, v175\n"
"v_exp_f32 v176, v176\n"
"v_exp_f32 v177, v177\n"
"v_exp_f32 v178, v178\n"
"v_exp_f32 v179, v179\n"
"v_exp_f32 v180, v180\n"
"v_exp_f32 v181, v181\n"
"v_exp_f32 v182, v182\n"
"v_exp_f32 v183, v183\n"
"v_exp_f32 v184, v184\n"
"v_exp_f32 v185, v185\n"
"v_exp_f32 v186, v186\n"
"v_exp_f32 v187, v187\n"
"v_exp_f32 v188, v188\n"
"v_exp_f32 v189, v189\n"
"v_exp_f32 v190, v190\n"
"v_exp_f32 v191, v191\n"
"v_exp_f32 v192, v192\n"
"v_exp_f32 v193, v193\n"
"v_exp_f32 v194, v194\n"
"v_exp_f32 v195, v195\n"
"v_exp_f32 v196, v196\n"
"v_exp_f32 v197, v197\n"
"v_exp_f32 v198, v198\n"
"v_exp_f32 v199, v199\n"
"s_nop 0\n"
"v_cvt_pk_bf16_f32 v200, v160, v161\n"
"v_cvt_pk_bf16_f32 v201, v162, v163\n"
"v_cvt_pk_bf16_f32 v202, v164, v165\n"
"v_cvt_pk_bf16_f32 v203, v166, v167\n"
"v_cvt_pk_bf16_f32 v204, v168, v169\n"
"v_cvt_pk_bf16_f32 v205, v170, v171\n"
"v_cvt_pk_bf16_f32 v206, v172, v173\n"
"v_cvt_pk_bf16_f32 v207, v174, v175\n"
"v_cvt_pk_bf16_f32 v208, v176, v177\n"
"v_cvt_pk_bf16_f32 v209, v178, v179\n"
"v_cvt_pk_bf16_f32 v210, v180, v181\n"
"v_cvt_pk_bf16_f32 v211, v182, v183\n"
"v_cvt_pk_bf16_f32 v212, v184, v185\n"
"v_cvt_pk_bf16_f32 v213, v186, v187\n"
"v_cvt_pk_bf16_f32 v214, v188, v189\n"
"v_cvt_pk_bf16_f32 v215, v190, v191\n"
"v_cvt_pk_bf16_f32 v216, v192, v193\n"
"v_cvt_pk_bf16_f32 v217, v194, v195\n"
"v_cvt_pk_bf16_f32 v218, v196, v197\n"
"v_cvt_pk_bf16_f32 v219, v198, v199\n"
"buffer_load_ushort v164, v255, s[48:51], s78 offen offset:0\n"
"buffer_load_ushort v165, v255, s[48:51], s78 offen offset:256\n"
"buffer_load_ushort v166, v255, s[48:51], s78 offen offset:512\n"
"buffer_load_ushort v167, v255, s[48:51], s78 offen offset:768\n"
"buffer_load_ushort v168, v255, s[48:51], s83 offen offset:0\n"
"buffer_load_ushort v169, v255, s[48:51], s83 offen offset:256\n"
"buffer_load_ushort v170, v255, s[48:51], s83 offen offset:512\n"
"buffer_load_ushort v171, v255, s[48:51], s83 offen offset:768\n"
"s_waitcnt vmcnt(0)\n"
"v_lshl_or_b32 v160, v165, 16, v164\n"
"v_lshl_or_b32 v161, v167, 16, v166\n"
"v_lshl_or_b32 v162, v169, 16, v168\n"
"v_lshl_or_b32 v163, v171, 16, v170\n"
"s_nop 1\n"
"v_mfma_f32_16x16x32_bf16 v[0:3], v[160:163], v[200:203], v[0:3]\n"
"v_mfma_f32_16x16x32_bf16 v[4:7], v[160:163], v[204:207], v[4:7]\n"
"v_mfma_f32_16x16x32_bf16 v[8:11], v[160:163], v[208:211], v[8:11]\n"
"v_mfma_f32_16x16x32_bf16 v[12:15], v[160:163], v[212:215], v[12:15]\n"
"v_mfma_f32_16x16x32_bf16 v[16:19], v[160:163], v[216:219], v[16:19]\n"
"buffer_load_ushort v164, v255, s[48:51], s78 offen offset:32\n"
"buffer_load_ushort v165, v255, s[48:51], s78 offen offset:288\n"
"buffer_load_ushort v166, v255, s[48:51], s78 offen offset:544\n"
"buffer_load_ushort v167, v255, s[48:51], s78 offen offset:800\n"
"buffer_load_ushort v168, v255, s[48:51], s83 offen offset:32\n"
"buffer_load_ushort v169, v255, s[48:51], s83 offen offset:288\n"
"buffer_load_ushort v170, v255, s[48:51], s83 offen offset:544\n"
"buffer_load_ushort v171, v255, s[48:51], s83 offen offset:800\n"
"s_waitcnt vmcnt(0)\n"
"v_lshl_or_b32 v160, v165, 16, v164\n"
"v_lshl_or_b32 v161, v167, 16, v166\n"
"v_lshl_or_b32 v162, v169, 16, v168\n"
"v_lshl_or_b32 v163, v171, 16, v170\n"
"s_nop 1\n"
"v_mfma_f32_16x16x32_bf16 v[20:23], v[160:163], v[200:203], v[20:23]\n"
"v_mfma_f32_16x16x32_bf16 v[24:27], v[160:163], v[204:207], v[24:27]\n"
"v_mfma_f32_16x16x32_bf16 v[28:31], v[160:163], v[208:211], v[28:31]\n"
"v_mfma_f32_16x16x32_bf16 v[32:35], v[160:163], v[212:215], v[32:35]\n"
"v_mfma_f32_16x16x32_bf16 v[36:39], v[160:163], v[216:219], v[36:39]\n"
"buffer_load_ushort v164, v255, s[48:51], s78 offen offset:64\n"
"buffer_load_ushort v165, v255, s[48:51], s78 offen offset:320\n"
"buffer_load_ushort v166, v255, s[48:51], s78 offen offset:576\n"
"buffer_load_ushort v167, v255, s[48:51], s78 offen offset:832\n"
"buffer_load_ushort v168, v255, s[48:51], s83 offen offset:64\n"
"buffer_load_ushort v169, v255, s[48:51], s83 offen offset:320\n"
"buffer_load_ushort v170, v255, s[48:51], s83 offen offset:576\n"
"buffer_load_ushort v171, v255, s[48:51], s83 offen offset:832\n"
"s_waitcnt vmcnt(0)\n"
"v_lshl_or_b32 v160, v165, 16, v164\n"
"v_lshl_or_b32 v161, v167, 16, v166\n"
"v_lshl_or_b32 v162, v169, 16, v168\n"
"v_lshl_or_b32 v163, v171, 16, v170\n"
"s_nop 1\n"
"v_mfma_f32_16x16x32_bf16 v[40:43], v[160:163], v[200:203], v[40:43]\n"
"v_mfma_f32_16x16x32_bf16 v[44:47], v[160:163], v[204:207], v[44:47]\n"
"v_mfma_f32_16x16x32_bf16 v[48:51], v[160:163], v[208:211], v[48:51]\n"
"v_mfma_f32_16x16x32_bf16 v[52:55], v[160:163], v[212:215], v[52:55]\n"
"v_mfma_f32_16x16x32_bf16 v[56:59], v[160:163], v[216:219], v[56:59]\n"
"buffer_load_ushort v164, v255, s[48:51], s78 offen offset:96\n"
"buffer_load_ushort v165, v255, s[48:51], s78 offen offset:352\n"
"buffer_load_ushort v166, v255, s[48:51], s78 offen offset:608\n"
"buffer_load_ushort v167, v255, s[48:51], s78 offen offset:864\n"
"buffer_load_ushort v168, v255, s[48:51], s83 offen offset:96\n"
"buffer_load_ushort v169, v255, s[48:51], s83 offen offset:352\n"
"buffer_load_ushort v170, v255, s[48:51], s83 offen offset:608\n"
"buffer_load_ushort v171, v255, s[48:51], s83 offen offset:864\n"
"s_waitcnt vmcnt(0)\n"
"v_lshl_or_b32 v160, v165, 16, v164\n"
"v_lshl_or_b32 v161, v167, 16, v166\n"
"v_lshl_or_b32 v162, v169, 16, v168\n"
"v_lshl_or_b32 v163, v171, 16, v170\n"
"s_nop 1\n"
"v_mfma_f32_16x16x32_bf16 v[60:63], v[160:163], v[200:203], v[60:63]\n"
"v_mfma_f32_16x16x32_bf16 v[64:67], v[160:163], v[204:207], v[64:67]\n"
"v_mfma_f32_16x16x32_bf16 v[68:71], v[160:163], v[208:211], v[68:71]\n"
"v_mfma_f32_16x16x32_bf16 v[72:75], v[160:163], v[212:215], v[72:75]\n"
"v_mfma_f32_16x16x32_bf16 v[76:79], v[160:163], v[216:219], v[76:79]\n"
"buffer_load_ushort v164, v255, s[48:51], s78 offen offset:128\n"
"buffer_load_ushort v165, v255, s[48:51], s78 offen offset:384\n"
"buffer_load_ushort v166, v255, s[48:51], s78 offen offset:640\n"
"buffer_load_ushort v167, v255, s[48:51], s78 offen offset:896\n"
"buffer_load_ushort v168, v255, s[48:51], s83 offen offset:128\n"
"buffer_load_ushort v169, v255, s[48:51], s83 offen offset:384\n"
"buffer_load_ushort v170, v255, s[48:51], s83 offen offset:640\n"
"buffer_load_ushort v171, v255, s[48:51], s83 offen offset:896\n"
"s_waitcnt vmcnt(0)\n"
"v_lshl_or_b32 v160, v165, 16, v164\n"
"v_lshl_or_b32 v161, v167, 16, v166\n"
"v_lshl_or_b32 v162, v169, 16, v168\n"
"v_lshl_or_b32 v163, v171, 16, v170\n"
"s_nop 1\n"
"v_mfma_f32_16x16x32_bf16 v[80:83], v[160:163], v[200:203], v[80:83]\n"
"v_mfma_f32_16x16x32_bf16 v[84:87], v[160:163], v[204:207], v[84:87]\n"
"v_mfma_f32_16x16x32_bf16 v[88:91], v[160:163], v[208:211], v[88:91]\n"
"v_mfma_f32_16x16x32_bf16 v[92:95], v[160:163], v[212:215], v[92:95]\n"
"v_mfma_f32_16x16x32_bf16 v[96:99], v[160:163], v[216:219], v[96:99]\n"
"buffer_load_ushort v164, v255, s[48:51], s78 offen offset:160\n"
"buffer_load_ushort v165, v255, s[48:51], s78 offen offset:416\n"
"buffer_load_ushort v166, v255, s[48:51], s78 offen offset:672\n"
"buffer_load_ushort v167, v255, s[48:51], s78 offen offset:928\n"
"buffer_load_ushort v168, v255, s[48:51], s83 offen offset:160\n"
"buffer_load_ushort v169, v255, s[48:51], s83 offen offset:416\n"
"buffer_load_ushort v170, v255, s[48:51], s83 offen offset:672\n"
"buffer_load_ushort v171, v255, s[48:51], s83 offen offset:928\n"
"s_waitcnt vmcnt(0)\n"
"v_lshl_or_b32 v160, v165, 16, v164\n"
"v_lshl_or_b32 v161, v167, 16, v166\n"
"v_lshl_or_b32 v162, v169, 16, v168\n"
"v_lshl_or_b32 v163, v171, 16, v170\n"
"s_nop 1\n"
"v_mfma_f32_16x16x32_bf16 v[100:103], v[160:163], v[200:203], v[100:103]\n"
"v_mfma_f32_16x16x32_bf16 v[104:107], v[160:163], v[204:207], v[104:107]\n"
"v_mfma_f32_16x16x32_bf16 v[108:111], v[160:163], v[208:211], v[108:111]\n"
"v_mfma_f32_16x16x32_bf16 v[112:115], v[160:163], v[212:215], v[112:115]\n"
"v_mfma_f32_16x16x32_bf16 v[116:119], v[160:163], v[216:219], v[116:119]\n"
"buffer_load_ushort v164, v255, s[48:51], s78 offen offset:192\n"
"buffer_load_ushort v165, v255, s[48:51], s78 offen offset:448\n"
"buffer_load_ushort v166, v255, s[48:51], s78 offen offset:704\n"
"buffer_load_ushort v167, v255, s[48:51], s78 offen offset:960\n"
"buffer_load_ushort v168, v255, s[48:51], s83 offen offset:192\n"
"buffer_load_ushort v169, v255, s[48:51], s83 offen offset:448\n"
"buffer_load_ushort v170, v255, s[48:51], s83 offen offset:704\n"
"buffer_load_ushort v171, v255, s[48:51], s83 offen offset:960\n"
"s_waitcnt vmcnt(0)\n"
"v_lshl_or_b32 v160, v165, 16, v164\n"
"v_lshl_or_b32 v161, v167, 16, v166\n"
"v_lshl_or_b32 v162, v169, 16, v168\n"
"v_lshl_or_b32 v163, v171, 16, v170\n"
"s_nop 1\n"
"v_mfma_f32_16x16x32_bf16 v[120:123], v[160:163], v[200:203], v[120:123]\n"
"v_mfma_f32_16x16x32_bf16 v[124:127], v[160:163], v[204:207], v[124:127]\n"
"v_mfma_f32_16x16x32_bf16 v[128:131], v[160:163], v[208:211], v[128:131]\n"
"v_mfma_f32_16x16x32_bf16 v[132:135], v[160:163], v[212:215], v[132:135]\n"
"v_mfma_f32_16x16x32_bf16 v[136:139], v[160:163], v[216:219], v[136:139]\n"
"buffer_load_ushort v164, v255, s[48:51], s78 offen offset:224\n"
"buffer_load_ushort v165, v255, s[48:51], s78 offen offset:480\n"
"buffer_load_ushort v166, v255, s[48:51], s78 offen offset:736\n"
"buffer_load_ushort v167, v255, s[48:51], s78 offen offset:992\n"
"buffer_load_ushort v168, v255, s[48:51], s83 offen offset:224\n"
"buffer_load_ushort v169, v255, s[48:51], s83 offen offset:480\n"
"buffer_load_ushort v170, v255, s[48:51], s83 offen offset:736\n"
"buffer_load_ushort v171, v255, s[48:51], s83 offen offset:992\n"
"s_waitcnt vmcnt(0)\n"
"v_lshl_or_b32 v160, v165, 16, v164\n"
"v_lshl_or_b32 v161, v167, 16, v166\n"
"v_lshl_or_b32 v162, v169, 16, v168\n"
"v_lshl_or_b32 v163, v171, 16, v170\n"
"s_nop 1\n"
"v_mfma_f32_16x16x32_bf16 v[140:143], v[160:163], v[200:203], v[140:143]\n"
"v_mfma_f32_16x16x32_bf16 v[144:147], v[160:163], v[204:207], v[144:147]\n"
"v_mfma_f32_16x16x32_bf16 v[148:151], v[160:163], v[208:211], v[148:151]\n"
"v_mfma_f32_16x16x32_bf16 v[152:155], v[160:163], v[212:215], v[152:155]\n"
"v_mfma_f32_16x16x32_bf16 v[156:159], v[160:163], v[216:219], v[156:159]\n"
"v_mfma_f32_16x16x32_bf16 a[144:147], a[164:167], v[200:203], a[144:147]\n"
"v_mfma_f32_16x16x32_bf16 a[148:151], a[164:167], v[204:207], a[148:151]\n"
"v_mfma_f32_16x16x32_bf16 a[152:155], a[164:167], v[208:211], a[152:155]\n"
"v_mfma_f32_16x16x32_bf16 a[156:159], a[164:167], v[212:215], a[156:159]\n"
"v_mfma_f32_16x16x32_bf16 a[160:163], a[164:167], v[216:219], a[160:163]\n"
"s_add_u32 s78, s78, 8192\n"
"s_add_u32 s83, s83, 8192\n"
"s_sub_u32 s75, s75, 1\n"
"s_cmp_lg_u32 s75, 0\n"
"s_cbranch_scc1 L_slb1%=\n"
"s_nop 7\n"
"v_accvgpr_read_b32 v232, a144\n"
"s_nop 0\n"
"v_rcp_f32 v232, v232\n"
"s_nop 0\n"
"v_pk_mul_f32 v[0:1], v[0:1], v[232:233] op_sel:[0,0] op_sel_hi:[1,0]\n"
"v_pk_mul_f32 v[2:3], v[2:3], v[232:233] op_sel:[0,0] op_sel_hi:[1,0]\n"
"v_pk_mul_f32 v[20:21], v[20:21], v[232:233] op_sel:[0,0] op_sel_hi:[1,0]\n"
"v_pk_mul_f32 v[22:23], v[22:23], v[232:233] op_sel:[0,0] op_sel_hi:[1,0]\n"
"v_cvt_pk_bf16_f32 v0, v0, v1\n"
"v_cvt_pk_bf16_f32 v1, v2, v3\n"
"v_cvt_pk_bf16_f32 v2, v20, v21\n"
"v_cvt_pk_bf16_f32 v3, v22, v23\n"
"v_pk_mul_f32 v[40:41], v[40:41], v[232:233] op_sel:[0,0] op_sel_hi:[1,0]\n"
"v_pk_mul_f32 v[42:43], v[42:43], v[232:233] op_sel:[0,0] op_sel_hi:[1,0]\n"
"v_pk_mul_f32 v[60:61], v[60:61], v[232:233] op_sel:[0,0] op_sel_hi:[1,0]\n"
"v_pk_mul_f32 v[62:63], v[62:63], v[232:233] op_sel:[0,0] op_sel_hi:[1,0]\n"
"v_cvt_pk_bf16_f32 v40, v40, v41\n"
"v_cvt_pk_bf16_f32 v41, v42, v43\n"
"v_cvt_pk_bf16_f32 v42, v60, v61\n"
"v_cvt_pk_bf16_f32 v43, v62, v63\n"
"ds_read_b64_tr_b16 v[160:161], v240 offset:0\n"
"ds_read_b64_tr_b16 v[162:163], v240 offset:2048\n"
"ds_read_b64_tr_b16 v[164:165], v240 offset:4096\n"
"ds_read_b64_tr_b16 v[166:167], v240 offset:6144\n"
"ds_read_b64_tr_b16 v[168:169], v240 offset:8192\n"
"ds_read_b64_tr_b16 v[170:171], v240 offset:10240\n"
"ds_read_b64_tr_b16 v[172:173], v240 offset:12288\n"
"ds_read_b64_tr_b16 v[174:175], v240 offset:14336\n"
"s_branch L_sback1%=\n"
"L_spg%=:\n"
"s_sleep 4\n"
"buffer_load_dwordx2 v[76:77], v78, s[56:59], 0 offen sc1\n"
"s_waitcnt vmcnt(0)\n"
"v_cmp_eq_u64 vcc, v[76:77], v[248:249]\n"
"s_add_u32 s24, s24, 1\n"
"s_cmp_lt_u32 s24, 400000\n"
"s_cbranch_scc1 L_g0%=\n"
"s_branch L_bkg%=\n"
"L_spd%=:\n"
"s_sleep 4\n"
"buffer_load_dwordx2 v[76:77], v78, s[56:59], 0 offen sc1\n"
"s_waitcnt vmcnt(0)\n"
"v_cmp_eq_u64 vcc, v[76:77], v[248:249]\n"
"s_add_u32 s24, s24, 1\n"
"s_cmp_lt_u32 s24, 400000\n"
"s_cbranch_scc1 L_chk%=\n"
"s_branch L_bkd%=\n"
"L_spinf1%=:\n"
"s_mov_b32 s93, 0\n"
"L_sp2f1%=:\n"
"s_sleep 4\n"
"buffer_load_dwordx2 v[100:101], v250, s[56:59], s88 offen sc1\n"
"s_waitcnt vmcnt(0)\n"
"v_cmp_eq_u64 vcc, v[100:101], v[248:249]\n"
"s_add_u32 s93, s93, 1\n"
"s_nop 0\n"
"s_cmp_eq_u64 vcc, -1\n"
"s_cbranch_scc1 L_backf1%=\n"
"s_cmp_lt_u32 s93, 200000\n"
"s_cbranch_scc1 L_sp2f1%=\n"
"s_branch L_backf1%=\n"
"L_end%=:\n"
"L_codeend%=:\n"

    :
    : [x] "s"(px), [wq] "s"(pwq), [k] "s"(pk), [v] "s"(pv), [wo] "s"(pwo), [ws] "s"(pws), [out] "s"(pout), [info] "s"(info)
    : "v0","v1","v2","v3","v4","v5","v6","v7","v8","v9","v10","v11","v12","v13","v14","v15","v16","v17","v18","v19","v20","v21","v22","v23","v24","v25","v26","v27","v28","v29","v30","v31","v32","v33","v34","v35","v36","v37","v38","v39","v40","v41","v42","v43","v44","v45","v46","v47","v48","v49","v50","v51","v52","v53","v54","v55","v56","v57","v58","v59","v60","v61","v62","v63","v64","v65","v66","v67","v68","v69","v70","v71","v72","v73","v74","v75","v76","v77","v78","v79","v80","v81","v82","v83","v84","v85","v86","v87","v88","v89","v90","v91","v92","v93","v94","v95","v96","v97","v98","v99","v100","v101","v102","v103","v104","v105","v106","v107","v108","v109","v110","v111","v112","v113","v114","v115","v116","v117","v118","v119","v120","v121","v122","v123","v124","v125","v126","v127","v128","v129","v130","v131","v132","v133","v134","v135","v136","v137","v138","v139","v140","v141","v142","v143","v144","v145","v146","v147","v148","v149","v150","v151","v152","v153","v154","v155","v156","v157","v158","v159","v160","v161","v162","v163","v164","v165","v166","v167","v168","v169","v170","v171","v172","v173","v174","v175","v176","v177","v178","v179","v180","v181","v182","v183","v184","v185","v186","v187","v188","v189","v190","v191","v192","v193","v194","v195","v196","v197","v198","v199","v200","v201","v202","v203","v204","v205","v206","v207","v208","v209","v210","v211","v212","v213","v214","v215","v216","v217","v218","v219","v220","v221","v222","v223","v224","v225","v226","v227","v228","v229","v230","v231","v232","v233","v234","v235","v236","v237","v238","v239","v240","v241","v242","v243","v244","v245","v246","v247","v248","v249","v250","v251","v252","v253","v254","v255","a0","a1","a2","a3","a4","a5","a6","a7","a8","a9","a10","a11","a12","a13","a14","a15","a16","a17","a18","a19","a20","a21","a22","a23","a24","a25","a26","a27","a28","a29","a30","a31","a32","a33","a34","a35","a36","a37","a38","a39","a40","a41","a42","a43","a44","a45","a46","a47","a48","a49","a50","a51","a52","a53","a54","a55","a56","a57","a58","a59","a60","a61","a62","a63","a64","a65","a66","a67","a68","a69","a70","a71","a72","a73","a74","a75","a76","a77","a78","a79","a80","a81","a82","a83","a84","a85","a86","a87","a88","a89","a90","a91","a92","a93","a94","a95","a96","a97","a98","a99","a100","a101","a102","a103","a104","a105","a106","a107","a108","a109","a110","a111","a112","a113","a114","a115","a116","a117","a118","a119","a120","a121","a122","a123","a124","a125","a126","a127","a128","a129","a130","a131","a132","a133","a134","a135","a136","a137","a138","a139","a140","a141","a142","a143","a144","a145","a146","a147","a148","a149","a150","a151","a152","a153","a154","a155","a156","a157","a158","a159","a160","a161","a162","a163","a164","a165","a166","a167","a168","a169","a170","a171","a172","a173","a174","a175","a176","a177","a178","a179","a180","a181","a182","a183","a184","a185","a186","a187","a188","a189","a190","a191","a192","a193","a194","a195","a196","a197","a198","a199","a200","a201","a202","a203","a204","a205","a206","a207","a208","a209","a210","a211","a212","a213","a214","a215","a216","a217","a218","a219","a220","a221","a222","a223","a224","a225","a226","a227","a228","a229","a230","a231","a232","a233","a234","a235","a236","a237","a238","a239","a240","a241","a242","a243","a244","a245","a246","a247","a248","a249","a250","a251","a252","a253","a254","a255","s16","s17","s18","s19","s20","s21","s22","s23","s24","s25","s26","s27","s28","s29","s30","s31","s36","s37","s38","s39","s40","s41","s42","s43","s44","s45","s46","s47","s48","s49","s50","s51","s52","s53","s54","s55","s56","s57","s58","s59","s60","s61","s62","s63","s64","s65","s66","s67","s68","s69","s70","s71","s72","s73","s74","s75","s76","s77","s78","s79","s80","s81","s82","s83","s84","s85","s86","s87","s88","s89","s90","s91","s92","s93","s94","s95","s96","s97","s98","s99","vcc","scc","memory");
}

extern "C" void kernel_launch(void* const* d_in, const int* in_sizes, int n_in, void* d_out, int out_size, void* d_ws, size_t ws_size, hipStream_t stream) {
  (void)hipFuncSetAttribute((const void*)mha_main, hipFuncAttributeMaxDynamicSharedMemorySize, 163840);
  mha_main<<<256, 256, 163840, stream>>>((const char*)d_in[0], (const char*)d_in[1], (const char*)d_in[2], (const char*)d_in[3], (const char*)d_in[4], (char*)d_ws, (char*)d_out);
}
```
